```python
import torch

def _softmax(x, axis=-1):
    return torch.softmax(x, dim=axis)

def _gelu(x):
    return torch.nn.functional.gelu(x, approximate='tanh')

def _silu(x):
    return torch.nn.functional.silu(x)

def _sigmoid(x):
    return torch.sigmoid(x)

def _layernorm(x, axis=-1, eps=1e-05):
    m = x.mean(dim=axis, keepdim=True)
    v = x.var(dim=axis, keepdim=True, unbiased=False)
    return (x - m) / torch.sqrt(v + eps)

def _scatter_add(tgt, idx, src, axis=0):
    return tgt.clone().index_add_(axis, idx, src)

def _segment_sum(src, seg, n):
    out = torch.zeros((n,) + src.shape[1:], dtype=src.dtype)
    return out.index_add_(0, seg, src)

def _pad(x, pw):
    flat = tuple((v for pair in reversed(pw) for v in pair))
    return torch.nn.functional.pad(x, flat)
kernel_name = 'adaLN_cond__mha_block__moe_arch_mod__prompt'

def setup_inputs():

    _w = lambda a: (lambda t: t.to(torch.bfloat16) if t.is_floating_point() else t)(torch.as_tensor(a))

    def _A_setup():
        B, S, D, H = (1, 2048, 1024, 32)
        return {'x': _w((lambda *s: torch.randn(*s, dtype=torch.bfloat16) * 0.05)(B, S, D)), 'wqkv': _w((lambda *s: torch.randn(*s, dtype=torch.bfloat16) * 0.05)(D, 3 * D)), 'wo': _w((lambda *s: torch.randn(*s, dtype=torch.bfloat16) * 0.05)(D, D))}
    _A_in = _A_setup()

    def _B_setup():
        B, S, D, F, C = (1, 2048, 1024, 3584, 1)
        return {'x': _w((lambda *s: torch.randn(*s, dtype=torch.bfloat16) * 0.05)(B, S, D)), 'w_r': _w((lambda *s: torch.randn(*s, dtype=torch.bfloat16) * 0.05)(D)), 'w_gate': _w((lambda *s: torch.randn(*s, dtype=torch.bfloat16) * 0.05)(D, F)), 'w_up': _w((lambda *s: torch.randn(*s, dtype=torch.bfloat16) * 0.05)(D, F)), 'w_down': _w((lambda *s: torch.randn(*s, dtype=torch.bfloat16) * 0.05)(F, D))}
    _B_in = _B_setup()
    return {'A_x': _A_in['x'], 'A_wqkv': _A_in['wqkv'], 'A_wo': _A_in['wo'], 'B_x': _B_in['x'], 'B_w_r': _B_in['w_r'], 'B_w_gate': _B_in['w_gate'], 'B_w_up': _B_in['w_up'], 'B_w_down': _B_in['w_down'], 'c': _w((lambda *s: torch.randn(*s, dtype=torch.bfloat16) * 0.1)(1, 1024) * 8.0), 'w_mod': _w((lambda *s: torch.randn(*s, dtype=torch.bfloat16) * 0.1)(1024, 6 * 1024) * 1024 ** (-0.5) * 8.0)}

def reference(A_x, A_wqkv, A_wo, B_x, B_w_r, B_w_gate, B_w_up, B_w_down, c, w_mod):

    def _A_ref(x, wqkv, wo):
        B, S, D = x.shape
        H = 32
        Dh = D // H
        qkv = (lambda x, *d: x.permute(*d))((x @ wqkv).reshape(B, S, 3, H, Dh), 2, 0, 3, 1, 4)
        q, k, v = (qkv[0], qkv[1], qkv[2])
        scores = torch.einsum('bhqd,bhkd->bhqk', q, k) * Dh ** (-0.5)
        p = _softmax(scores, axis=-1)
        o = torch.einsum('bhqk,bhkd->bhqd', p, v)
        return (lambda x, *d: x.permute(*d))(o, 0, 2, 1, 3).reshape(B, S, D) @ wo

    def _B_ref(x, w_r, w_gate, w_up, w_down):
        B, S, D = x.shape
        C = 1
        r = x @ w_r
        idx = torch.argsort(-r, axis=-1)[:, :C]
        idx_e = (lambda x, n, axis: torch.repeat_interleave(x, n, dim=axis))(idx.reshape(B, C, 1), D, axis=2)
        x_sel = (lambda x, i, axis: torch.take_along_dim(x, i, dim=axis))(x, idx_e, axis=1)
        g = _silu(x_sel @ w_gate)
        u = x_sel @ w_up
        y = g * u @ w_down
        r_sel = _sigmoid((lambda x, i, axis: torch.take_along_dim(x, i, dim=axis))(r, idx, axis=1))
        y = y * r_sel.reshape(B, C, 1)
        b_off = (torch.arange(B) * S).reshape(B, 1)
        flat_idx = (idx + b_off).reshape(B * C)
        zero = (x * 0.0).reshape(B * S, D)
        upd = _scatter_add(zero, flat_idx, y.reshape(B * C, D), axis=0)
        return x + upd.reshape(B, S, D)
    x = A_x
    mod = (_silu(c) @ w_mod).reshape(c.shape[0], 6, 1, -1)
    sa, ca, ga, sb, cb, gb = (mod[:, 0].clone(), mod[:, 1].clone(), mod[:, 2].clone(), mod[:, 3].clone(), mod[:, 4].clone(), mod[:, 5].clone())
    h = _layernorm(x, axis=-1) * (1 + ca) + sa
    x = x + ga * _A_ref(h, A_wqkv, A_wo)
    h = _layernorm(x, axis=-1) * (1 + cb) + sb
    x = x + gb * _B_ref(h, B_w_r, B_w_gate, B_w_up, B_w_down)
    return x
```

```cpp
#include <hip/hip_runtime.h>
#include <hip/hip_bf16.h>
#include <stdint.h>

typedef unsigned short u16;
using bf16x8 = __attribute__((ext_vector_type(8))) short;
using s16x4 = __attribute__((ext_vector_type(4))) short;
using f32x16 = __attribute__((ext_vector_type(16))) float;
using f32x4 = __attribute__((ext_vector_type(4))) float;
using f32x2 = __attribute__((ext_vector_type(2))) float;
using u32x4 = __attribute__((ext_vector_type(4))) unsigned;
using u32x2 = __attribute__((ext_vector_type(2))) unsigned;
typedef __bf16 bf16x2_t __attribute__((ext_vector_type(2)));
#define LAS __attribute__((address_space(3)))
#define DI __device__ __forceinline__

constexpr int S = 2048, D = 1024, NH = 32, FF = 3584;
constexpr float QSCALE = 0.17677669529663687f * 1.4426950408889634f;

constexpr size_t WS_MODP = 0;
constexpr size_t WS_LN1 = 65536;
constexpr size_t WS_R = 98304;
constexpr size_t WS_ACT = 114688;
constexpr size_t WS_Q = 1u << 20;
constexpr size_t WS_K = WS_Q + (4u << 20);
constexpr size_t WS_V = WS_K + (4u << 20);
constexpr size_t WS_O = WS_V + (4u << 20);
constexpr size_t WS_X1 = WS_O + (4u << 20);

DI float bf2f(u16 v) { return __uint_as_float((unsigned)v << 16); }
DI float bflo(unsigned w) { return __uint_as_float(w << 16); }
DI float bfhi(unsigned w) { return __uint_as_float(w & 0xffff0000u); }
DI unsigned pack2(float lo, float hi) {
  f32x2 v = {lo, hi};
  bf16x2_t b = __builtin_convertvector(v, bf16x2_t);
  return __builtin_bit_cast(unsigned, b);
}
DI float rbf(float x) { return bflo(pack2(x, 0.f)); }
#define MFMA32(a, b, c) __builtin_amdgcn_mfma_f32_32x32x16_bf16((a), (b), (c), 0, 0, 0)
#define MFMA16(a, b, c) __builtin_amdgcn_mfma_f32_16x16x32_bf16((a), (b), (c), 0, 0, 0)
#define SBAR() __builtin_amdgcn_sched_barrier(0)
#define LDS_BARRIER() asm volatile("s_waitcnt lgkmcnt(0)\n\ts_barrier" ::: "memory")

DI float wave_sum(float v) {
#pragma unroll
  for (int o = 32; o > 0; o >>= 1) v += __shfl_xor(v, o);
  return v;
}
DI void glds16(const void* sbase, unsigned voff, unsigned lds_dst) {
  unsigned keep;
  asm volatile("s_mov_b32 %0, m0\n\ts_mov_b32 m0, %3\n\ts_nop 0\n\tglobal_load_lds_dwordx4 %1, %2\n\ts_mov_b32 m0, %0"
               : "=&s"(keep)
               : "v"(voff), "s"(sbase), "s"(lds_dst)
               : "memory");
}
DI bf16x8 lds_b128(unsigned addr) { return *(const LAS bf16x8*)(uintptr_t)addr; }
typedef short v4i16_t __attribute__((ext_vector_type(4)));
DI s16x4 lds_tr(unsigned addr) {
  return __builtin_bit_cast(s16x4, __builtin_amdgcn_ds_read_tr16_b64_v4i16((LAS v4i16_t*)(uintptr_t)addr));
}
DI bf16x8 cat8(s16x4 lo, s16x4 hi) { return __builtin_shufflevector(lo, hi, 0, 1, 2, 3, 4, 5, 6, 7); }

__global__ __launch_bounds__(512) void k1_mod_ln(const u16* __restrict__ c, const u16* __restrict__ wmod,
                                                 const u16* __restrict__ x, float* __restrict__ modp,
                                                 float* __restrict__ ln1) {
  __shared__ float red[8][64];
  const int t = threadIdx.x, lane = t & 63, wid = t >> 6, bid = blockIdx.x;
  if (bid < 192) {
    const int cg = bid % 96, kh = bid / 96;
    const int c8 = t & 7, rg = t >> 3;
    u32x4 w[8];
    float sc[8];
#pragma unroll
    for (int i = 0; i < 8; ++i) {
      const int k = kh * 512 + rg + 64 * i;
      w[i] = *(const u32x4*)(wmod + (size_t)k * 6144 + cg * 64 + c8 * 8);
      const float cv = bf2f(c[k]);
      sc[i] = rbf(cv / (1.f + __expf(-cv)));
    }
    float acc[8] = {0.f, 0.f, 0.f, 0.f, 0.f, 0.f, 0.f, 0.f};
#pragma unroll
    for (int i = 0; i < 8; ++i)
#pragma unroll
      for (int j = 0; j < 4; ++j) {
        acc[2 * j] = fmaf(sc[i], bflo(w[i][j]), acc[2 * j]);
        acc[2 * j + 1] = fmaf(sc[i], bfhi(w[i][j]), acc[2 * j + 1]);
      }
#pragma unroll
    for (int j = 0; j < 8; ++j) {
      acc[j] += __shfl_xor(acc[j], 8);
      acc[j] += __shfl_xor(acc[j], 16);
      acc[j] += __shfl_xor(acc[j], 32);
    }
    if (lane < 8) {
#pragma unroll
      for (int j = 0; j < 8; ++j) red[wid][lane * 8 + j] = acc[j];
    }
    __syncthreads();
    if (t < 64) {
      float s = 0.f;
#pragma unroll
      for (int w8 = 0; w8 < 8; ++w8) s += red[w8][t];
      modp[kh * 6144 + cg * 64 + t] = s;
    }
  } else {
    const int r0 = (bid - 192) * 32 + wid * 4;
    u32x4 a[4], b[4];
#pragma unroll
    for (int i = 0; i < 4; ++i) {
      a[i] = *(const u32x4*)(x + (size_t)(r0 + i) * D + lane * 8);
      b[i] = *(const u32x4*)(x + (size_t)(r0 + i) * D + 512 + lane * 8);
    }
#pragma unroll
    for (int i = 0; i < 4; ++i) {
      float v[16];
#pragma unroll
      for (int j = 0; j < 4; ++j) {
        v[2 * j] = bflo(a[i][j]); v[2 * j + 1] = bfhi(a[i][j]);
        v[8 + 2 * j] = bflo(b[i][j]); v[8 + 2 * j + 1] = bfhi(b[i][j]);
      }
      float s = 0.f;
#pragma unroll
      for (int j = 0; j < 16; ++j) s += v[j];
      const float mean = wave_sum(s) * (1.f / D);
      float q = 0.f;
#pragma unroll
      for (int j = 0; j < 16; ++j) { const float d = v[j] - mean; q = fmaf(d, d, q); }
      const float var = wave_sum(q) * (1.f / D);
      const float rstd = 1.f / sqrtf(var + 1e-5f);
      if (lane == 0) *(f32x2*)(ln1 + (size_t)(r0 + i) * 2) = (f32x2){rstd, -mean * rstd};
    }
  }
}

constexpr int K2_NB = 4, K2_BSLOT = 24576, K2_ASLOT = 16384;
constexpr int K2_LDS_A = K2_NB * K2_BSLOT, K2_LDS_GS = K2_LDS_A + 2 * K2_ASLOT, K2_LDS_LN = K2_LDS_GS + 8192;
constexpr int K2_LDS = K2_LDS_LN + 1024;

#define GLOAD16(dst, voff, sbase) \
  asm volatile("global_load_dwordx4 %0, %1, %2" : "=v"(dst) : "v"(voff), "s"(sbase) : "memory")

template <int KT>
DI void k2_issue_b(const u16* wq, unsigned wvoff, unsigned lds0, int pw) {
  if constexpr (KT <= 15) {
#pragma unroll
    for (int sg = 0; sg < 3; ++sg)
#pragma unroll
      for (int pc = 0; pc < 2; ++pc) {
        const u16* sb = wq + (size_t)(KT * 64 + pc * 8) * 3072 + sg * 1024;
        glds16(sb, wvoff, lds0 + (KT % K2_NB) * K2_BSLOT + sg * 8192 + pw * 2048 + pc * 1024);
      }
  }
}
template <int KT>
DI void k2_issue_x(const u16* xb, unsigned xvoff, u32x4 (&xr)[4]) {
  if constexpr (KT <= 15) {
#pragma unroll
    for (int i = 0; i < 4; ++i) {
      const u16* sb = xb + (size_t)(32 * i) * D + KT * 64;
      GLOAD16(xr[i], xvoff, sb);
    }
  }
}
template <int N>
DI void wait_vm_x(u32x4 (&xr)[4]) {
  asm volatile("s_waitcnt vmcnt(%4)" : "+v"(xr[0]), "+v"(xr[1]), "+v"(xr[2]), "+v"(xr[3]) : "i"(N) : "memory");
}

template <int KT>
DI void k2_prod_step(const u16* wq, const u16* xb, unsigned wvoff, unsigned xvoff, unsigned lds0, int pw, int c8,
                     unsigned awr, const float (&la)[4], const float (&lb)[4], u32x4 (&xr)[4]) {
  k2_issue_b<KT + 3>(wq, wvoff, lds0, pw);
  constexpr int NOUT = ((KT + 2 <= 15) ? 10 : 0) + ((KT + 3 <= 15) ? 6 : 0);
  const unsigned gsa = lds0 + K2_LDS_GS + ((KT + 1) * 64 + c8 * 8) * 4;
  const f32x4 g0 = *(const LAS f32x4*)(uintptr_t)(gsa), g1 = *(const LAS f32x4*)(uintptr_t)(gsa + 16);
  const f32x4 s0 = *(const LAS f32x4*)(uintptr_t)(gsa + 4096), s1 = *(const LAS f32x4*)(uintptr_t)(gsa + 4096 + 16);
  wait_vm_x<NOUT>(xr);
#pragma unroll
  for (int i = 0; i < 4; ++i) {
    u32x4 o;
#pragma unroll
    for (int j = 0; j < 4; ++j) {
      const float gl = j < 2 ? g0[2 * j] : g1[2 * j - 4], gh = j < 2 ? g0[2 * j + 1] : g1[2 * j - 3];
      const float sl = j < 2 ? s0[2 * j] : s1[2 * j - 4], sh = j < 2 ? s0[2 * j + 1] : s1[2 * j - 3];
      const float lo = fmaf(fmaf(bflo(xr[i][j]), la[i], lb[i]), gl, sl);
      const float hi = fmaf(fmaf(bfhi(xr[i][j]), la[i], lb[i]), gh, sh);
      o[j] = pack2(lo, hi);
    }
    *(LAS u32x4*)(uintptr_t)(awr + ((KT + 1) & 1) * K2_ASLOT + i * 4096) = o;
  }
  asm volatile("" ::: "memory");
  k2_issue_x<KT + 3>(xb, xvoff, xr);
  LDS_BARRIER();
}

__global__ __launch_bounds__(512) void k2_qkv(const u16* __restrict__ x, const u16* __restrict__ wqkv,
                                              const float* __restrict__ modp, const float* __restrict__ ln1,
                                              u16* __restrict__ qf, u16* __restrict__ kf, u16* __restrict__ vf) {
  extern __shared__ __attribute__((aligned(16))) char smem[];
  const unsigned lds0 = (unsigned)(uintptr_t)smem;
  const int tid = threadIdx.x, lane = tid & 63;
  const int wid = __builtin_amdgcn_readfirstlane(tid >> 6);
  const int bid = blockIdx.x, xcd = bid & 7, jj = bid >> 3;
  const int mt = (xcd & 1) * 8 + (jj & 7), nt = (xcd >> 1) * 4 + (jj >> 3);
  const int m0 = mt * 128;
  if (wid >= 4) {
    const int pw = wid - 4, pt = tid - 256;
    const int kr = lane >> 3, pos = lane & 7;
    const unsigned wvoff = (unsigned)(((pw * 16 + kr) * 3072 + (((pos >> 2) ^ ((kr >> 1) & 1)) * 32) + (pos & 3) * 8) * 2);
    const u16* wq = wqkv + nt * 64;
    const int c8 = pt & 7, row0 = pt >> 3;
    const unsigned xvoff = (unsigned)((row0 * D + c8 * 8) * 2);
    const u16* xb = x + (size_t)m0 * D;
    const unsigned awr = lds0 + K2_LDS_A + row0 * 128 + ((c8 ^ ((row0 >> 1) & 7)) << 4);
    u32x4 xa[4], xbr[4];
    k2_issue_b<0>(wq, wvoff, lds0, pw);
    k2_issue_x<0>(xb, xvoff, xa);
    k2_issue_b<1>(wq, wvoff, lds0, pw);
    k2_issue_x<1>(xb, xvoff, xbr);
    LDS_BARRIER();
    float la[4], lb[4];
#pragma unroll
    for (int i = 0; i < 4; ++i) {
      const f32x2 v = *(const LAS f32x2*)(uintptr_t)(lds0 + K2_LDS_LN + (row0 + 32 * i) * 8);
      la[i] = v[0]; lb[i] = v[1];
    }
#define PSTEP(KT, XR) k2_prod_step<KT>(wq, xb, wvoff, xvoff, lds0, pw, c8, awr, la, lb, XR)
    PSTEP(-1, xa); PSTEP(0, xbr); PSTEP(1, xa); PSTEP(2, xbr); PSTEP(3, xa); PSTEP(4, xbr); PSTEP(5, xa); PSTEP(6, xbr);
    PSTEP(7, xa); PSTEP(8, xbr); PSTEP(9, xa); PSTEP(10, xbr); PSTEP(11, xa); PSTEP(12, xbr); PSTEP(13, xa); PSTEP(14, xbr);
#undef PSTEP
    return;
  }
  {
    float* gs = (float*)(smem + K2_LDS_GS);
    const f32x4 sa0 = *(const f32x4*)(modp + tid * 4), sa1 = *(const f32x4*)(modp + 6144 + tid * 4);
    const f32x4 ca0 = *(const f32x4*)(modp + 1024 + tid * 4), ca1 = *(const f32x4*)(modp + 6144 + 1024 + tid * 4);
    f32x4 gv, sv;
#pragma unroll
    for (int j = 0; j < 4; ++j) {
      sv[j] = rbf(sa0[j] + sa1[j]);
      gv[j] = rbf(1.f + rbf(ca0[j] + ca1[j]));
    }
    *(f32x4*)(gs + tid * 4) = gv;
    *(f32x4*)(gs + 1024 + tid * 4) = sv;
    ((float*)(smem + K2_LDS_LN))[tid] = ln1[m0 * 2 + tid];
  }
  LDS_BARRIER();
  const int wm = wid & 1, wn = wid >> 1;
  const int r32 = lane & 31, h = lane >> 5;
  const int arow = wm * 64 + r32;
  const unsigned abase = lds0 + K2_LDS_A + arow * 128;
  unsigned aoff[4];
#pragma unroll
  for (int k = 0; k < 4; ++k) aoff[k] = abase + (((2 * k + h) ^ ((arow >> 1) & 7)) << 4);
  const int q4 = (lane & 15) >> 2, p4 = lane & 3, ch = (lane >> 4) & 1;
  const unsigned bbase = lds0 + (8 * h + q4) * 128 + ((wn ^ ((q4 >> 1) & 1)) * 64) + ch * 32 + p4 * 8;
  f32x16 acc[2][3];
#pragma unroll
  for (int a = 0; a < 2; ++a)
#pragma unroll
    for (int b = 0; b < 3; ++b) acc[a][b] = (f32x16){};
  bf16x8 fa[2][2], fb[2][3];
#define LOADF(SET, ASL, BSL, K16)                                                                       \
  do {                                                                                                  \
    fa[SET][0] = lds_b128(aoff[K16] + (ASL));                                                           \
    fa[SET][1] = lds_b128(aoff[K16] + (ASL) + 4096);                                                    \
    _Pragma("unroll") for (int sg = 0; sg < 3; ++sg) fb[SET][sg] =                                      \
        cat8(lds_tr(bbase + (BSL) + sg * 8192 + (K16) * 2048), lds_tr(bbase + (BSL) + sg * 8192 + (K16) * 2048 + 512)); \
  } while (0)
#define MMA(SET)                                               \
  do {                                                         \
    acc[0][0] = MFMA32(fb[SET][0], fa[SET][0], acc[0][0]);     \
    acc[1][0] = MFMA32(fb[SET][0], fa[SET][1], acc[1][0]);     \
    acc[0][1] = MFMA32(fb[SET][1], fa[SET][0], acc[0][1]);     \
    acc[1][1] = MFMA32(fb[SET][1], fa[SET][1], acc[1][1]);     \
    acc[0][2] = MFMA32(fa[SET][0], fb[SET][2], acc[0][2]);     \
    acc[1][2] = MFMA32(fa[SET][1], fb[SET][2], acc[1][2]);     \
  } while (0)
  LDS_BARRIER();
  LOADF(0, 0, 0, 0);
#pragma unroll 1
  for (int kt = 0; kt < 16; ++kt) {
    const unsigned asl = (kt & 1) * K2_ASLOT, bsl = (kt & 3) * K2_BSLOT;
    const unsigned asn = ((kt + 1) & 1) * K2_ASLOT, bsn = ((kt + 1) & 3) * K2_BSLOT;
    LOADF(1, asl, bsl, 1); SBAR(); MMA(0); SBAR();
    LOADF(0, asl, bsl, 2); SBAR(); MMA(1); SBAR();
    LOADF(1, asl, bsl, 3); SBAR(); MMA(0); SBAR();
    if (kt < 15) {
      LDS_BARRIER();
      LOADF(0, asn, bsn, 0);
    }
    SBAR(); MMA(1); SBAR();
  }
#undef LOADF
#undef MMA
  const int head = nt * 2 + wn;
#pragma unroll
  for (int mb = 0; mb < 2; ++mb) {
    const int tt = mt * 4 + wm * 2 + mb;
    const size_t off = ((size_t)(head * 64 + tt) * 2) * 512 + lane * 8;
#pragma unroll
    for (int sg = 0; sg < 3; ++sg) {
      u16* dst = (sg == 0 ? qf : sg == 1 ? kf : vf) + off;
      const float sc = sg == 0 ? QSCALE : 1.f;
#pragma unroll
      for (int s = 0; s < 2; ++s) {
        u32x4 o;
#pragma unroll
        for (int j = 0; j < 4; ++j) o[j] = pack2(acc[mb][sg][8 * s + 2 * j] * sc, acc[mb][sg][8 * s + 2 * j + 1] * sc);
        *(u32x4*)(dst + s * 512) = o;
      }
    }
  }
}

constexpr int K3_SLOT = 16384, K3_NS = 4, K3_LDS = K3_SLOT * K3_NS;

DI float ex2(float x) { return __builtin_amdgcn_exp2f(x); }
DI float xhalf_max(float v) {
  auto rr = __builtin_amdgcn_permlane32_swap(__float_as_uint(v), __float_as_uint(v), false, false);
  return fmaxf(__uint_as_float(rr[0]), __uint_as_float(rr[1]));
}

__global__ __launch_bounds__(512) void k3_attn(const u16* __restrict__ qf, const u16* __restrict__ kf,
                                               const u16* __restrict__ vf, u16* __restrict__ of) {
  extern __shared__ __attribute__((aligned(16))) char smem[];
  const unsigned lds0 = (unsigned)(uintptr_t)smem;
  const int tid = threadIdx.x, lane = tid & 63;
  const int wid = __builtin_amdgcn_readfirstlane(tid >> 6);
  const int bid = blockIdx.x, xcd = bid & 7, jj = bid >> 3;
  const int head = xcd * 4 + (jj >> 3), qb = jj & 7;
  const int qt = qb * 8 + wid;
  const u16* kh = kf + (size_t)head * 65536;
  const u16* vh = vf + (size_t)head * 65536;
  const unsigned svoff = (unsigned)(wid * 1024 + lane * 16);
#define STAGE(st)                                                                         \
  do {                                                                                    \
    glds16(kh + (size_t)(st) * 4096, svoff, lds0 + ((st) & 3) * K3_SLOT + wid * 1024);     \
    glds16(vh + (size_t)(st) * 4096, svoff, lds0 + ((st) & 3) * K3_SLOT + 8192 + wid * 1024); \
  } while (0)
  bf16x8 q0, q1;
  {
    const u16* qp = qf + ((size_t)(head * 64 + qt) * 2) * 512 + lane * 8;
    u32x4 t0, t1;
    asm volatile("global_load_dwordx4 %0, %1, off" : "=v"(t0) : "v"(qp) : "memory");
    asm volatile("global_load_dwordx4 %0, %1, off offset:1024" : "=v"(t1) : "v"(qp) : "memory");
    STAGE(0); STAGE(1); STAGE(2);
    asm volatile("s_waitcnt vmcnt(4)\n\ts_barrier" : "+v"(t0), "+v"(t1)::"memory");
    q0 = __builtin_bit_cast(bf16x8, t0); q1 = __builtin_bit_cast(bf16x8, t1);
  }
  const unsigned rbase = lds0 + lane * 16;
  bf16x8 ones;
  {
    const short o = ((((lane & 15) >> 2) & 1) == ((lane >> 4) & 1)) ? (short)0x3F80 : (short)0;
#pragma unroll
    for (int j = 0; j < 8; ++j) ones[j] = o;
  }
  bf16x8 k0 = lds_b128(rbase), k1 = lds_b128(rbase + 1024);
  f32x16 cneg;
  {
    f32x16 s = MFMA32(k0, q0, (f32x16){});
    s = MFMA32(k1, q1, s);
    float m = s[0];
#pragma unroll
    for (int r = 1; r < 16; ++r) m = fmaxf(m, s[r]);
    m = xhalf_max(m);
#pragma unroll
    for (int r = 0; r < 16; ++r) cneg[r] = -m;
  }
  f32x16 sA = MFMA32(k0, q0, cneg);
  sA = MFMA32(k1, q1, sA);
  f32x16 sB;
  k0 = lds_b128(rbase + 2048); k1 = lds_b128(rbase + 3072);
  bf16x8 v0 = lds_b128(rbase + 8192), v1 = lds_b128(rbase + 8192 + 1024);
  f32x16 o = (f32x16){};
  f32x4 l = (f32x4){};
  u32x4 p0, p1 = (u32x4){0u, 0u, 0u, 0u};
  bf16x8 v1p = v1;
#define PK(S_, i) pack2(S_[i], S_[i + 1])
#define EX(S_, i) S_[i] = ex2(S_[i])
#define PIN(x_) asm volatile("" : "+v"(x_))
#define TILE(SC, SN, KN, VN, GK, GV)                                                       \
  do {                                                                                     \
    SN = MFMA32(k0, q0, cneg); SBAR();                                                     \
    if (GK) k0 = lds_b128(KN);                                                             \
    EX(SC, 0); EX(SC, 1); EX(SC, 2); p0[0] = PK(SC, 0); PIN(p0); SBAR();                   \
    o = MFMA32(v1p, __builtin_bit_cast(bf16x8, p1), o); SBAR();                            \
    v1p = v1;                                                                              \
    EX(SC, 3); EX(SC, 4); EX(SC, 5); p0[1] = PK(SC, 2); PIN(p0); SBAR();                   \
    l = MFMA16(ones, __builtin_bit_cast(bf16x8, p1), l); SBAR();                           \
    EX(SC, 6); EX(SC, 7); p0[2] = PK(SC, 4); p0[3] = PK(SC, 6); PIN(p0); SBAR();           \
    SN = MFMA32(k1, q1, SN); SBAR();                                                       \
    if (GK) k1 = lds_b128((KN) + 1024);                                                    \
    EX(SC, 8); EX(SC, 9); EX(SC, 10); p1[0] = PK(SC, 8); PIN(p1); SBAR();                  \
    o = MFMA32(v0, __builtin_bit_cast(bf16x8, p0), o); SBAR();                             \
    if (GV) { v0 = lds_b128(VN); v1 = lds_b128((VN) + 1024); }                             \
    EX(SC, 11); EX(SC, 12); EX(SC, 13); p1[1] = PK(SC, 10); PIN(p1); SBAR();               \
    l = MFMA16(ones, __builtin_bit_cast(bf16x8, p0), l); SBAR();                           \
    EX(SC, 14); EX(SC, 15); p1[2] = PK(SC, 12); p1[3] = PK(SC, 14); PIN(p1); SBAR();       \
  } while (0)
#pragma unroll 1
  for (int st = 0; st < 16; ++st) {
    const unsigned cur = rbase + (st & 3) * K3_SLOT, nxt = rbase + ((st + 1) & 3) * K3_SLOT;
    TILE(sA, sB, cur + 4096, cur + 8192 + 2048, true, true);
    TILE(sB, sA, cur + 6144, cur + 8192 + 4096, true, true);
    if (st < 15) {
      if (st < 14) asm volatile("s_waitcnt vmcnt(2) lgkmcnt(0)\n\ts_barrier" ::: "memory");
      else asm volatile("s_waitcnt vmcnt(0) lgkmcnt(0)\n\ts_barrier" ::: "memory");
      if (st < 13) STAGE(st + 3);
      TILE(sA, sB, nxt, cur + 8192 + 6144, true, true);
      TILE(sB, sA, nxt + 2048, nxt + 8192, true, true);
    } else {
      TILE(sA, sB, nxt, cur + 8192 + 6144, false, true);
      TILE(sB, sA, nxt, nxt, false, false);
    }
  }
  o = MFMA32(v1p, __builtin_bit_cast(bf16x8, p1), o);
  l = MFMA16(ones, __builtin_bit_cast(bf16x8, p1), l);
#undef TILE
#undef STAGE
  float lsum = l[0];
  float rl = 1.f / lsum;
  bool bad = !(lsum < 1.0e18f);
#pragma unroll
  for (int r = 0; r < 16; ++r) {
    o[r] *= rl;
    bad |= !(fabsf(o[r]) < 3.0e38f);
  }
  if (__builtin_expect(__any(bad), 0)) {
    float m = -INFINITY, ls = 0.f;
    o = (f32x16){};
    for (int t = 0; t < 64; ++t) {
      const bf16x8 a0 = *(const bf16x8*)(kh + (size_t)t * 1024 + lane * 8), a1 = *(const bf16x8*)(kh + (size_t)t * 1024 + 512 + lane * 8);
      const bf16x8 b0 = *(const bf16x8*)(vh + (size_t)t * 1024 + lane * 8), b1 = *(const bf16x8*)(vh + (size_t)t * 1024 + 512 + lane * 8);
      f32x16 s = MFMA32(a0, q0, (f32x16){});
      s = MFMA32(a1, q1, s);
      float tm = s[0];
#pragma unroll
      for (int r = 1; r < 16; ++r) tm = fmaxf(tm, s[r]);
      tm = xhalf_max(tm);
      const float mn = fmaxf(m, tm);
      const float alpha = ex2(m - mn);
      m = mn;
      u32x4 pa, pb;
      float ps = 0.f;
#pragma unroll
      for (int j = 0; j < 4; ++j) {
        pa[j] = pack2(ex2(s[2 * j] - mn), ex2(s[2 * j + 1] - mn));
        pb[j] = pack2(ex2(s[8 + 2 * j] - mn), ex2(s[8 + 2 * j + 1] - mn));
        ps += bflo(pa[j]) + bfhi(pa[j]) + bflo(pb[j]) + bfhi(pb[j]);
      }
      ps += __shfl_xor(ps, 32);
      ls = ls * alpha + ps;
#pragma unroll
      for (int r = 0; r < 16; ++r) o[r] *= alpha;
      o = MFMA32(b0, __builtin_bit_cast(bf16x8, pa), o);
      o = MFMA32(b1, __builtin_bit_cast(bf16x8, pb), o);
    }
    rl = 1.f / ls;
#pragma unroll
    for (int r = 0; r < 16; ++r) o[r] *= rl;
  }
  u16* op = of + ((size_t)(qt * 32 + head) * 2) * 512 + lane * 8;
#pragma unroll
  for (int s = 0; s < 2; ++s) {
    u32x4 w;
#pragma unroll
    for (int j = 0; j < 4; ++j) w[j] = pack2(o[8 * s + 2 * j], o[8 * s + 2 * j + 1]);
    *(u32x4*)(op + s * 512) = w;
  }
#undef PK
#undef EX
}

constexpr int K4_LDS = 131072;
__global__ __launch_bounds__(512) void k4_oproj(const u16* __restrict__ of, const u16* __restrict__ wo,
                                                const u16* __restrict__ x, const float* __restrict__ modp,
                                                u16* __restrict__ x1) {
  extern __shared__ __attribute__((aligned(16))) char smem[];
  const unsigned lds0 = (unsigned)(uintptr_t)smem;
  const int tid = threadIdx.x, lane = tid & 63;
  const int wid = __builtin_amdgcn_readfirstlane(tid >> 6);
  const int bid = blockIdx.x, xcd = bid & 7, jj = bid >> 3;
  const int mt = (xcd >> 1) * 4 + (jj & 3), nt = (xcd & 1) * 8 + (jj >> 2);
  const int kh = wid >> 2, wt = wid & 3;
  const int kr = lane >> 3, pos = lane & 7;
  const unsigned wvoff = (unsigned)(((wt * 16 + kr) * D + (((pos >> 2) ^ ((kr >> 1) & 1)) * 32) + (pos & 3) * 8) * 2);
  const u16* wb = wo + (size_t)(kh * 512) * D + nt * 64;
  const u16* ob = of + ((size_t)((mt * 4 + wt) * 32 + kh * 16) * 2) * 512;
  const unsigned ovoff = lane * 16;
  u32x4 ofr[32];
#pragma unroll
  for (int st = 0; st < 8; ++st) {
    glds16(wb + (size_t)(st * 64) * D, wvoff, lds0 + kh * 65536 + st * 8192 + wt * 2048);
    glds16(wb + (size_t)(st * 64 + 8) * D, wvoff, lds0 + kh * 65536 + st * 8192 + wt * 2048 + 1024);
#pragma unroll
    for (int i = 0; i < 4; ++i) {
      const u16* sb = ob + (size_t)(st * 4 + i) * 512;
      GLOAD16(ofr[st * 4 + i], ovoff, sb);
    }
  }
  const int erow = tid >> 3, ec = tid & 7;
  u32x4 xr[2];
  {
    const unsigned xv = (unsigned)((erow * D + ec * 8) * 2);
    const u16* xb0 = x + (size_t)(mt * 128) * D + nt * 64;
    const u16* xb1 = xb0 + (size_t)64 * D;
    GLOAD16(xr[0], xv, xb0);
    GLOAD16(xr[1], xv, xb1);
  }
  const int h = lane >> 5, q4 = (lane & 15) >> 2, p4 = lane & 3, ch = (lane >> 4) & 1;
  const unsigned bb0 = lds0 + kh * 65536 + (4 * h + q4) * 128 + ((0 ^ ((q4 >> 1) & 1)) * 64) + ch * 32 + p4 * 8;
  const unsigned bb1 = lds0 + kh * 65536 + (4 * h + q4) * 128 + ((1 ^ ((q4 >> 1) & 1)) * 64) + ch * 32 + p4 * 8;
  f32x16 acc0 = (f32x16){}, acc1 = (f32x16){};
#pragma unroll
  for (int st = 0; st < 8; ++st) {
    asm volatile("s_waitcnt vmcnt(%4)\n\ts_barrier"
                 : "+v"(ofr[st * 4]), "+v"(ofr[st * 4 + 1]), "+v"(ofr[st * 4 + 2]), "+v"(ofr[st * 4 + 3])
                 : "i"((7 - st) * 6 + 2)
                 : "memory");
#pragma unroll
    for (int i = 0; i < 4; ++i) {
      const unsigned o = st * 8192 + i * 2048;
      const bf16x8 w0 = cat8(lds_tr(bb0 + o), lds_tr(bb0 + o + 1024));
      const bf16x8 w1 = cat8(lds_tr(bb1 + o), lds_tr(bb1 + o + 1024));
      const bf16x8 a = __builtin_bit_cast(bf16x8, ofr[st * 4 + i]);
      acc0 = MFMA32(w0, a, acc0);
      acc1 = MFMA32(w1, a, acc1);
    }
  }
  LDS_BARRIER();
  {
    const unsigned wa = lds0 + kh * 34816 + (wt * 32 + (lane & 31)) * 272 + h * 16;
#pragma unroll
    for (int g = 0; g < 4; ++g) {
      *(LAS f32x4*)(uintptr_t)(wa + g * 32) = (f32x4){acc0[4 * g], acc0[4 * g + 1], acc0[4 * g + 2], acc0[4 * g + 3]};
      *(LAS f32x4*)(uintptr_t)(wa + 128 + g * 32) = (f32x4){acc1[4 * g], acc1[4 * g + 1], acc1[4 * g + 2], acc1[4 * g + 3]};
    }
  }
  asm volatile("s_waitcnt vmcnt(0) lgkmcnt(0)\n\ts_barrier" : "+v"(xr[0]), "+v"(xr[1])::"memory");
  float ga[8];
  {
    const float* g0 = modp + 2048 + nt * 64 + ec * 8;
#pragma unroll
    for (int j = 0; j < 8; ++j) ga[j] = rbf(g0[j] + g0[6144 + j]);
  }
#pragma unroll
  for (int i = 0; i < 2; ++i) {
    const int row = erow + 64 * i;
    const unsigned ra = lds0 + row * 272 + ec * 32;
    const f32x4 a0 = *(const LAS f32x4*)(uintptr_t)(ra), a1 = *(const LAS f32x4*)(uintptr_t)(ra + 16);
    const f32x4 b0 = *(const LAS f32x4*)(uintptr_t)(ra + 34816), b1 = *(const LAS f32x4*)(uintptr_t)(ra + 34816 + 16);
    u32x4 o;
#pragma unroll
    for (int j = 0; j < 4; ++j) {
      const float lo = (j < 2 ? a0[2 * j] + b0[2 * j] : a1[2 * j - 4] + b1[2 * j - 4]);
      const float hi = (j < 2 ? a0[2 * j + 1] + b0[2 * j + 1] : a1[2 * j - 3] + b1[2 * j - 3]);
      o[j] = pack2(fmaf(ga[2 * j], lo, bflo(xr[i][j])), fmaf(ga[2 * j + 1], hi, bfhi(xr[i][j])));
    }
    *(u32x4*)(x1 + (size_t)(mt * 128 + row) * D + nt * 64 + ec * 8) = o;
  }
}

DI void ln2_row(const u16* __restrict__ x1row, const float* __restrict__ modp, int lane, float (&xv)[16], float (&hv)[16]) {
  const u32x4 a = *(const u32x4*)(x1row + lane * 8), b = *(const u32x4*)(x1row + 512 + lane * 8);
#pragma unroll
  for (int j = 0; j < 4; ++j) {
    xv[2 * j] = bflo(a[j]); xv[2 * j + 1] = bfhi(a[j]);
    xv[8 + 2 * j] = bflo(b[j]); xv[8 + 2 * j + 1] = bfhi(b[j]);
  }
  float s = 0.f;
#pragma unroll
  for (int j = 0; j < 16; ++j) s += xv[j];
  const float mean = wave_sum(s) * (1.f / D);
  float q = 0.f;
#pragma unroll
  for (int j = 0; j < 16; ++j) { const float d = xv[j] - mean; q = fmaf(d, d, q); }
  const float rstd = 1.f / sqrtf(wave_sum(q) * (1.f / D) + 1e-5f);
#pragma unroll
  for (int j = 0; j < 16; ++j) {
    const int col = (j < 8 ? 0 : 512 - 8) + lane * 8 + j;
    const float sb = rbf(modp[3072 + col] + modp[6144 + 3072 + col]);
    const float g = rbf(1.f + rbf(modp[4096 + col] + modp[6144 + 4096 + col]));
    hv[j] = rbf(fmaf((xv[j] - mean) * rstd, g, sb));
  }
}

__global__ __launch_bounds__(512) void k5_ln2(const u16* __restrict__ x1, const float* __restrict__ modp,
                                              const u16* __restrict__ wr, float* __restrict__ rbuf, u16* __restrict__ out) {
  const int lane = threadIdx.x & 63, row = blockIdx.x * 8 + (threadIdx.x >> 6);
  float xv[16], hv[16];
  ln2_row(x1 + (size_t)row * D, modp, lane, xv, hv);
  float r = 0.f;
  u32x4 o[2];
#pragma unroll
  for (int j = 0; j < 16; j += 2) {
    const int col = (j < 8 ? 0 : 512 - 8) + lane * 8 + j;
    r = fmaf(hv[j], bf2f(wr[col]), r);
    r = fmaf(hv[j + 1], bf2f(wr[col + 1]), r);
    const float gb0 = rbf(modp[5120 + col] + modp[6144 + 5120 + col]);
    const float gb1 = rbf(modp[5120 + col + 1] + modp[6144 + 5120 + col + 1]);
    o[j >> 3][(j & 7) >> 1] = pack2(fmaf(gb0, hv[j], xv[j]), fmaf(gb1, hv[j + 1], xv[j + 1]));
  }
  *(u32x4*)(out + (size_t)row * D + lane * 8) = o[0];
  *(u32x4*)(out + (size_t)row * D + 512 + lane * 8) = o[1];
  r = wave_sum(r);
  if (lane == 0) rbuf[row] = r;
}

DI int block_argmax(const float* __restrict__ rbuf, float* sval, int* sidx, float& rbest) {
  const int t = threadIdx.x, lane = t & 63, wid = t >> 6;
  const f32x4 v = *(const f32x4*)(rbuf + t * 4);
  float bv = v[0];
  int bi = t * 4;
#pragma unroll
  for (int j = 1; j < 4; ++j)
    if (v[j] > bv) { bv = v[j]; bi = t * 4 + j; }
#pragma unroll
  for (int o = 32; o > 0; o >>= 1) {
    const float ov = __shfl_xor(bv, o);
    const int oi = __shfl_xor(bi, o);
    if (ov > bv || (ov == bv && oi < bi)) { bv = ov; bi = oi; }
  }
  if (lane == 0) { sval[wid] = bv; sidx[wid] = bi; }
  __syncthreads();
  bv = sval[0]; bi = sidx[0];
#pragma unroll
  for (int w = 1; w < 8; ++w)
    if (sval[w] > bv) { bv = sval[w]; bi = sidx[w]; }
  rbest = bv;
  return bi;
}

__global__ __launch_bounds__(512) void k6_gateup(const u16* __restrict__ x1, const float* __restrict__ modp,
                                                 const float* __restrict__ rbuf, const u16* __restrict__ wg,
                                                 const u16* __restrict__ wu, float* __restrict__ act) {
  __shared__ float sval[8];
  __shared__ int sidx[8];
  __shared__ float hs[1024];
  __shared__ float red[8][32];
  const int t = threadIdx.x, lane = t & 63, wid = t >> 6;
  const int c0 = blockIdx.x * 16, half = t & 1, rg = t >> 1;
  u32x4 g[4], u[4];
#pragma unroll
  for (int i = 0; i < 4; ++i) {
    g[i] = *(const u32x4*)(wg + (size_t)(rg + 256 * i) * FF + c0 + half * 8);
    u[i] = *(const u32x4*)(wu + (size_t)(rg + 256 * i) * FF + c0 + half * 8);
  }
  float rbest;
  const int idx = block_argmax(rbuf, sval, sidx, rbest);
  if (wid == 0) {
    float xv[16], hv[16];
    ln2_row(x1 + (size_t)idx * D, modp, lane, xv, hv);
#pragma unroll
    for (int j = 0; j < 16; ++j) hs[(j < 8 ? 0 : 512 - 8) + lane * 8 + j] = hv[j];
  }
  __syncthreads();
  float ag[8] = {0.f, 0.f, 0.f, 0.f, 0.f, 0.f, 0.f, 0.f}, au[8] = {0.f, 0.f, 0.f, 0.f, 0.f, 0.f, 0.f, 0.f};
#pragma unroll
  for (int i = 0; i < 4; ++i) {
    const float hv = hs[rg + 256 * i];
#pragma unroll
    for (int j = 0; j < 4; ++j) {
      ag[2 * j] = fmaf(hv, bflo(g[i][j]), ag[2 * j]); ag[2 * j + 1] = fmaf(hv, bfhi(g[i][j]), ag[2 * j + 1]);
      au[2 * j] = fmaf(hv, bflo(u[i][j]), au[2 * j]); au[2 * j + 1] = fmaf(hv, bfhi(u[i][j]), au[2 * j + 1]);
    }
  }
#pragma unroll
  for (int j = 0; j < 8; ++j) {
#pragma unroll
    for (int o = 2; o < 64; o <<= 1) { ag[j] += __shfl_xor(ag[j], o); au[j] += __shfl_xor(au[j], o); }
  }
  if (lane < 2) {
#pragma unroll
    for (int j = 0; j < 8; ++j) { red[wid][lane * 8 + j] = ag[j]; red[wid][16 + lane * 8 + j] = au[j]; }
  }
  __syncthreads();
  if (t < 16) {
    float sg = 0.f, su = 0.f;
#pragma unroll
    for (int w = 0; w < 8; ++w) { sg += red[w][t]; su += red[w][16 + t]; }
    sg = rbf(sg); su = rbf(su);
    const float si = rbf(sg / (1.f + __expf(-sg)));
    act[c0 + t] = rbf(si * su);
  }
}

__global__ __launch_bounds__(512) void k7_down(const u16* __restrict__ x1, const float* __restrict__ modp,
                                               const float* __restrict__ rbuf, const float* __restrict__ act,
                                               const u16* __restrict__ wd, u16* __restrict__ out) {
  __shared__ float sval[8];
  __shared__ int sidx[8];
  __shared__ float hs[1024];
  __shared__ float xs[1024];
  __shared__ float red[8][8];
  const int t = threadIdx.x, lane = t & 63, wid = t >> 6;
  const int c0 = blockIdx.x * 8;
  u32x4 w[7];
  float a[7];
#pragma unroll
  for (int i = 0; i < 7; ++i) {
    w[i] = *(const u32x4*)(wd + (size_t)(t + 512 * i) * D + c0);
    a[i] = act[t + 512 * i];
  }
  float rbest;
  const int idx = block_argmax(rbuf, sval, sidx, rbest);
  if (wid == 0) {
    float xv[16], hv[16];
    ln2_row(x1 + (size_t)idx * D, modp, lane, xv, hv);
#pragma unroll
    for (int j = 0; j < 16; ++j) {
      hs[(j < 8 ? 0 : 512 - 8) + lane * 8 + j] = hv[j];
      xs[(j < 8 ? 0 : 512 - 8) + lane * 8 + j] = xv[j];
    }
  }
  float acc[8] = {0.f, 0.f, 0.f, 0.f, 0.f, 0.f, 0.f, 0.f};
#pragma unroll
  for (int i = 0; i < 7; ++i)
#pragma unroll
    for (int j = 0; j < 4; ++j) {
      acc[2 * j] = fmaf(a[i], bflo(w[i][j]), acc[2 * j]);
      acc[2 * j + 1] = fmaf(a[i], bfhi(w[i][j]), acc[2 * j + 1]);
    }
#pragma unroll
  for (int j = 0; j < 8; ++j) acc[j] = wave_sum(acc[j]);
  if (lane == 0) {
#pragma unroll
    for (int j = 0; j < 8; ++j) red[wid][j] = acc[j];
  }
  __syncthreads();
  if (t < 8) {
    float y = 0.f;
#pragma unroll
    for (int w8 = 0; w8 < 8; ++w8) y += red[w8][t];
    const float rs = rbf(rbest);
    const float sig = rbf(1.f / (1.f + __expf(-rs)));
    y = rbf(rbf(y) * sig);
    const int col = c0 + t;
    const float gb = rbf(modp[5120 + col] + modp[6144 + 5120 + col]);
    const float v = fmaf(gb, rbf(hs[col] + y), xs[col]);
    out[(size_t)idx * D + col] = (u16)(pack2(v, 0.f) & 0xffffu);
  }
}

extern "C" void kernel_launch(void* const* d_in, const int* in_sizes, int n_in, void* d_out, int out_size, void* d_ws,
                              size_t ws_size, hipStream_t stream) {
  const u16* x = (const u16*)d_in[0];
  const u16* wqkv = (const u16*)d_in[1];
  const u16* wo = (const u16*)d_in[2];
  const u16* wr = (const u16*)d_in[4];
  const u16* wg = (const u16*)d_in[5];
  const u16* wu = (const u16*)d_in[6];
  const u16* wd = (const u16*)d_in[7];
  const u16* c = (const u16*)d_in[8];
  const u16* wmod = (const u16*)d_in[9];
  char* ws = (char*)d_ws;
  float* modp = (float*)(ws + WS_MODP);
  float* ln1 = (float*)(ws + WS_LN1);
  float* rbuf = (float*)(ws + WS_R);
  float* act = (float*)(ws + WS_ACT);
  u16* qf = (u16*)(ws + WS_Q);
  u16* kf = (u16*)(ws + WS_K);
  u16* vf = (u16*)(ws + WS_V);
  u16* of = (u16*)(ws + WS_O);
  u16* x1 = (u16*)(ws + WS_X1);
  u16* out = (u16*)d_out;
  (void)hipFuncSetAttribute((const void*)k2_qkv, hipFuncAttributeMaxDynamicSharedMemorySize, K2_LDS);
  (void)hipFuncSetAttribute((const void*)k3_attn, hipFuncAttributeMaxDynamicSharedMemorySize, K3_LDS);
  (void)hipFuncSetAttribute((const void*)k4_oproj, hipFuncAttributeMaxDynamicSharedMemorySize, K4_LDS);
  k1_mod_ln<<<256, 512, 0, stream>>>(c, wmod, x, modp, ln1);
  k2_qkv<<<256, 512, K2_LDS, stream>>>(x, wqkv, modp, ln1, qf, kf, vf);
  k3_attn<<<256, 512, K3_LDS, stream>>>(qf, kf, vf, of);
  k4_oproj<<<256, 512, K4_LDS, stream>>>(of, wo, x, modp, x1);
  k5_ln2<<<256, 512, 0, stream>>>(x1, modp, wr, rbuf, out);
  k6_gateup<<<224, 512, 0, stream>>>(x1, modp, rbuf, wg, wu, act);
  k7_down<<<128, 512, 0, stream>>>(x1, modp, rbuf, act, wd, out);
}
```

```cpp
#include <hip/hip_runtime.h>
#include <hip/hip_bf16.h>
#include <stdint.h>

typedef unsigned short u16;
using bf16x8 = __attribute__((ext_vector_type(8))) short;
using s16x4 = __attribute__((ext_vector_type(4))) short;
using f32x16 = __attribute__((ext_vector_type(16))) float;
using f32x4 = __attribute__((ext_vector_type(4))) float;
using f32x2 = __attribute__((ext_vector_type(2))) float;
using u32x4 = __attribute__((ext_vector_type(4))) unsigned;
using u32x2 = __attribute__((ext_vector_type(2))) unsigned;
typedef __bf16 bf16x2_t __attribute__((ext_vector_type(2)));
#define LAS __attribute__((address_space(3)))
#define DI __device__ __forceinline__

constexpr int S = 2048, D = 1024, NH = 32, FF = 3584;
constexpr float QSCALE = 0.17677669529663687f * 1.4426950408889634f;

constexpr size_t WS_MODP = 0;
constexpr size_t WS_LN1 = 65536;
constexpr size_t WS_MODF = 98304;
constexpr size_t WS_SYNC = 131072;
constexpr int SYNC_VEC16 = (4096 + 4 * 3584 + 16 * 512) / 2;
constexpr size_t WS_Q = 1u << 20;
constexpr size_t WS_K = WS_Q + (4u << 20);
constexpr size_t WS_V = WS_K + (4u << 20);
constexpr size_t WS_O = WS_V + (4u << 20);
constexpr size_t WS_X1 = WS_O + (4u << 20);

#define PROF_BEGIN()
#define PROF_END(kid)
#define TS_DECL()
#define TS(i)
#define TS_END(kid, w)

DI float bf2f(u16 v) { return __uint_as_float((unsigned)v << 16); }
DI float bflo(unsigned w) { return __uint_as_float(w << 16); }
DI float bfhi(unsigned w) { return __uint_as_float(w & 0xffff0000u); }
DI unsigned pack2(float lo, float hi) {
  f32x2 v = {lo, hi};
  bf16x2_t b = __builtin_convertvector(v, bf16x2_t);
  return __builtin_bit_cast(unsigned, b);
}
DI float rbf(float x) { return bflo(pack2(x, 0.f)); }
#define MFMA32(a, b, c) __builtin_amdgcn_mfma_f32_32x32x16_bf16((a), (b), (c), 0, 0, 0)
#define MFMA16(a, b, c) __builtin_amdgcn_mfma_f32_16x16x32_bf16((a), (b), (c), 0, 0, 0)
#define SBAR() __builtin_amdgcn_sched_barrier(0)
#define LDS_BARRIER() asm volatile("s_waitcnt lgkmcnt(0)\n\ts_barrier" ::: "memory")

DI float wave_sum(float v) {
#pragma unroll
  for (int o = 32; o > 0; o >>= 1) v += __shfl_xor(v, o);
  return v;
}
DI void glds16(const void* sbase, unsigned voff, unsigned lds_dst) {
  unsigned keep;
  asm volatile("s_mov_b32 %0, m0\n\ts_mov_b32 m0, %3\n\ts_nop 0\n\tglobal_load_lds_dwordx4 %1, %2\n\ts_mov_b32 m0, %0"
               : "=&s"(keep)
               : "v"(voff), "s"(sbase), "s"(lds_dst)
               : "memory");
}
DI bf16x8 lds_b128(unsigned addr) { return *(const LAS bf16x8*)(uintptr_t)addr; }
typedef short v4i16_t __attribute__((ext_vector_type(4)));
DI s16x4 lds_tr(unsigned addr) {
  return __builtin_bit_cast(s16x4, __builtin_amdgcn_ds_read_tr16_b64_v4i16((LAS v4i16_t*)(uintptr_t)addr));
}
DI bf16x8 cat8(s16x4 lo, s16x4 hi) { return __builtin_shufflevector(lo, hi, 0, 1, 2, 3, 4, 5, 6, 7); }

__global__ __launch_bounds__(512) void k1_mod_ln(const u16* __restrict__ c, const u16* __restrict__ wmod,
                                                 const u16* __restrict__ x, float* __restrict__ modp,
                                                 float* __restrict__ ln1, u32x4* __restrict__ sync) {
  PROF_BEGIN();
  __shared__ float red[8][64];
  const int t = threadIdx.x, lane = t & 63, wid = t >> 6, bid = blockIdx.x;
  static_assert(SYNC_VEC16 == 256 * 52, "sync clear");
  if (t < 52) sync[bid * 52 + t] = (u32x4){0u, 0u, 0u, 0u};
  if (bid < 192) {
    const int cg = bid % 96, kh = bid / 96;
    const int c8 = t & 7, rg = t >> 3;
    u32x4 w[8];
    float sc[8];
#pragma unroll
    for (int i = 0; i < 8; ++i) {
      const int k = kh * 512 + rg + 64 * i;
      w[i] = __builtin_nontemporal_load((const u32x4*)(wmod + (size_t)k * 6144 + cg * 64 + c8 * 8));
      const float cv = bf2f(c[k]);
      sc[i] = rbf(cv / (1.f + __expf(-cv)));
    }
    float acc[8] = {0.f, 0.f, 0.f, 0.f, 0.f, 0.f, 0.f, 0.f};
#pragma unroll
    for (int i = 0; i < 8; ++i)
#pragma unroll
      for (int j = 0; j < 4; ++j) {
        acc[2 * j] = fmaf(sc[i], bflo(w[i][j]), acc[2 * j]);
        acc[2 * j + 1] = fmaf(sc[i], bfhi(w[i][j]), acc[2 * j + 1]);
      }
#pragma unroll
    for (int j = 0; j < 8; ++j) {
      acc[j] += __shfl_xor(acc[j], 8);
      acc[j] += __shfl_xor(acc[j], 16);
      acc[j] += __shfl_xor(acc[j], 32);
    }
    if (lane < 8) {
#pragma unroll
      for (int j = 0; j < 8; ++j) red[wid][lane * 8 + j] = acc[j];
    }
    __syncthreads();
    if (t < 64) {
      float s = 0.f;
#pragma unroll
      for (int w8 = 0; w8 < 8; ++w8) s += red[w8][t];
      modp[kh * 6144 + cg * 64 + t] = s;
    }
  } else {
    const int r0 = (bid - 192) * 32 + wid * 4;
    u32x4 a[4], b[4];
#pragma unroll
    for (int i = 0; i < 4; ++i) {
      a[i] = *(const u32x4*)(x + (size_t)(r0 + i) * D + lane * 8);
      b[i] = *(const u32x4*)(x + (size_t)(r0 + i) * D + 512 + lane * 8);
    }
#pragma unroll
    for (int i = 0; i < 4; ++i) {
      float v[16];
#pragma unroll
      for (int j = 0; j < 4; ++j) {
        v[2 * j] = bflo(a[i][j]); v[2 * j + 1] = bfhi(a[i][j]);
        v[8 + 2 * j] = bflo(b[i][j]); v[8 + 2 * j + 1] = bfhi(b[i][j]);
      }
      float s = 0.f;
#pragma unroll
      for (int j = 0; j < 16; ++j) s += v[j];
      const float mean = wave_sum(s) * (1.f / D);
      float q = 0.f;
#pragma unroll
      for (int j = 0; j < 16; ++j) { const float d = v[j] - mean; q = fmaf(d, d, q); }
      const float var = wave_sum(q) * (1.f / D);
      const float rstd = 1.f / sqrtf(var + 1e-5f);
      if (lane == 0) *(f32x2*)(ln1 + (size_t)(r0 + i) * 2) = (f32x2){rstd, -mean * rstd};
    }
  }
  PROF_END(1);
}

constexpr int K2_NB = 4, K2_BSLOT = 24576, K2_ASLOT = 16384;
constexpr int K2_LDS_A = K2_NB * K2_BSLOT, K2_LDS_GS = K2_LDS_A + 2 * K2_ASLOT, K2_LDS_LN = K2_LDS_GS + 8192;
constexpr int K2_LDS = K2_LDS_LN + 1024;

#define GLOAD16(dst, voff, sbase) \
  asm volatile("global_load_dwordx4 %0, %1, %2" : "=v"(dst) : "v"(voff), "s"(sbase) : "memory")

#define GLOAD16NT(dst, voff, sbase) \
  asm volatile("global_load_dwordx4 %0, %1, %2 nt" : "=v"(dst) : "v"(voff), "s"(sbase) : "memory")

template <int KT>
DI void k2_issue_b(const u16* wq, unsigned wvoff, unsigned lds0, int pw) {
  if constexpr (KT <= 15) {
#pragma unroll
    for (int sg = 0; sg < 3; ++sg)
#pragma unroll
      for (int pc = 0; pc < 2; ++pc) {
        const u16* sb = wq + (size_t)(KT * 64 + pc * 8) * 3072 + sg * 1024;
        glds16(sb, wvoff, lds0 + (KT % K2_NB) * K2_BSLOT + sg * 8192 + pw * 2048 + pc * 1024);
      }
  }
}
template <int KT>
DI void k2_issue_x(const u16* xb, unsigned xvoff, u32x4 (&xr)[4]) {
  if constexpr (KT <= 15) {
#pragma unroll
    for (int i = 0; i < 4; ++i) {
      const u16* sb = xb + (size_t)(32 * i) * D + KT * 64;
      GLOAD16(xr[i], xvoff, sb);
    }
  }
}
template <int N>
DI void wait_vm_x(u32x4 (&xr)[4]) {
  asm volatile("s_waitcnt vmcnt(%4)" : "+v"(xr[0]), "+v"(xr[1]), "+v"(xr[2]), "+v"(xr[3]) : "i"(N) : "memory");
}

template <int KT>
DI void k2_prod_step(const u16* wq, const u16* xb, unsigned wvoff, unsigned xvoff, unsigned lds0, int pw, int c8,
                     unsigned awr, const float (&la)[4], const float (&lb)[4], u32x4 (&xr)[4]) {
  k2_issue_b<KT + 3>(wq, wvoff, lds0, pw);
  constexpr int NOUT = ((KT + 2 <= 15) ? 10 : 0) + ((KT + 3 <= 15) ? 6 : 0);
  const unsigned gsa = lds0 + K2_LDS_GS + ((KT + 1) * 64 + c8 * 8) * 4;
  const f32x4 g0 = *(const LAS f32x4*)(uintptr_t)(gsa), g1 = *(const LAS f32x4*)(uintptr_t)(gsa + 16);
  const f32x4 s0 = *(const LAS f32x4*)(uintptr_t)(gsa + 4096), s1 = *(const LAS f32x4*)(uintptr_t)(gsa + 4096 + 16);
  wait_vm_x<NOUT>(xr);
#pragma unroll
  for (int i = 0; i < 4; ++i) {
    u32x4 o;
#pragma unroll
    for (int j = 0; j < 4; ++j) {
      const float gl = j < 2 ? g0[2 * j] : g1[2 * j - 4], gh = j < 2 ? g0[2 * j + 1] : g1[2 * j - 3];
      const float sl = j < 2 ? s0[2 * j] : s1[2 * j - 4], sh = j < 2 ? s0[2 * j + 1] : s1[2 * j - 3];
      const float lo = fmaf(fmaf(bflo(xr[i][j]), la[i], lb[i]), gl, sl);
      const float hi = fmaf(fmaf(bfhi(xr[i][j]), la[i], lb[i]), gh, sh);
      o[j] = pack2(lo, hi);
    }
    *(LAS u32x4*)(uintptr_t)(awr + ((KT + 1) & 1) * K2_ASLOT + i * 4096) = o;
  }
  asm volatile("" ::: "memory");
  k2_issue_x<KT + 3>(xb, xvoff, xr);
  LDS_BARRIER();
}

__global__ __launch_bounds__(512) void k2_qkv(const u16* __restrict__ x, const u16* __restrict__ wqkv,
                                              const float* __restrict__ modp, const float* __restrict__ ln1,
                                              u16* __restrict__ qf, u16* __restrict__ kf, u16* __restrict__ vf,
                                              u16* __restrict__ modf) {
  PROF_BEGIN();
  extern __shared__ __attribute__((aligned(16))) char smem[];
  const unsigned lds0 = (unsigned)(uintptr_t)smem;
  const int tid = threadIdx.x, lane = tid & 63;
  const int wid = __builtin_amdgcn_readfirstlane(tid >> 6);
  const int bid = blockIdx.x, xcd = bid & 7, jj = bid >> 3;
  const int mt = (xcd & 1) * 8 + (jj & 7), nt = (xcd >> 1) * 4 + (jj >> 3);
  const int m0 = mt * 128;
  if (wid >= 4) {
    const int pw = wid - 4, pt = tid - 256;
    const int kr = lane >> 3, pos = lane & 7;
    const unsigned wvoff = (unsigned)(((pw * 16 + kr) * 3072 + (((pos >> 2) ^ ((kr >> 1) & 1)) * 32) + (pos & 3) * 8) * 2);
    const u16* wq = wqkv + nt * 64;
    const int c8 = pt & 7, row0 = pt >> 3;
    const unsigned xvoff = (unsigned)((row0 * D + c8 * 8) * 2);
    const u16* xb = x + (size_t)m0 * D;
    const unsigned awr = lds0 + K2_LDS_A + row0 * 128 + ((c8 ^ ((row0 >> 1) & 7)) << 4);
    u32x4 xa[4], xbr[4];
    k2_issue_b<0>(wq, wvoff, lds0, pw);
    k2_issue_x<0>(xb, xvoff, xa);
    k2_issue_b<1>(wq, wvoff, lds0, pw);
    k2_issue_x<1>(xb, xvoff, xbr);
    LDS_BARRIER();
    float la[4], lb[4];
#pragma unroll
    for (int i = 0; i < 4; ++i) {
      const f32x2 v = *(const LAS f32x2*)(uintptr_t)(lds0 + K2_LDS_LN + (row0 + 32 * i) * 8);
      la[i] = v[0]; lb[i] = v[1];
    }
#define PSTEP(KT, XR) k2_prod_step<KT>(wq, xb, wvoff, xvoff, lds0, pw, c8, awr, la, lb, XR)
    PSTEP(-1, xa); PSTEP(0, xbr); PSTEP(1, xa); PSTEP(2, xbr); PSTEP(3, xa); PSTEP(4, xbr); PSTEP(5, xa); PSTEP(6, xbr);
    PSTEP(7, xa); PSTEP(8, xbr); PSTEP(9, xa); PSTEP(10, xbr); PSTEP(11, xa); PSTEP(12, xbr); PSTEP(13, xa); PSTEP(14, xbr);
#undef PSTEP
    PROF_END(2);
    return;
  }
  {
    float* gs = (float*)(smem + K2_LDS_GS);
    const f32x4 sa0 = *(const f32x4*)(modp + tid * 4), sa1 = *(const f32x4*)(modp + 6144 + tid * 4);
    const f32x4 ca0 = *(const f32x4*)(modp + 1024 + tid * 4), ca1 = *(const f32x4*)(modp + 6144 + 1024 + tid * 4);
    f32x4 gv, sv;
#pragma unroll
    for (int j = 0; j < 4; ++j) {
      sv[j] = rbf(sa0[j] + sa1[j]);
      gv[j] = rbf(1.f + rbf(ca0[j] + ca1[j]));
    }
    *(f32x4*)(gs + tid * 4) = gv;
    *(f32x4*)(gs + 1024 + tid * 4) = sv;
    ((float*)(smem + K2_LDS_LN))[tid] = ln1[m0 * 2 + tid];
    if (bid >= 2 && bid < 6) {
      const f32x4 p0 = *(const f32x4*)(modp + bid * 1024 + tid * 4), p1 = *(const f32x4*)(modp + 6144 + bid * 1024 + tid * 4);
      float v[4];
#pragma unroll
      for (int j = 0; j < 4; ++j) { v[j] = rbf(p0[j] + p1[j]); if (bid == 4) v[j] += 1.f; }
      *(u32x2*)(modf + bid * 1024 + tid * 4) = (u32x2){pack2(v[0], v[1]), pack2(v[2], v[3])};
    }
  }
  LDS_BARRIER();
  const int wm = wid & 1, wn = wid >> 1;
  const int r32 = lane & 31, h = lane >> 5;
  const int arow = wm * 64 + r32;
  const unsigned abase = lds0 + K2_LDS_A + arow * 128;
  unsigned aoff[4];
#pragma unroll
  for (int k = 0; k < 4; ++k) aoff[k] = abase + (((2 * k + h) ^ ((arow >> 1) & 7)) << 4);
  const int q4 = (lane & 15) >> 2, p4 = lane & 3, ch = (lane >> 4) & 1;
  const unsigned bbase = lds0 + (8 * h + q4) * 128 + ((wn ^ ((q4 >> 1) & 1)) * 64) + ch * 32 + p4 * 8;
  f32x16 acc[2][3];
#pragma unroll
  for (int a = 0; a < 2; ++a)
#pragma unroll
    for (int b = 0; b < 3; ++b) acc[a][b] = (f32x16){};
  bf16x8 fa[2][2], fb[2][3];
#define LOADF(SET, ASL, BSL, K16)                                                                       \
  do {                                                                                                  \
    fa[SET][0] = lds_b128(aoff[K16] + (ASL));                                                           \
    fa[SET][1] = lds_b128(aoff[K16] + (ASL) + 4096);                                                    \
    _Pragma("unroll") for (int sg = 0; sg < 3; ++sg) fb[SET][sg] =                                      \
        cat8(lds_tr(bbase + (BSL) + sg * 8192 + (K16) * 2048), lds_tr(bbase + (BSL) + sg * 8192 + (K16) * 2048 + 512)); \
  } while (0)
#define MMA(SET)                                               \
  do {                                                         \
    acc[0][0] = MFMA32(fb[SET][0], fa[SET][0], acc[0][0]);     \
    acc[1][0] = MFMA32(fb[SET][0], fa[SET][1], acc[1][0]);     \
    acc[0][1] = MFMA32(fb[SET][1], fa[SET][0], acc[0][1]);     \
    acc[1][1] = MFMA32(fb[SET][1], fa[SET][1], acc[1][1]);     \
    acc[0][2] = MFMA32(fa[SET][0], fb[SET][2], acc[0][2]);     \
    acc[1][2] = MFMA32(fa[SET][1], fb[SET][2], acc[1][2]);     \
  } while (0)
  LDS_BARRIER();
  LOADF(0, 0, 0, 0);
#pragma unroll 1
  for (int kt = 0; kt < 16; ++kt) {
    const unsigned asl = (kt & 1) * K2_ASLOT, bsl = (kt & 3) * K2_BSLOT;
    const unsigned asn = ((kt + 1) & 1) * K2_ASLOT, bsn = ((kt + 1) & 3) * K2_BSLOT;
    LOADF(1, asl, bsl, 1); SBAR(); MMA(0); SBAR();
    LOADF(0, asl, bsl, 2); SBAR(); MMA(1); SBAR();
    LOADF(1, asl, bsl, 3); SBAR(); MMA(0); SBAR();
    if (kt < 15) {
      LDS_BARRIER();
      LOADF(0, asn, bsn, 0);
    }
    SBAR(); MMA(1); SBAR();
  }
#undef LOADF
#undef MMA
  const int head = nt * 2 + wn;
#pragma unroll
  for (int mb = 0; mb < 2; ++mb) {
    const int tt = mt * 4 + wm * 2 + mb;
    const size_t off = ((size_t)(head * 64 + tt) * 2) * 512 + lane * 8;
#pragma unroll
    for (int sg = 0; sg < 3; ++sg) {
      u16* dst = (sg == 0 ? qf : sg == 1 ? kf : vf) + off;
      const float sc = sg == 0 ? QSCALE : 1.f;
#pragma unroll
      for (int s = 0; s < 2; ++s) {
        u32x4 o;
#pragma unroll
        for (int j = 0; j < 4; ++j) o[j] = pack2(acc[mb][sg][8 * s + 2 * j] * sc, acc[mb][sg][8 * s + 2 * j + 1] * sc);
        *(u32x4*)(dst + s * 512) = o;
      }
    }
  }
  PROF_END(2);
}

constexpr int K3_PART = 4096 + 256, K3_LDS = 8 * 4 * K3_PART;

DI float ex2(float x) { return __builtin_amdgcn_exp2f(x); }
DI float xhalf_max(float v) {
  auto rr = __builtin_amdgcn_permlane32_swap(__float_as_uint(v), __float_as_uint(v), false, false);
  return fmaxf(__uint_as_float(rr[0]), __uint_as_float(rr[1]));
}

__global__ __launch_bounds__(512) void k3_attn(const u16* __restrict__ qf, const u16* __restrict__ kf,
                                               const u16* __restrict__ vf, u16* __restrict__ of) {
  PROF_BEGIN();
  extern __shared__ __attribute__((aligned(16))) char smem[];
  const unsigned lds0 = (unsigned)(uintptr_t)smem;
  const int tid = threadIdx.x, lane = tid & 63;
  const int wid = __builtin_amdgcn_readfirstlane(tid >> 6);
  const int bid = blockIdx.x, xcd = bid & 7, jj = bid >> 3;
  const int head = xcd * 4 + (jj >> 3), qb = jj & 7;
  const int qg = wid >> 2, kq = wid & 3;
  const int qt0 = qb * 8 + qg * 4;
  const u16* kh = kf + (size_t)head * 65536;
  const u16* vh = vf + (size_t)head * 65536;
  const u16* kp = kh + kq * 16384 + lane * 8;
  const u16* vp = vh + kq * 16384 + lane * 8;
  TS_DECL(); TS(0);
#define LDF(ptr) (*(const bf16x8*)(ptr))
  bf16x8 ka0 = LDF(kp), ka1 = LDF(kp + 512);
  bf16x8 q[4][2];
#pragma unroll
  for (int i = 0; i < 4; ++i) {
    const u16* qp = qf + ((size_t)(head * 64 + qt0 + ((i + kq) & 3)) * 2) * 512 + lane * 8;
    q[i][0] = LDF(qp); q[i][1] = LDF(qp + 512);
  }
  bf16x8 va0 = LDF(vp), va1 = LDF(vp + 512);
  bf16x8 kb0 = LDF(kp + 1024), kb1 = LDF(kp + 1024 + 512);
  bf16x8 vb0, vb1 = va1;
  bf16x8 ones;
  {
    const short o = ((((lane & 15) >> 2) & 1) == ((lane >> 4) & 1)) ? (short)0x3F80 : (short)0;
#pragma unroll
    for (int j = 0; j < 8; ++j) ones[j] = o;
  }
  const f32x16 zero = (f32x16){};
  f32x16 o[4] = {zero, zero, zero, zero};
  f32x4 l[4] = {(f32x4){}, (f32x4){}, (f32x4){}, (f32x4){}};
  f32x16 sA = MFMA32(ka0, q[0][0], zero), sB;
  sA = MFMA32(ka1, q[0][1], sA);
  u32x4 p0, p1 = (u32x4){0u, 0u, 0u, 0u};
#define PK(S_, i) pack2(S_[i], S_[i + 1])
#define EX(S_, i) S_[i] = ex2(S_[i])
#define PIN(x_) asm volatile("" : "+v"(x_))
#define UNIT(SC, SN, KN0, KN1, N, C, V0C, P, V1P, LD_A, LD_B)                               \
  do {                                                                                     \
    SN = MFMA32(KN0, q[N][0], zero); SBAR();                                               \
    EX(SC, 0); EX(SC, 1); EX(SC, 2); p0[0] = PK(SC, 0); PIN(p0); SBAR();                   \
    o[P] = MFMA32(V1P, __builtin_bit_cast(bf16x8, p1), o[P]); SBAR();                      \
    LD_A;                                                                                  \
    EX(SC, 3); EX(SC, 4); EX(SC, 5); p0[1] = PK(SC, 2); PIN(p0); SBAR();                   \
    l[P] = MFMA16(ones, __builtin_bit_cast(bf16x8, p1), l[P]); SBAR();                     \
    EX(SC, 6); EX(SC, 7); p0[2] = PK(SC, 4); p0[3] = PK(SC, 6); PIN(p0); SBAR();           \
    SN = MFMA32(KN1, q[N][1], SN); SBAR();                                                 \
    LD_B;                                                                                  \
    EX(SC, 8); EX(SC, 9); EX(SC, 10); p1[0] = PK(SC, 8); PIN(p1); SBAR();                  \
    o[C] = MFMA32(V0C, __builtin_bit_cast(bf16x8, p0), o[C]); SBAR();                      \
    EX(SC, 11); EX(SC, 12); EX(SC, 13); p1[1] = PK(SC, 10); PIN(p1); SBAR();               \
    l[C] = MFMA16(ones, __builtin_bit_cast(bf16x8, p0), l[C]); SBAR();                     \
    EX(SC, 14); EX(SC, 15); p1[2] = PK(SC, 12); p1[3] = PK(SC, 14); PIN(p1); SBAR();       \
  } while (0)
  TS(1);
#pragma unroll 1
  for (int j = 0; j < 16; j += 2) {
    const u16* kn = kp + ((j + 2) & 15) * 1024;
    const u16* vn = vp + (j + 1) * 1024;
    const u16* vn2 = vp + ((j + 2) & 15) * 1024;
    UNIT(sA, sB, ka0, ka1, 1, 0, va0, 3, vb1, vb0 = LDF(vn), vb1 = LDF(vn + 512));
    UNIT(sB, sA, ka0, ka1, 2, 1, va0, 0, va1, (void)0, (void)0);
    UNIT(sA, sB, ka0, ka1, 3, 2, va0, 1, va1, (void)0, (void)0);
    UNIT(sB, sA, kb0, kb1, 0, 3, va0, 2, va1, ka0 = LDF(kn), ka1 = LDF(kn + 512));
    UNIT(sA, sB, kb0, kb1, 1, 0, vb0, 3, va1, va0 = LDF(vn2), va1 = LDF(vn2 + 512));
    UNIT(sB, sA, kb0, kb1, 2, 1, vb0, 0, vb1, (void)0, (void)0);
    UNIT(sA, sB, kb0, kb1, 3, 2, vb0, 1, vb1, (void)0, (void)0);
    UNIT(sB, sA, ka0, ka1, 0, 3, vb0, 2, vb1, kb0 = LDF(kn + 1024), kb1 = LDF(kn + 1024 + 512));
  }
  o[3] = MFMA32(vb1, __builtin_bit_cast(bf16x8, p1), o[3]);
  l[3] = MFMA16(ones, __builtin_bit_cast(bf16x8, p1), l[3]);
  TS(2);
#undef UNIT
#pragma unroll
  for (int i = 1; i < 4; ++i) {
    const unsigned wa = lds0 + ((qg * 4 + ((i + kq) & 3)) * 4 + kq) * K3_PART;
#pragma unroll
    for (int g = 0; g < 4; ++g)
      *(LAS f32x4*)(uintptr_t)(wa + g * 1024 + lane * 16) = (f32x4){o[i][4 * g], o[i][4 * g + 1], o[i][4 * g + 2], o[i][4 * g + 3]};
    *(LAS float*)(uintptr_t)(wa + 4096 + lane * 4) = l[i][0];
  }
  LDS_BARRIER();
  f32x16 oo = o[0];
  float lsum = l[0][0];
#pragma unroll
  for (int i = 1; i < 4; ++i) {
    const unsigned ra = lds0 + (wid * 4 + ((i + kq) & 3)) * K3_PART;
#pragma unroll
    for (int g = 0; g < 4; ++g) {
      const f32x4 v = *(const LAS f32x4*)(uintptr_t)(ra + g * 1024 + lane * 16);
      oo[4 * g] += v[0]; oo[4 * g + 1] += v[1]; oo[4 * g + 2] += v[2]; oo[4 * g + 3] += v[3];
    }
    lsum += *(const LAS float*)(uintptr_t)(ra + 4096 + lane * 4);
  }
  float rl = 1.f / lsum;
  bool bad = !(lsum < 1.0e30f && lsum > 1.0e-30f);
#pragma unroll
  for (int r = 0; r < 16; ++r) {
    oo[r] *= rl;
    bad |= !(fabsf(oo[r]) < 3.0e38f);
  }
  if (__builtin_expect(__any(bad), 0)) {
    const bf16x8 q0 = q[0][0], q1 = q[0][1];
    float m = -INFINITY, ls = 0.f;
    oo = zero;
    for (int t = 0; t < 64; ++t) {
      const bf16x8 a0 = LDF(kh + (size_t)t * 1024 + lane * 8), a1 = LDF(kh + (size_t)t * 1024 + 512 + lane * 8);
      const bf16x8 b0 = LDF(vh + (size_t)t * 1024 + lane * 8), b1 = LDF(vh + (size_t)t * 1024 + 512 + lane * 8);
      f32x16 s = MFMA32(a0, q0, zero);
      s = MFMA32(a1, q1, s);
      float tm = s[0];
#pragma unroll
      for (int r = 1; r < 16; ++r) tm = fmaxf(tm, s[r]);
      tm = xhalf_max(tm);
      const float mn = fmaxf(m, tm);
      const float alpha = ex2(m - mn);
      m = mn;
      u32x4 pa, pb;
      float ps = 0.f;
#pragma unroll
      for (int j = 0; j < 4; ++j) {
        pa[j] = pack2(ex2(s[2 * j] - mn), ex2(s[2 * j + 1] - mn));
        pb[j] = pack2(ex2(s[8 + 2 * j] - mn), ex2(s[8 + 2 * j + 1] - mn));
        ps += bflo(pa[j]) + bfhi(pa[j]) + bflo(pb[j]) + bfhi(pb[j]);
      }
      ps += __shfl_xor(ps, 32);
      ls = ls * alpha + ps;
#pragma unroll
      for (int r = 0; r < 16; ++r) oo[r] *= alpha;
      oo = MFMA32(b0, __builtin_bit_cast(bf16x8, pa), oo);
      oo = MFMA32(b1, __builtin_bit_cast(bf16x8, pb), oo);
    }
    rl = 1.f / ls;
#pragma unroll
    for (int r = 0; r < 16; ++r) oo[r] *= rl;
  }
  u16* op = of + ((size_t)((qt0 + kq) * 32 + head) * 2) * 512 + lane * 8;
#pragma unroll
  for (int s = 0; s < 2; ++s) {
    u32x4 w;
#pragma unroll
    for (int j = 0; j < 4; ++j) w[j] = pack2(oo[8 * s + 2 * j], oo[8 * s + 2 * j + 1]);
    *(u32x4*)(op + s * 512) = w;
  }
  TS(3);
  TS_END(3, 0);
  PROF_END(3);
#undef PK
#undef EX
#undef LDF
}

constexpr int K4_LDS = 131072;
__global__ __launch_bounds__(512) void k4_oproj(const u16* __restrict__ of, const u16* __restrict__ wo,
                                                const u16* __restrict__ x, const float* __restrict__ modp,
                                                u16* __restrict__ x1) {
  PROF_BEGIN();
  extern __shared__ __attribute__((aligned(16))) char smem[];
  const unsigned lds0 = (unsigned)(uintptr_t)smem;
  const int tid = threadIdx.x, lane = tid & 63;
  const int wid = __builtin_amdgcn_readfirstlane(tid >> 6);
  const int bid = blockIdx.x, xcd = bid & 7, jj = bid >> 3;
  const int mt = (xcd >> 1) * 4 + (jj & 3), nt = (xcd & 1) * 8 + (jj >> 2);
  const int kh = wid >> 2, wt = wid & 3;
  const int kr = lane >> 3, pos = lane & 7;
  const unsigned wvoff = (unsigned)(((wt * 16 + kr) * D + (((pos >> 2) ^ ((kr >> 1) & 1)) * 32) + (pos & 3) * 8) * 2);
  const u16* wb = wo + (size_t)(kh * 512) * D + nt * 64;
  const u16* ob = of + ((size_t)((mt * 4 + wt) * 32 + kh * 16) * 2) * 512;
  const unsigned ovoff = lane * 16;
  u32x4 ofr[32];
#pragma unroll
  for (int st = 0; st < 8; ++st) {
    glds16(wb + (size_t)(st * 64) * D, wvoff, lds0 + kh * 65536 + st * 8192 + wt * 2048);
    glds16(wb + (size_t)(st * 64 + 8) * D, wvoff, lds0 + kh * 65536 + st * 8192 + wt * 2048 + 1024);
#pragma unroll
    for (int i = 0; i < 4; ++i) {
      const u16* sb = ob + (size_t)(st * 4 + i) * 512;
      GLOAD16(ofr[st * 4 + i], ovoff, sb);
    }
  }
  const int erow = tid >> 3, ec = tid & 7;
  u32x4 xr[2];
  {
    const unsigned xv = (unsigned)((erow * D + ec * 8) * 2);
    const u16* xb0 = x + (size_t)(mt * 128) * D + nt * 64;
    const u16* xb1 = xb0 + (size_t)64 * D;
    GLOAD16(xr[0], xv, xb0);
    GLOAD16(xr[1], xv, xb1);
  }
  const int h = lane >> 5, q4 = (lane & 15) >> 2, p4 = lane & 3, ch = (lane >> 4) & 1;
  const unsigned bb0 = lds0 + kh * 65536 + (4 * h + q4) * 128 + ((0 ^ ((q4 >> 1) & 1)) * 64) + ch * 32 + p4 * 8;
  const unsigned bb1 = lds0 + kh * 65536 + (4 * h + q4) * 128 + ((1 ^ ((q4 >> 1) & 1)) * 64) + ch * 32 + p4 * 8;
  f32x16 acc0 = (f32x16){}, acc1 = (f32x16){};
#pragma unroll
  for (int st = 0; st < 8; ++st) {
    asm volatile("s_waitcnt vmcnt(%4)\n\ts_barrier"
                 : "+v"(ofr[st * 4]), "+v"(ofr[st * 4 + 1]), "+v"(ofr[st * 4 + 2]), "+v"(ofr[st * 4 + 3])
                 : "i"((7 - st) * 6 + 2)
                 : "memory");
#pragma unroll
    for (int i = 0; i < 4; ++i) {
      const unsigned o = st * 8192 + i * 2048;
      const bf16x8 w0 = cat8(lds_tr(bb0 + o), lds_tr(bb0 + o + 1024));
      const bf16x8 w1 = cat8(lds_tr(bb1 + o), lds_tr(bb1 + o + 1024));
      const bf16x8 a = __builtin_bit_cast(bf16x8, ofr[st * 4 + i]);
      acc0 = MFMA32(w0, a, acc0);
      acc1 = MFMA32(w1, a, acc1);
    }
  }
  LDS_BARRIER();
  {
    const unsigned wa = lds0 + kh * 34816 + (wt * 32 + (lane & 31)) * 272 + h * 16;
#pragma unroll
    for (int g = 0; g < 4; ++g) {
      *(LAS f32x4*)(uintptr_t)(wa + g * 32) = (f32x4){acc0[4 * g], acc0[4 * g + 1], acc0[4 * g + 2], acc0[4 * g + 3]};
      *(LAS f32x4*)(uintptr_t)(wa + 128 + g * 32) = (f32x4){acc1[4 * g], acc1[4 * g + 1], acc1[4 * g + 2], acc1[4 * g + 3]};
    }
  }
  asm volatile("s_waitcnt vmcnt(0) lgkmcnt(0)\n\ts_barrier" : "+v"(xr[0]), "+v"(xr[1])::"memory");
  float ga[8];
  {
    const float* g0 = modp + 2048 + nt * 64 + ec * 8;
#pragma unroll
    for (int j = 0; j < 8; ++j) ga[j] = rbf(g0[j] + g0[6144 + j]);
  }
#pragma unroll
  for (int i = 0; i < 2; ++i) {
    const int row = erow + 64 * i;
    const unsigned ra = lds0 + row * 272 + ec * 32;
    const f32x4 a0 = *(const LAS f32x4*)(uintptr_t)(ra), a1 = *(const LAS f32x4*)(uintptr_t)(ra + 16);
    const f32x4 b0 = *(const LAS f32x4*)(uintptr_t)(ra + 34816), b1 = *(const LAS f32x4*)(uintptr_t)(ra + 34816 + 16);
    u32x4 o;
#pragma unroll
    for (int j = 0; j < 4; ++j) {
      const float lo = (j < 2 ? a0[2 * j] + b0[2 * j] : a1[2 * j - 4] + b1[2 * j - 4]);
      const float hi = (j < 2 ? a0[2 * j + 1] + b0[2 * j + 1] : a1[2 * j - 3] + b1[2 * j - 3]);
      o[j] = pack2(fmaf(ga[2 * j], lo, bflo(xr[i][j])), fmaf(ga[2 * j + 1], hi, bfhi(xr[i][j])));
    }
    *(u32x4*)(x1 + (size_t)(mt * 128 + row) * D + nt * 64 + ec * 8) = o;
  }
  PROF_END(4);
}

#define GAS __attribute__((address_space(1)))
typedef GAS unsigned long long gu64;
#define RLX_AGENT __ATOMIC_RELAXED, __HIP_MEMORY_SCOPE_AGENT
constexpr unsigned SPIN_CAP = 1u << 17;
DI unsigned long long mk64(unsigned lo, unsigned hi) { return ((unsigned long long)hi << 32) | lo; }
template <int CTRL>
DI float dpp_add(float v) {
  return v + __uint_as_float(__builtin_amdgcn_update_dpp(0u, __float_as_uint(v), CTRL, 0xf, 0xf, true));
}
DI float swap32_add(float a, float b) {
  auto r = __builtin_amdgcn_permlane32_swap(__float_as_uint(a), __float_as_uint(b), false, false);
  return __uint_as_float(r[0]) + __uint_as_float(r[1]);
}
DI float swap16_add(float a, float b) {
  auto r = __builtin_amdgcn_permlane16_swap(__float_as_uint(a), __float_as_uint(b), false, false);
  return __uint_as_float(r[0]) + __uint_as_float(r[1]);
}
DI float wsum(float v) {
  v = dpp_add<0xB1>(v);
  v = dpp_add<0x4E>(v);
  v = dpp_add<0x141>(v);
  v = dpp_add<0x140>(v);
  v = swap16_add(v, v);
  return swap32_add(v, v);
}
template <int CTRL>
DI unsigned dpp_maxu(unsigned v) {
  const unsigned o = __builtin_amdgcn_update_dpp(0u, v, CTRL, 0xf, 0xf, true);
  return v > o ? v : o;
}
DI unsigned wmaxu(unsigned v) {
  v = dpp_maxu<0xB1>(v); v = dpp_maxu<0x4E>(v); v = dpp_maxu<0x141>(v); v = dpp_maxu<0x140>(v);
  auto r = __builtin_amdgcn_permlane16_swap(v, v, false, false);
  v = r[0] > r[1] ? r[0] : r[1];
  auto q = __builtin_amdgcn_permlane32_swap(v, v, false, false);
  return q[0] > q[1] ? q[0] : q[1];
}
DI f32x2 up2(unsigned w) { return (f32x2){bflo(w), bfhi(w)}; }
#define GLOADV(dst, ptr) asm volatile("global_load_dwordx4 %0, %1, off" : "=v"(dst) : "v"(ptr) : "memory")

__global__ __launch_bounds__(1024) void k5_tail(const u16* __restrict__ x1, const u16* __restrict__ modf,
                                                const u16* __restrict__ wr, const u16* __restrict__ wg,
                                                const u16* __restrict__ wu, const u16* __restrict__ wd,
                                                u16* __restrict__ out, unsigned long long* sync) {
  PROF_BEGIN();
  __shared__ __attribute__((aligned(16))) float s_row[8][4];
  __shared__ __attribute__((aligned(16))) float s_h[256];
  __shared__ __attribute__((aligned(16))) float s_a[256];
  __shared__ __attribute__((aligned(16))) float s_red[8][128];
  __shared__ __attribute__((aligned(16))) float s_red2[8][64];
  __shared__ int s_idx;
  __shared__ unsigned s_cnt;
  const int t = threadIdx.x, lane = t & 63;
  const int wid = __builtin_amdgcn_readfirstlane(t >> 6);
  const int b = blockIdx.x;
  gu64* g1 = (gu64*)sync;
  gu64* gp = (gu64*)sync + 4096;
  gu64* gy = (gu64*)sync + 4096 + 4 * FF;
  const bool has_gu = b < 224;
  const int cg = b % 56, ks = b / 56;
  const int dcg = b & 15, dks = b >> 4;
  TS_DECL(); TS(0);
  if (wid >= 8) {
    const int tt = t - 512, c8 = tt & 7, rg = tt >> 3;
    asm volatile("s_barrier" ::: "memory");
    f32x2 wf[8][4];
    u32x4 wdn[4];
    if (has_gu) {
      u32x4 wgu[8];
      const unsigned vo = (unsigned)((rg * FF + c8 * 8) * 2);
      const size_t base = (size_t)(ks * 256) * FF + cg * 64;
#define TDEPTH 3
#pragma unroll
      for (int i = 0; i < 8; ++i) {
        if (i >= TDEPTH) asm volatile("s_waitcnt vmcnt(%0)" ::"i"(TDEPTH - 1) : "memory");
        GLOAD16NT(wgu[i], vo, (i < 4 ? wg : wu) + base + (size_t)(64 * (i & 3)) * FF);
      }
      asm volatile("s_waitcnt vmcnt(0)"
                   : "+v"(wgu[0]), "+v"(wgu[1]), "+v"(wgu[2]), "+v"(wgu[3]), "+v"(wgu[4]), "+v"(wgu[5]), "+v"(wgu[6]), "+v"(wgu[7])::"memory");
#pragma unroll
      for (int i = 0; i < 8; ++i)
#pragma unroll
        for (int j = 0; j < 4; ++j) { wf[i][j] = up2(wgu[i][j]); asm volatile("" : "+v"(wf[i][j])); }
    }
    if (wid > 8) {
      const unsigned vo = (unsigned)(((rg - 8) * D + c8 * 8) * 2);
#pragma unroll
      for (int i = 0; i < 4; ++i) {
        if (i >= TDEPTH) asm volatile("s_waitcnt vmcnt(%0)" ::"i"(TDEPTH - 1) : "memory");
        GLOAD16NT(wdn[i], vo, wd + (size_t)(dks * 224 + 56 * i) * D + dcg * 64);
      }
    }
    LDS_BARRIER();
    if (has_gu) {
      f32x2 ag[4], au[4];
#pragma unroll
      for (int i = 0; i < 4; ++i) {
        const float h = s_h[rg + 64 * i];
#pragma unroll
        for (int j = 0; j < 4; ++j) {
          ag[j] = i == 0 ? wf[0][j] * h : wf[i][j] * h + ag[j];
          au[j] = i == 0 ? wf[4][j] * h : wf[4 + i][j] * h + au[j];
        }
      }
      float v8[8], v4[4];
#pragma unroll
      for (int j = 0; j < 8; ++j) v8[j] = swap32_add(ag[j >> 1][j & 1], au[j >> 1][j & 1]);
#pragma unroll
      for (int j = 0; j < 4; ++j) v4[j] = dpp_add<0x128>(swap16_add(v8[j], v8[4 + j]));
      if ((lane & 8) == 0)
        *(f32x4*)&s_red[wid - 8][(lane >> 5) * 64 + (lane & 7) * 8 + ((lane >> 4) & 1) * 4] = (f32x4){v4[0], v4[1], v4[2], v4[3]};
    }
    LDS_BARRIER();
    f32x2 wdf[4][4];
    if (wid == 8) {
      if (has_gu) {
        float sg = 0.f, su = 0.f;
#pragma unroll
        for (int w = 0; w < 8; ++w) { sg += s_red[w][lane]; su += s_red[w][64 + lane]; }
        __hip_atomic_store(gp + ks * FF + cg * 64 + lane, mk64(__float_as_uint(sg) | 1u, __float_as_uint(su)), RLX_AGENT);
      }
    } else {
      asm volatile("s_waitcnt vmcnt(0)" : "+v"(wdn[0]), "+v"(wdn[1]), "+v"(wdn[2]), "+v"(wdn[3])::"memory");
#pragma unroll
      for (int i = 0; i < 4; ++i)
#pragma unroll
        for (int j = 0; j < 4; ++j) { wdf[i][j] = up2(wdn[i][j]); asm volatile("" : "+v"(wdf[i][j])); }
    }
    LDS_BARRIER();
    if (wid > 8) {
      f32x2 a[4];
#pragma unroll
      for (int i = 0; i < 4; ++i) {
        const float h = s_a[rg - 8 + 56 * i];
#pragma unroll
        for (int j = 0; j < 4; ++j) a[j] = i == 0 ? wdf[0][j] * h : wdf[i][j] * h + a[j];
      }
      float v4[4], v2[2];
#pragma unroll
      for (int j = 0; j < 4; ++j) v4[j] = swap32_add(a[j >> 1][j & 1], a[2 + (j >> 1)][j & 1]);
#pragma unroll
      for (int j = 0; j < 2; ++j) v2[j] = dpp_add<0x128>(swap16_add(v4[j], v4[2 + j]));
      if ((lane & 8) == 0)
        *(f32x2*)&s_red2[wid - 8][(lane & 7) * 8 + (lane >> 5) * 4 + ((lane >> 4) & 1) * 2] = (f32x2){v2[0], v2[1]};
    }
    LDS_BARRIER();
    PROF_END(5);
    return;
  }
  const int row = b * 8 + wid;
  u32x4 xa, xb, g2a, g2b, sba, sbb, gba, gbb, wra, wrb;
  {
    const unsigned vo = lane * 16;
    const u16* xr = x1 + (size_t)row * D;
    GLOAD16(xa, vo, xr); GLOAD16(xb, vo, xr + 512);
    GLOAD16(g2a, vo, modf + 4096); GLOAD16(g2b, vo, modf + 4096 + 512);
    GLOAD16(sba, vo, modf + 3072); GLOAD16(sbb, vo, modf + 3072 + 512);
    GLOAD16(wra, vo, wr); GLOAD16(wrb, vo, wr + 512);
    GLOAD16(gba, vo, modf + 5120); GLOAD16(gbb, vo, modf + 5120 + 512);
  }
  const int col = has_gu ? ks * 256 + lane * 4 : dcg * 64 + (lane & 15) * 4;
  u32x2 g2s, sbs, gbs;
  if (wid == 0) {
    const unsigned vo = col * 2;
    asm volatile("global_load_dwordx2 %0, %1, %2" : "=v"(g2s) : "v"(vo), "s"(modf + 4096) : "memory");
    asm volatile("global_load_dwordx2 %0, %1, %2" : "=v"(sbs) : "v"(vo), "s"(modf + 3072) : "memory");
    asm volatile("global_load_dwordx2 %0, %1, %2" : "=v"(gbs) : "v"(vo), "s"(modf + 5120) : "memory");
  }
  if (t == 0) s_cnt = 0;
  LDS_BARRIER();
  asm volatile("s_waitcnt vmcnt(0)"
               : "+v"(xa), "+v"(xb), "+v"(g2a), "+v"(g2b), "+v"(sba), "+v"(sbb), "+v"(wra), "+v"(wrb), "+v"(gba), "+v"(gbb),
                 "+v"(g2s), "+v"(sbs), "+v"(gbs)::"memory");
  TS(1);
  f32x2 xv[8], hv[8];
#pragma unroll
  for (int j = 0; j < 4; ++j) { xv[j] = up2(xa[j]); xv[4 + j] = up2(xb[j]); }
  f32x2 s2 = ((xv[0] + xv[1]) + (xv[2] + xv[3])) + ((xv[4] + xv[5]) + (xv[6] + xv[7]));
  const float mean = wsum(s2[0] + s2[1]) * (1.f / D);
  f32x2 q2 = (f32x2){0.f, 0.f}, q3 = (f32x2){0.f, 0.f};
#pragma unroll
  for (int j = 0; j < 8; j += 2) {
    hv[j] = xv[j] - mean; q2 = hv[j] * hv[j] + q2;
    hv[j + 1] = xv[j + 1] - mean; q3 = hv[j + 1] * hv[j + 1] + q3;
  }
  q2 += q3;
  const float rstd = __builtin_amdgcn_rsqf(wsum(q2[0] + q2[1]) * (1.f / D) + 1e-5f);
  f32x2 r2 = (f32x2){0.f, 0.f}, r3 = (f32x2){0.f, 0.f};
#pragma unroll
  for (int j = 0; j < 8; ++j) {
    const f32x2 g = up2(j < 4 ? g2a[j] : g2b[j - 4]), sb = up2(j < 4 ? sba[j] : sbb[j - 4]);
    const f32x2 n = hv[j] * rstd;
    const f32x2 hh = (f32x2){fmaf(n[0], g[0], sb[0]), fmaf(n[1], g[1], sb[1])};
    hv[j] = up2(pack2(hh[0], hh[1]));
    if (j & 1) r3 = hv[j] * up2(j < 4 ? wra[j] : wrb[j - 4]) + r3;
    else r2 = hv[j] * up2(j < 4 ? wra[j] : wrb[j - 4]) + r2;
  }
  r2 += r3;
  const float rr = wsum(r2[0] + r2[1]);
  if (lane == 0) {
    *(f32x4*)s_row[wid] = (f32x4){rr, mean, rstd, 0.f};
    __hip_atomic_fetch_add(&s_cnt, 1u, __ATOMIC_RELEASE, __HIP_MEMORY_SCOPE_WORKGROUP);
  }
  float fx0 = 0.f, fx1 = 0.f, fh0 = 0.f, fh1 = 0.f, fg0 = 0.f, fg1 = 0.f, fsig = 0.f;
  if (wid == 0) {
    while (__hip_atomic_load(&s_cnt, __ATOMIC_ACQUIRE, __HIP_MEMORY_SCOPE_WORKGROUP) < 8u) {}
    TS(2);
    int wb = 0;
    float bk = -INFINITY;
#pragma unroll
    for (int w = 0; w < 8; ++w) {
      const float v = s_row[w][0];
      if (v > bk) { bk = v; wb = w; }
    }
    if (lane < 2) {
      const f32x4 v = *(const f32x4*)s_row[wb];
      const unsigned lo = __float_as_uint(lane == 0 ? v[0] : v[1]);
      const unsigned hi = (lane == 0 ? (unsigned)(b * 8 + wb) : __float_as_uint(v[2])) | 0x80000000u;
      __hip_atomic_store(g1 + b * 16 + lane, mk64(lo, hi), RLX_AGENT);
    }
  }
  u32x4 o0, o1;
#pragma unroll
  for (int j = 0; j < 8; ++j) {
    const f32x2 gb = up2(j < 4 ? gba[j] : gbb[j - 4]);
    const unsigned w = pack2(fmaf(gb[0], hv[j][0], xv[j][0]), fmaf(gb[1], hv[j][1], xv[j][1]));
    if (j < 4) o0[j] = w; else o1[j - 4] = w;
  }
  if (wid == 0) {
    u32x4 c[4];
    const unsigned vo = lane * 128;
    _Pragma("nounroll") for (unsigned spin = 0; spin < SPIN_CAP; ++spin) {
      asm volatile("global_load_dwordx4 %0, %4, %5 sc1\n\tglobal_load_dwordx4 %1, %4, %6 sc1\n\t"
                   "global_load_dwordx4 %2, %4, %7 sc1\n\tglobal_load_dwordx4 %3, %4, %8 sc1\n\ts_waitcnt vmcnt(0)"
                   : "=&v"(c[0]), "=&v"(c[1]), "=&v"(c[2]), "=&v"(c[3])
                   : "v"(vo), "s"(sync), "s"(sync + 1024), "s"(sync + 2048), "s"(sync + 3072)
                   : "memory");
      const unsigned m = (c[0][1] & c[0][3]) & (c[1][1] & c[1][3]) & (c[2][1] & c[2][3]) & (c[3][1] & c[3][3]);
      if (__all((int)m < 0)) break;
      __builtin_amdgcn_s_sleep(1);
    }
    TS(3);
    unsigned bk2 = 0, bi = 0, bm = 0, bs = 0, braw = 0;
#pragma unroll
    for (int i = 0; i < 4; ++i) {
      const unsigned raw = c[i][0];
      const float v0 = __uint_as_float(raw);
      const unsigned key = v0 == v0 ? (raw ^ ((unsigned)((int)raw >> 31) | 0x80000000u)) : 0u;
      const bool take = i == 0 || key > bk2;
      bk2 = take ? key : bk2;
      bi = take ? (c[i][1] & 0x7ffu) : bi;
      braw = take ? raw : braw;
      bm = take ? c[i][2] : bm;
      bs = take ? c[i][3] : bs;
    }
    const unsigned kmax = wmaxu(bk2);
    const int idx = __builtin_amdgcn_readfirstlane((int)~wmaxu(bk2 == kmax ? ~bi : 0u));
    const u32x2 xs = *(const u32x2*)(x1 + (size_t)idx * D + col);
    const int wl = (idx >> 3) & 63;
    const float sel_r = __uint_as_float(__builtin_amdgcn_readlane(braw, wl));
    const float sel_mean = __uint_as_float(__builtin_amdgcn_readlane(bm, wl));
    const float sel_rstd = __uint_as_float(__builtin_amdgcn_readlane(bs, wl) & 0x7fffffffu);
    float hs[4];
#pragma unroll
    for (int j = 0; j < 2; ++j) {
      const float n0 = (bflo(xs[j]) - sel_mean) * sel_rstd, n1 = (bfhi(xs[j]) - sel_mean) * sel_rstd;
      const unsigned w = pack2(fmaf(n0, bflo(g2s[j]), bflo(sbs[j])), fmaf(n1, bfhi(g2s[j]), bfhi(sbs[j])));
      hs[2 * j] = bflo(w); hs[2 * j + 1] = bfhi(w);
    }
    *(f32x4*)(s_h + lane * 4) = (f32x4){hs[0], hs[1], hs[2], hs[3]};
    if (lane == 0) s_idx = idx;
    TS(4);
    LDS_BARRIER();
    if (b >= 240) {
      const int src = (lane & 31) >> 1, e = lane & 1;
      const unsigned x0 = __shfl(xs[0], src), x1w = __shfl(xs[1], src);
      const unsigned gq0 = __shfl(gbs[0], src), gq1 = __shfl(gbs[1], src);
      const float h0 = __shfl(hs[0], src), h1 = __shfl(hs[1], src), h2 = __shfl(hs[2], src), h3 = __shfl(hs[3], src);
      const unsigned xw2 = e ? x1w : x0, gw2 = e ? gq1 : gq0;
      fx0 = bflo(xw2); fx1 = bfhi(xw2); fg0 = bflo(gw2); fg1 = bfhi(gw2);
      fh0 = e ? h2 : h0; fh1 = e ? h3 : h1;
      const float rs = rbf(sel_r);
      fsig = rbf(1.f / (1.f + __expf(-rs)));
    }
  } else {
    LDS_BARRIER();
  }
  TS(5);
  const int idx = __builtin_amdgcn_readfirstlane(s_idx);
  u16* orow = out + (size_t)row * D + lane * 8;
  if (wid >= 4 && row != idx) { *(u32x4*)orow = o0; *(u32x4*)(orow + 512) = o1; }
  asm volatile("s_barrier" ::: "memory");
  TS(6);
  if (wid < 4) {
    const int u = t < 224 ? t : 223;
    unsigned long long c[4];
    _Pragma("nounroll") for (unsigned spin = 0; spin < SPIN_CAP; ++spin) {
      bool ok = true;
#pragma unroll
      for (int i = 0; i < 4; ++i) {
        c[i] = __hip_atomic_load(gp + i * FF + dks * 224 + u, RLX_AGENT);
        ok &= (c[i] & 1ull) != 0;
      }
      if (__all(ok)) break;
      __builtin_amdgcn_s_sleep(1);
    }
    float sg = 0.f, su = 0.f;
#pragma unroll
    for (int i = 0; i < 4; ++i) { sg += __uint_as_float((unsigned)c[i]); su += __uint_as_float((unsigned)(c[i] >> 32)); }
    sg = rbf(sg); su = rbf(su);
    const float si = rbf(sg / (1.f + __expf(-sg)));
    s_a[t] = rbf(si * su);
  }
  TS(7);
  LDS_BARRIER();
  TS(8);
  if (wid > 0 && wid < 4 && row != idx) { *(u32x4*)orow = o0; *(u32x4*)(orow + 512) = o1; }
  asm volatile("s_barrier" ::: "memory");
  if (wid == 0) {
    if (lane < 32) {
      f32x2 y = (f32x2){0.f, 0.f};
#pragma unroll
      for (int w = 1; w < 8; ++w) y += *(const f32x2*)&s_red2[w][lane * 2];
      __hip_atomic_store(gy + dks * 512 + dcg * 32 + lane, mk64(__float_as_uint(y[0]) | 1u, __float_as_uint(y[1])), RLX_AGENT);
    }
    TS(9);
    if (row != idx) { *(u32x4*)orow = o0; *(u32x4*)(orow + 512) = o1; }
    if (b >= 240) {
      unsigned long long c[8];
      _Pragma("nounroll") for (unsigned spin = 0; spin < SPIN_CAP; ++spin) {
        bool ok = true;
#pragma unroll
        for (int i = 0; i < 8; ++i) {
          c[i] = __hip_atomic_load(gy + ((lane >> 5) * 8 + i) * 512 + dcg * 32 + (lane & 31), RLX_AGENT);
          ok &= (c[i] & 1ull) != 0;
        }
        if (__all(ok)) break;
        __builtin_amdgcn_s_sleep(1);
      }
      float y0 = 0.f, y1 = 0.f;
#pragma unroll
      for (int i = 0; i < 8; ++i) { y0 += __uint_as_float((unsigned)c[i]); y1 += __uint_as_float((unsigned)(c[i] >> 32)); }
      y0 = swap32_add(y0, y0); y1 = swap32_add(y1, y1);
      y0 = rbf(rbf(y0) * fsig); y1 = rbf(rbf(y1) * fsig);
      const unsigned w = pack2(fmaf(fg0, rbf(fh0 + y0), fx0), fmaf(fg1, rbf(fh1 + y1), fx1));
      if (lane < 32) *(unsigned*)(out + (size_t)idx * D + dcg * 64 + lane * 2) = w;
    }
  }
  TS(10);
  TS_END(5, 0);
  PROF_END(5);
}

extern "C" void kernel_launch(void* const* d_in, const int* in_sizes, int n_in, void* d_out, int out_size, void* d_ws,
                              size_t ws_size, hipStream_t stream) {
  const u16* x = (const u16*)d_in[0];
  const u16* wqkv = (const u16*)d_in[1];
  const u16* wo = (const u16*)d_in[2];
  const u16* wr = (const u16*)d_in[4];
  const u16* wg = (const u16*)d_in[5];
  const u16* wu = (const u16*)d_in[6];
  const u16* wd = (const u16*)d_in[7];
  const u16* c = (const u16*)d_in[8];
  const u16* wmod = (const u16*)d_in[9];
  char* ws = (char*)d_ws;
  float* modp = (float*)(ws + WS_MODP);
  float* ln1 = (float*)(ws + WS_LN1);
  u16* modf = (u16*)(ws + WS_MODF);
  u16* qf = (u16*)(ws + WS_Q);
  u16* kf = (u16*)(ws + WS_K);
  u16* vf = (u16*)(ws + WS_V);
  u16* of = (u16*)(ws + WS_O);
  u16* x1 = (u16*)(ws + WS_X1);
  u16* out = (u16*)d_out;
  (void)hipFuncSetAttribute((const void*)k2_qkv, hipFuncAttributeMaxDynamicSharedMemorySize, K2_LDS);
  (void)hipFuncSetAttribute((const void*)k3_attn, hipFuncAttributeMaxDynamicSharedMemorySize, K3_LDS);
  (void)hipFuncSetAttribute((const void*)k4_oproj, hipFuncAttributeMaxDynamicSharedMemorySize, K4_LDS);
  k1_mod_ln<<<256, 512, 0, stream>>>(c, wmod, x, modp, ln1, (u32x4*)(ws + WS_SYNC));
  k2_qkv<<<256, 512, K2_LDS, stream>>>(x, wqkv, modp, ln1, qf, kf, vf, modf);
  k3_attn<<<256, 512, K3_LDS, stream>>>(qf, kf, vf, of);
  k4_oproj<<<256, 512, K4_LDS, stream>>>(of, wo, x, modp, x1);
  k5_tail<<<256, 1024, 0, stream>>>(x1, modf, wr, wg, wu, wd, out, (unsigned long long*)(ws + WS_SYNC));
}
```

```cpp
#include <hip/hip_runtime.h>
#include <hip/hip_bf16.h>
#include <stdint.h>

typedef unsigned short u16;
using bf16x8 = __attribute__((ext_vector_type(8))) short;
using s16x4 = __attribute__((ext_vector_type(4))) short;
using f32x16 = __attribute__((ext_vector_type(16))) float;
using f32x4 = __attribute__((ext_vector_type(4))) float;
using f32x2 = __attribute__((ext_vector_type(2))) float;
using u32x4 = __attribute__((ext_vector_type(4))) unsigned;
using u32x2 = __attribute__((ext_vector_type(2))) unsigned;
typedef __bf16 bf16x2_t __attribute__((ext_vector_type(2)));
#define LAS __attribute__((address_space(3)))
#define DI __device__ __forceinline__

constexpr int S = 2048, D = 1024, NH = 32, FF = 3584;
constexpr float QSCALE = 0.17677669529663687f * 1.4426950408889634f;

constexpr size_t WS_MODP = 0;
constexpr size_t WS_MODF = 98304;
constexpr size_t WS_SYNC = 131072;
constexpr int SYNC_VEC16 = (4096 + 4 * 3584 + 16 * 512) / 2;
constexpr size_t WS_Q = 1u << 20;
constexpr size_t WS_K = WS_Q + (4u << 20);
constexpr size_t WS_V = WS_K + (4u << 20);
constexpr size_t WS_O = WS_V + (4u << 20);
constexpr size_t WS_X1 = WS_O + (4u << 20);
constexpr size_t WS_N = WS_X1 + (4u << 20);

#define PROF_BEGIN()
#define PROF_END(kid)
#define TS_DECL()
#define TS(i)
#define TS_END(kid, w)

#define TR(base, w, kt, i)
#define TR_FLUSH(base, w)
#define TR_INIT(base, w)

DI float bf2f(u16 v) { return __uint_as_float((unsigned)v << 16); }
DI float bflo(unsigned w) { return __uint_as_float(w << 16); }
DI float bfhi(unsigned w) { return __uint_as_float(w & 0xffff0000u); }
DI unsigned pack2(float lo, float hi) {
  f32x2 v = {lo, hi};
  bf16x2_t b = __builtin_convertvector(v, bf16x2_t);
  return __builtin_bit_cast(unsigned, b);
}
DI float rbf(float x) { return bflo(pack2(x, 0.f)); }
#define MFMA32(a, b, c) __builtin_amdgcn_mfma_f32_32x32x16_bf16((a), (b), (c), 0, 0, 0)
#define MFMA16(a, b, c) __builtin_amdgcn_mfma_f32_16x16x32_bf16((a), (b), (c), 0, 0, 0)
#define SBAR() __builtin_amdgcn_sched_barrier(0)
#define LDS_BARRIER() asm volatile("s_waitcnt lgkmcnt(0)\n\ts_barrier" ::: "memory")

DI float wave_sum(float v) {
#pragma unroll
  for (int o = 32; o > 0; o >>= 1) v += __shfl_xor(v, o);
  return v;
}
template <int CTRL>
DI float dpp_add(float v) {
  return v + __uint_as_float(__builtin_amdgcn_update_dpp(0u, __float_as_uint(v), CTRL, 0xf, 0xf, true));
}
DI float swap32_add(float a, float b) {
  auto r = __builtin_amdgcn_permlane32_swap(__float_as_uint(a), __float_as_uint(b), false, false);
  return __uint_as_float(r[0]) + __uint_as_float(r[1]);
}
DI float swap16_add(float a, float b) {
  auto r = __builtin_amdgcn_permlane16_swap(__float_as_uint(a), __float_as_uint(b), false, false);
  return __uint_as_float(r[0]) + __uint_as_float(r[1]);
}
DI float wsum(float v) {
  v = dpp_add<0xB1>(v);
  v = dpp_add<0x4E>(v);
  v = dpp_add<0x141>(v);
  v = dpp_add<0x140>(v);
  v = swap16_add(v, v);
  return swap32_add(v, v);
}
DI unsigned ln_pair(float lo, float hi) {
  const unsigned l = pack2(lo, 0.f), b = __float_as_uint(hi);
  const int t = max((int)(b & 0x7fffffffu) - (int)l + 0x8000, 0);
  return (b & 0x80000000u) | ((unsigned)t & 0x7fff0000u) | l;
}
DI void glds16(const void* sbase, unsigned voff, unsigned lds_dst) {
  unsigned keep;
  asm volatile("s_mov_b32 %0, m0\n\ts_mov_b32 m0, %3\n\ts_nop 0\n\tglobal_load_lds_dwordx4 %1, %2\n\ts_mov_b32 m0, %0"
               : "=&s"(keep)
               : "v"(voff), "s"(sbase), "s"(lds_dst)
               : "memory");
}
DI void st16(void* p, u32x4 v) {
  asm volatile("s_nop 0\n\tglobal_store_dwordx4 %0, %1, off sc1\n\ts_nop 1" ::"v"(p), "v"(v) : "memory");
}
DI bf16x8 lds_b128(unsigned addr) { return *(const LAS bf16x8*)(uintptr_t)addr; }
typedef short v4i16_t __attribute__((ext_vector_type(4)));
DI s16x4 lds_tr(unsigned addr) {
  return __builtin_bit_cast(s16x4, __builtin_amdgcn_ds_read_tr16_b64_v4i16((LAS v4i16_t*)(uintptr_t)addr));
}
DI bf16x8 cat8(s16x4 lo, s16x4 hi) { return __builtin_shufflevector(lo, hi, 0, 1, 2, 3, 4, 5, 6, 7); }

#define WPFPOL ""
__global__ __launch_bounds__(512) void k1_mod_ln(const u16* __restrict__ c, const u16* __restrict__ wmod,
                                                 const u16* __restrict__ x, float* __restrict__ modp,
                                                 u16* __restrict__ nbuf, u32x4* __restrict__ sync,
                                                 const u16* __restrict__ wqkv) {
  PROF_BEGIN();
  extern __shared__ __attribute__((aligned(16))) char smem[];
  float (*red)[64] = (float (*)[64])smem;
  const int t = threadIdx.x, lane = t & 63, wid = t >> 6, bid = blockIdx.x;
  static_assert(SYNC_VEC16 == 256 * 52, "sync clear");
  if (t < 52) sync[bid * 52 + t] = (u32x4){0u, 0u, 0u, 0u};
  if (bid < 192) {
    const int cg = bid % 96, kh = bid / 96;
    const int c8 = t & 7, rg = t >> 3;
    u32x4 w[8];
    float sc[8];
#pragma unroll
    for (int i = 0; i < 8; ++i) {
      const int k = kh * 512 + rg + 64 * i;
      w[i] = __builtin_nontemporal_load((const u32x4*)(wmod + (size_t)k * 6144 + cg * 64 + c8 * 8));
      const float cv = bf2f(c[k]);
      sc[i] = rbf(cv / (1.f + __expf(-cv)));
    }
    float acc[8] = {0.f, 0.f, 0.f, 0.f, 0.f, 0.f, 0.f, 0.f};
#pragma unroll
    for (int i = 0; i < 8; ++i)
#pragma unroll
      for (int j = 0; j < 4; ++j) {
        acc[2 * j] = fmaf(sc[i], bflo(w[i][j]), acc[2 * j]);
        acc[2 * j + 1] = fmaf(sc[i], bfhi(w[i][j]), acc[2 * j + 1]);
      }
#pragma unroll
    for (int j = 0; j < 8; ++j) {
      acc[j] += __shfl_xor(acc[j], 8);
      acc[j] += __shfl_xor(acc[j], 16);
      acc[j] += __shfl_xor(acc[j], 32);
    }
    if (lane < 8) {
#pragma unroll
      for (int j = 0; j < 8; ++j) red[wid][lane * 8 + j] = acc[j];
    }
    __syncthreads();
    if (t < 64) {
      float s = 0.f;
#pragma unroll
      for (int w8 = 0; w8 < 8; ++w8) s += red[w8][t];
      modp[kh * 6144 + cg * 64 + t] = s;
    }
  } else {
    const int r0 = (bid - 192) * 32 + wid * 4;
    u32x4 a[4], b[4];
#pragma unroll
    for (int i = 0; i < 4; ++i) {
      a[i] = *(const u32x4*)(x + (size_t)(r0 + i) * D + lane * 8);
      b[i] = *(const u32x4*)(x + (size_t)(r0 + i) * D + 512 + lane * 8);
    }
#pragma unroll
    for (int i = 0; i < 4; ++i) {
      float v[16];
#pragma unroll
      for (int j = 0; j < 4; ++j) {
        v[2 * j] = bflo(a[i][j]); v[2 * j + 1] = bfhi(a[i][j]);
        v[8 + 2 * j] = bflo(b[i][j]); v[8 + 2 * j + 1] = bfhi(b[i][j]);
      }
      float s = 0.f;
#pragma unroll
      for (int j = 0; j < 16; ++j) s += v[j];
      const float mean = wsum(s) * (1.f / D);
      float q = 0.f;
#pragma unroll
      for (int j = 0; j < 16; ++j) { v[j] -= mean; q = fmaf(v[j], v[j], q); }
      const float rstd = __builtin_amdgcn_rsqf(wsum(q) * (1.f / D) + 1e-5f);
      u32x4 o0, o1;
#pragma unroll
      for (int j = 0; j < 4; ++j) {
        o0[j] = ln_pair(v[2 * j] * rstd, v[2 * j + 1] * rstd);
        o1[j] = ln_pair(v[8 + 2 * j] * rstd, v[8 + 2 * j + 1] * rstd);
      }
      const int sw = ((wid * 4 + i) ^ (lane & 31)) * 16;
      *(u32x4*)(smem + lane * 512 + sw) = o0;
      *(u32x4*)(smem + (64 + lane) * 512 + sw) = o1;
    }
    __syncthreads();
#pragma unroll
    for (int i = 0; i < 8; ++i) {
      const int c = wid * 16 + i * 2 + (lane >> 5);
      const u32x4 v = *(const u32x4*)(smem + c * 512 + (((lane ^ c) & 31) * 16));
      st16(nbuf + (size_t)(bid - 192) * 32768 + (wid * 8 + i) * 512 + lane * 8, v);
    }
  }
  {
    const u16* wp = wqkv + (size_t)((bid >> 3) * 32 + wid * 4 + (lane >> 5)) * 3072 + ((bid & 7) >> 1) * 256 + (lane & 31) * 8;
    u32x4 pf[6];
#pragma unroll
    for (int i = 0; i < 6; ++i)
      asm volatile("global_load_dwordx4 %0, %1, off" WPFPOL : "=v"(pf[i]) : "v"(wp + (i & 1) * 2 * 3072 + (i >> 1) * 1024) : "memory");
    asm volatile("s_waitcnt vmcnt(0)" : "+v"(pf[0]), "+v"(pf[1]), "+v"(pf[2]), "+v"(pf[3]), "+v"(pf[4]), "+v"(pf[5])::"memory");
  }
  PROF_END(1);
}

constexpr int K2_NB = 6, K2_BSLOT = 24576, K2_DW = 3, K2_DN = 6;
constexpr int K2_LDS_GS = K2_NB * K2_BSLOT, K2_LDS_TR = K2_LDS_GS + 8192, K2_LDS = K2_LDS_TR + 2048;

#define GLOAD16(dst, voff, sbase) \
  asm volatile("global_load_dwordx4 %0, %1, %2" : "=v"(dst) : "v"(voff), "s"(sbase) : "memory")

#define GLOAD16NT(dst, voff, sbase) \
  asm volatile("global_load_dwordx4 %0, %1, %2 nt" : "=v"(dst) : "v"(voff), "s"(sbase) : "memory")

struct K2Sched { int wn[33]; int ww[17]; };
constexpr int k2_younger(const int* seq, int n, int id) {
  int c = 0;
  for (int i = n - 1; i >= 0 && seq[i] != id; --i) ++c;
  return c;
}
constexpr K2Sched k2_make() {
  K2Sched s{};
  int seq[128] = {};
  int n = 0;
  seq[n++] = 200; seq[n++] = 201;
  for (int i = 0; i < 4; ++i) {
    if (i == 0) seq[n++] = 0;
    if (i < K2_DW) { seq[n++] = 100 + 3 * i; seq[n++] = 101 + 3 * i; seq[n++] = 102 + 3 * i; }
    if (2 * i + 1 < K2_DN) seq[n++] = 2 * i + 1;
    if (2 * i + 2 < K2_DN) seq[n++] = 2 * i + 2;
  }
  s.wn[32] = k2_younger(seq, n, 201);
  s.ww[16] = k2_younger(seq, n, 102);
  seq[n++] = K2_DN;
  for (int p = 0; p < 32; ++p) {
    const int t = p >> 1, j = p & 1;
    if (p + 1 <= 31) s.wn[p] = k2_younger(seq, n, p + 1);
    if (j == 1 && t + 1 <= 15) s.ww[t] = k2_younger(seq, n, 100 + 3 * (t + 1) + 2);
    if (p + 1 + K2_DN <= 31) seq[n++] = p + 1 + K2_DN;
    if (t + K2_DW <= 15) {
      if (j == 0) { seq[n++] = 100 + 3 * (t + K2_DW); seq[n++] = 101 + 3 * (t + K2_DW); }
      else seq[n++] = 102 + 3 * (t + K2_DW);
    }
  }
  return s;
}
constexpr K2Sched K2S = k2_make();

#define K2_WPOL ""
template <int KT, int PC>
DI void k2_issue_b(const u16* wq, unsigned wvoff, unsigned lds_dst) {
  if constexpr (KT <= 15) {
    const u16* sb = wq + (size_t)KT * 64 * 3072 + (PC == 2 ? 2048 - 512 : 0);
    asm volatile("s_nop 0\n\tglobal_load_lds_dwordx4 %0, %1 offset:%2" K2_WPOL
                 :
                 : "v"(wvoff), "s"(sb), "i"(PC == 0 ? 0 : PC == 1 ? 2048 : 1024), "{m0}"(lds_dst + (KT % K2_NB) * K2_BSLOT)
                 : "memory");
  }
}
template <int F>
DI void k2_issue_n(const u16* nb, unsigned nvoff, u32x4& r) {
  if constexpr (F <= 31)
    asm volatile("global_load_dwordx4 %0, %1, %2 offset:%3" : "=v"(r) : "v"(nvoff), "s"(nb + (F >> 1) * 2048), "i"((F & 1) * 1024) : "memory");
}
struct K2GS { f32x4 g0, g1, s0, s1; };
DI K2GS k2_gs(unsigned gsa) {
  K2GS r;
  r.g0 = *(const LAS f32x4*)(uintptr_t)(gsa); r.g1 = *(const LAS f32x4*)(uintptr_t)(gsa + 16);
  r.s0 = *(const LAS f32x4*)(uintptr_t)(gsa + 4096); r.s1 = *(const LAS f32x4*)(uintptr_t)(gsa + 4096 + 16);
  return r;
}
template <int WAIT>
DI u32x4 k2_xform(u32x4& x, const K2GS& q) {
  asm volatile("s_waitcnt vmcnt(%1)" : "+v"(x) : "i"(WAIT) : "memory");
  unsigned t0, t1, t2, t3, o0, o1, o2, o3;
  asm volatile(
      "v_lshlrev_b32 %4, 16, %24\n\tv_lshlrev_b32 %5, 16, %25\n\tv_lshlrev_b32 %6, 16, %26\n\tv_lshlrev_b32 %7, 16, %27\n\t"
      "v_fma_f32 %0, %24, %9, %17\n\tv_fma_f32 %1, %25, %11, %19\n\tv_fma_f32 %2, %26, %13, %21\n\tv_fma_f32 %3, %27, %15, %23\n\t"
      "v_fma_f32 %4, %4, %8, %16\n\tv_fma_f32 %5, %5, %10, %18\n\tv_fma_f32 %6, %6, %12, %20\n\tv_fma_f32 %7, %7, %14, %22\n\t"
      "v_cvt_pk_bf16_f32 %0, %4, %0\n\tv_cvt_pk_bf16_f32 %1, %5, %1\n\tv_cvt_pk_bf16_f32 %2, %6, %2\n\tv_cvt_pk_bf16_f32 %3, %7, %3\n\t"
      "s_nop 1"
      : "=&v"(o0), "=&v"(o1), "=&v"(o2), "=&v"(o3), "=&v"(t0), "=&v"(t1), "=&v"(t2), "=&v"(t3)
      : "v"(q.g0[0]), "v"(q.g0[1]), "v"(q.g0[2]), "v"(q.g0[3]), "v"(q.g1[0]), "v"(q.g1[1]), "v"(q.g1[2]), "v"(q.g1[3]),
        "v"(q.s0[0]), "v"(q.s0[1]), "v"(q.s0[2]), "v"(q.s0[3]), "v"(q.s1[0]), "v"(q.s1[1]), "v"(q.s1[2]), "v"(q.s1[3]),
        "v"(x[0]), "v"(x[1]), "v"(x[2]), "v"(x[3]));
  return (u32x4){o0, o1, o2, o3};
}

struct K2State {
  f32x16 acc[2][3];
  bf16x8 fb[2][6];
  u32x4 nr[K2_DN];
  u32x4 hf[2];
  K2GS gs;
};
template <int P>
DI bf16x8 k2_ldb(const unsigned (&bb)[2][2], int hd, int sg) {
  constexpr int slot = (P >> 1) % K2_NB;
  const unsigned a = bb[hd][slot / 3] + (slot % 3) * K2_BSLOT + (P & 1) * 6144 + (sg == 0 ? 0 : sg == 1 ? 2048 : 1024);
  return cat8(lds_tr(a), lds_tr(a + 512));
}
template <int P>
DI void k2_phase(K2State& st, const unsigned (&bb)[2][2], unsigned gsa, const u16* wq, unsigned wvoff, unsigned wdst,
                 const u16* nb, unsigned nvoff, unsigned lds0) {
  constexpr int C = P & 1, N = C ^ 1, T = P >> 1, J = P & 1;
  constexpr bool NEXT = P < 31;
  const bf16x8 hf = __builtin_bit_cast(bf16x8, st.hf[C]);
#define LDB2(I0) do { if constexpr (NEXT) { st.fb[N][I0] = k2_ldb<P + 1>(bb, (I0) / 3, (I0) % 3); \
                                            st.fb[N][I0 + 1] = k2_ldb<P + 1>(bb, (I0 + 1) / 3, (I0 + 1) % 3); } } while (0)
#define MM(I) do { SBAR();                                                                                   \
    if ((I) % 3 == 2) st.acc[(I) / 3][2] = MFMA32(hf, st.fb[C][I], st.acc[(I) / 3][2]);                      \
    else st.acc[(I) / 3][(I) % 3] = MFMA32(st.fb[C][I], hf, st.acc[(I) / 3][(I) % 3]);                       \
    SBAR(); } while (0)
  if constexpr (J == 0) TR(lds0 + K2_LDS_TR, 0, T, 0);
  MM(0);
  if constexpr (NEXT) st.hf[N] = k2_xform<K2S.wn[P]>(st.nr[(P + 1) % K2_DN], st.gs);
  MM(1);
  if constexpr (J == 1 && NEXT) {
    TR(lds0 + K2_LDS_TR, 0, T, 1);
    asm volatile("s_waitcnt vmcnt(%0)" ::"i"(K2S.ww[T]) : "memory");
    TR(lds0 + K2_LDS_TR, 0, T, 2);
    asm volatile("s_barrier" ::: "memory");
    TR(lds0 + K2_LDS_TR, 0, T, 3);
  }
  LDB2(0);
  k2_issue_n<P + 1 + K2_DN>(nb, nvoff, st.nr[(P + 1) % K2_DN]);
  MM(2);
  LDB2(2);
  k2_issue_b<T + K2_DW, J == 0 ? 0 : 2>(wq, wvoff, wdst);
  MM(3);
  LDB2(4);
  if constexpr (J == 0) k2_issue_b<T + K2_DW, 1>(wq, wvoff, wdst);
  MM(4);
  if constexpr (P < 30) st.gs = k2_gs(gsa + ((P + 2) >> 1) * 256 + (P & 1) * 64);
  MM(5);
#undef LDB2
#undef MM
}
template <int P>
DI void k2_phases(K2State& st, const unsigned (&bb)[2][2], unsigned gsa, const u16* wq, unsigned wvoff, unsigned wdst,
                  const u16* nb, unsigned nvoff, unsigned lds0) {
  k2_phase<P>(st, bb, gsa, wq, wvoff, wdst, nb, nvoff, lds0);
  if constexpr (P < 31) k2_phases<P + 1>(st, bb, gsa, wq, wvoff, wdst, nb, nvoff, lds0);
}

__global__ __launch_bounds__(512) void k2_qkv(const u16* __restrict__ nf, const u16* __restrict__ wqkv,
                                              const float* __restrict__ modp,
                                              u16* __restrict__ qf, u16* __restrict__ kf, u16* __restrict__ vf,
                                              u16* __restrict__ modf) {
  PROF_BEGIN();
  extern __shared__ __attribute__((aligned(16))) char smem[];
  const unsigned lds0 = (unsigned)(uintptr_t)smem;
  const int tid = threadIdx.x, lane = tid & 63;
  const int wid = __builtin_amdgcn_readfirstlane(tid >> 6);
  const int bid = blockIdx.x, xcd = bid & 7, jj = bid >> 3;
  const int mt = (xcd & 1) * 8 + (jj & 7), nt = (xcd >> 1) * 4 + (jj >> 3);
  const int wm = wid & 3, kh = wid >> 2;
  TR_INIT(lds0 + K2_LDS_TR, 0);
  TR(lds0 + K2_LDS_TR, 0, 16, 0);
  K2State st;
  f32x4 mp0, mp1;
  {
    const unsigned vo = (tid & 255) * 16;
    const float* mb = modp + (kh == 0 ? 1024 : 0);
    GLOAD16(mp0, vo, mb);
    GLOAD16(mp1, vo, mb + 6144);
  }
  const int kr = lane >> 3, pos = lane & 7;
  const unsigned wvoff = (unsigned)(((wid * 8 + kr) * 3072 + (((pos >> 2) ^ ((kr >> 1) & 1)) * 32) + (pos & 3) * 8) * 2);
  const u16* wq = wqkv + nt * 64;
  const unsigned wdst = lds0 + wid * 3072;
  const u16* nb = nf + (size_t)(mt * 4 + wm) * 32768 + kh * 1024;
  const unsigned nvoff = lane * 16;
#define WT(KT) do { if constexpr (KT < K2_DW) { k2_issue_b<KT, 0>(wq, wvoff, wdst); k2_issue_b<KT, 1>(wq, wvoff, wdst); \
                                                k2_issue_b<KT, 2>(wq, wvoff, wdst); } } while (0)
#define NT(F) do { if constexpr (F < K2_DN) k2_issue_n<F>(nb, nvoff, st.nr[(F) % K2_DN]); } while (0)
  NT(0); WT(0); NT(1); NT(2); WT(1); NT(3); NT(4); WT(2); NT(5); NT(6); WT(3); NT(7); NT(8);
#undef WT
#undef NT
  TR(lds0 + K2_LDS_TR, 0, 16, 1);
  asm volatile("s_waitcnt vmcnt(%2)" : "+v"(mp0), "+v"(mp1) : "i"(K2S.wn[32]) : "memory");
  {
    f32x4 v;
#pragma unroll
    for (int j = 0; j < 4; ++j) {
      v[j] = rbf(mp0[j] + mp1[j]);
      if (kh == 0) v[j] = rbf(1.f + v[j]);
    }
    *(LAS f32x4*)(uintptr_t)(lds0 + K2_LDS_GS + tid * 16) = v;
  }
  const int h = lane >> 5, q4 = (lane & 15) >> 2, p4 = lane & 3, ch = (lane >> 4) & 1;
  unsigned bb[2][2];
#pragma unroll
  for (int hd = 0; hd < 2; ++hd) {
    bb[hd][0] = lds0 + kh * 12288 + h * 3072 + q4 * 128 + (((hd ^ kh) ^ ((q4 >> 1) & 1)) * 64) + ch * 32 + p4 * 8;
    bb[hd][1] = bb[hd][0] + 3 * K2_BSLOT;
    asm volatile("" : "+v"(bb[hd][0]), "+v"(bb[hd][1]));
  }
  unsigned gsa = lds0 + K2_LDS_GS + (32 * kh + 8 * h) * 4;
  asm volatile("" : "+v"(gsa));
#pragma unroll
  for (int a = 0; a < 2; ++a)
#pragma unroll
    for (int b = 0; b < 3; ++b) st.acc[a][b] = (f32x16){};
  asm volatile("s_waitcnt vmcnt(%0) lgkmcnt(0)\n\ts_barrier" ::"i"(K2S.ww[16]) : "memory");
  TR(lds0 + K2_LDS_TR, 0, 16, 2);
  st.gs = k2_gs(gsa);
#pragma unroll
  for (int i = 0; i < 6; ++i) st.fb[0][i] = k2_ldb<0>(bb, i / 3, i % 3);
  st.hf[0] = k2_xform<K2S.ww[16]>(st.nr[0], st.gs);
  k2_issue_n<K2_DN>(nb, nvoff, st.nr[0]);
  st.gs = k2_gs(gsa + 64);
  k2_phases<0>(st, bb, gsa, wq, wvoff, wdst, nb, nvoff, lds0);
  LDS_BARRIER();
#pragma unroll
  for (int sg = 0; sg < 3; ++sg)
#pragma unroll
    for (int g = 0; g < 4; ++g)
      *(LAS f32x4*)(uintptr_t)(lds0 + (wid ^ 4) * 12288 + sg * 4096 + g * 1024 + lane * 16) =
          (f32x4){st.acc[1][sg][4 * g], st.acc[1][sg][4 * g + 1], st.acc[1][sg][4 * g + 2], st.acc[1][sg][4 * g + 3]};
  LDS_BARRIER();
  const size_t off = ((size_t)((nt * 2 + kh) * 64 + mt * 4 + wm) * 2) * 512 + lane * 8;
#pragma unroll
  for (int sg = 0; sg < 3; ++sg) {
    u16* dst = (sg == 0 ? qf : sg == 1 ? kf : vf) + off;
    const float sc = sg == 0 ? QSCALE : 1.f;
#pragma unroll
    for (int s = 0; s < 2; ++s) {
      const f32x4 a = *(const LAS f32x4*)(uintptr_t)(lds0 + wid * 12288 + sg * 4096 + s * 2048 + lane * 16);
      const f32x4 b = *(const LAS f32x4*)(uintptr_t)(lds0 + wid * 12288 + sg * 4096 + s * 2048 + 1024 + lane * 16);
      u32x4 o;
#pragma unroll
      for (int j = 0; j < 4; ++j) {
        const float lo = st.acc[0][sg][8 * s + 2 * j] + (j < 2 ? a[2 * j] : b[2 * j - 4]);
        const float hi = st.acc[0][sg][8 * s + 2 * j + 1] + (j < 2 ? a[2 * j + 1] : b[2 * j - 3]);
        o[j] = pack2(lo * sc, hi * sc);
      }
      st16(dst + s * 512, o);
    }
  }
  if (bid >= 2 && bid < 6 && tid < 256) {
    const f32x4 p0 = *(const f32x4*)(modp + bid * 1024 + tid * 4), p1 = *(const f32x4*)(modp + 6144 + bid * 1024 + tid * 4);
    float v[4];
#pragma unroll
    for (int j = 0; j < 4; ++j) { v[j] = rbf(p0[j] + p1[j]); if (bid == 4) v[j] += 1.f; }
    *(u32x2*)(modf + bid * 1024 + tid * 4) = (u32x2){pack2(v[0], v[1]), pack2(v[2], v[3])};
  }
  PROF_END(2);
}

#define PF4POL ""
constexpr int K3_PART = 4096 + 256, K3_LDS = 8 * 4 * K3_PART;

DI float ex2(float x) { return __builtin_amdgcn_exp2f(x); }
DI float xhalf_max(float v) {
  auto rr = __builtin_amdgcn_permlane32_swap(__float_as_uint(v), __float_as_uint(v), false, false);
  return fmaxf(__uint_as_float(rr[0]), __uint_as_float(rr[1]));
}

__global__ __launch_bounds__(512) void k3_attn(const u16* __restrict__ qf, const u16* __restrict__ kf,
                                               const u16* __restrict__ vf, u16* __restrict__ of,
                                               const u16* __restrict__ wo, const u16* __restrict__ x) {
  PROF_BEGIN();
  extern __shared__ __attribute__((aligned(16))) char smem[];
  const unsigned lds0 = (unsigned)(uintptr_t)smem;
  const int tid = threadIdx.x, lane = tid & 63;
  const int wid = __builtin_amdgcn_readfirstlane(tid >> 6);
  const int bid = blockIdx.x, xcd = bid & 7, jj = bid >> 3;
  const int head = xcd * 4 + (jj >> 3), qb = jj & 7;
  const int qg = wid >> 2, kq = wid & 3;
  const int qt0 = qb * 8 + qg * 4;
  const u16* kh = kf + (size_t)head * 65536;
  const u16* vh = vf + (size_t)head * 65536;
  const u16* kp = kh + kq * 16384 + lane * 8;
  const u16* vp = vh + kq * 16384 + lane * 8;
  TS_DECL(); TS(0);
#define LDF(ptr) (*(const bf16x8*)(ptr))
  bf16x8 ka0 = LDF(kp), ka1 = LDF(kp + 512);
  bf16x8 q[4][2];
#pragma unroll
  for (int i = 0; i < 4; ++i) {
    const u16* qp = qf + ((size_t)(head * 64 + qt0 + ((i + kq) & 3)) * 2) * 512 + lane * 8;
    q[i][0] = LDF(qp); q[i][1] = LDF(qp + 512);
  }
  bf16x8 va0 = LDF(vp), va1 = LDF(vp + 512);
  bf16x8 kb0 = LDF(kp + 1024), kb1 = LDF(kp + 1024 + 512);
  bf16x8 vb0, vb1 = va1;
  bf16x8 ones;
  {
    const short o = ((((lane & 15) >> 2) & 1) == ((lane >> 4) & 1)) ? (short)0x3F80 : (short)0;
#pragma unroll
    for (int j = 0; j < 8; ++j) ones[j] = o;
  }
  const f32x16 zero = (f32x16){};
  f32x16 o[4] = {zero, zero, zero, zero};
  f32x4 l[4] = {(f32x4){}, (f32x4){}, (f32x4){}, (f32x4){}};
  f32x16 sA = MFMA32(ka0, q[0][0], zero), sB;
  sA = MFMA32(ka1, q[0][1], sA);
  u32x4 p0, p1 = (u32x4){0u, 0u, 0u, 0u};
#define PK(S_, i) pack2(S_[i], S_[i + 1])
#define EX(S_, i) S_[i] = ex2(S_[i])
#define PIN(x_) asm volatile("" : "+v"(x_))
#define UNIT(SC, SN, KN0, KN1, N, C, V0C, P, V1P, LD_A, LD_B)                               \
  do {                                                                                     \
    SN = MFMA32(KN0, q[N][0], zero); SBAR();                                               \
    EX(SC, 0); EX(SC, 1); EX(SC, 2); p0[0] = PK(SC, 0); PIN(p0); SBAR();                   \
    o[P] = MFMA32(V1P, __builtin_bit_cast(bf16x8, p1), o[P]); SBAR();                      \
    LD_A;                                                                                  \
    EX(SC, 3); EX(SC, 4); EX(SC, 5); p0[1] = PK(SC, 2); PIN(p0); SBAR();                   \
    l[P] = MFMA16(ones, __builtin_bit_cast(bf16x8, p1), l[P]); SBAR();                     \
    EX(SC, 6); EX(SC, 7); p0[2] = PK(SC, 4); p0[3] = PK(SC, 6); PIN(p0); SBAR();           \
    SN = MFMA32(KN1, q[N][1], SN); SBAR();                                                 \
    LD_B;                                                                                  \
    EX(SC, 8); EX(SC, 9); EX(SC, 10); p1[0] = PK(SC, 8); PIN(p1); SBAR();                  \
    o[C] = MFMA32(V0C, __builtin_bit_cast(bf16x8, p0), o[C]); SBAR();                      \
    EX(SC, 11); EX(SC, 12); EX(SC, 13); p1[1] = PK(SC, 10); PIN(p1); SBAR();               \
    l[C] = MFMA16(ones, __builtin_bit_cast(bf16x8, p0), l[C]); SBAR();                     \
    EX(SC, 14); EX(SC, 15); p1[2] = PK(SC, 12); p1[3] = PK(SC, 14); PIN(p1); SBAR();       \
  } while (0)
  TS(1);
#pragma unroll 1
  for (int j = 0; j < 16; j += 2) {
    const u16* kn = kp + ((j + 2) & 15) * 1024;
    const u16* vn = vp + (j + 1) * 1024;
    const u16* vn2 = vp + ((j + 2) & 15) * 1024;
    UNIT(sA, sB, ka0, ka1, 1, 0, va0, 3, vb1, vb0 = LDF(vn), vb1 = LDF(vn + 512));
    UNIT(sB, sA, ka0, ka1, 2, 1, va0, 0, va1, (void)0, (void)0);
    UNIT(sA, sB, ka0, ka1, 3, 2, va0, 1, va1, (void)0, (void)0);
    UNIT(sB, sA, kb0, kb1, 0, 3, va0, 2, va1, ka0 = LDF(kn), ka1 = LDF(kn + 512));
    UNIT(sA, sB, kb0, kb1, 1, 0, vb0, 3, va1, va0 = LDF(vn2), va1 = LDF(vn2 + 512));
    UNIT(sB, sA, kb0, kb1, 2, 1, vb0, 0, vb1, (void)0, (void)0);
    UNIT(sA, sB, kb0, kb1, 3, 2, vb0, 1, vb1, (void)0, (void)0);
    UNIT(sB, sA, ka0, ka1, 0, 3, vb0, 2, vb1, kb0 = LDF(kn + 1024), kb1 = LDF(kn + 1024 + 512));
  }
  o[3] = MFMA32(vb1, __builtin_bit_cast(bf16x8, p1), o[3]);
  l[3] = MFMA16(ones, __builtin_bit_cast(bf16x8, p1), l[3]);
  TS(2);
#undef UNIT
#pragma unroll
  for (int i = 1; i < 4; ++i) {
    const unsigned wa = lds0 + ((qg * 4 + ((i + kq) & 3)) * 4 + kq) * K3_PART;
#pragma unroll
    for (int g = 0; g < 4; ++g)
      *(LAS f32x4*)(uintptr_t)(wa + g * 1024 + lane * 16) = (f32x4){o[i][4 * g], o[i][4 * g + 1], o[i][4 * g + 2], o[i][4 * g + 3]};
    *(LAS float*)(uintptr_t)(wa + 4096 + lane * 4) = l[i][0];
  }
  if (qg == 0) {
    u32x4 pf[10];
    const u16* wp = wo + (size_t)((jj * 4 + kq) * 8) * D + (xcd & 1) * 512 + lane * 8;
    const u16* xp = x + (size_t)((xcd >> 1) * 512 + (jj * 4 + kq) * 4) * D + (xcd & 1) * 512 + lane * 8;
#pragma unroll
    for (int r = 0; r < 2; ++r) {
#pragma unroll
      for (int i = 0; i < 4; ++i)
        asm volatile("global_load_dwordx4 %0, %1, off" PF4POL : "=v"(pf[i]) : "v"(wp + (r * 4 + i) * D) : "memory");
#pragma unroll
      for (int i = 0; i < 2; ++i)
        asm volatile("global_load_dwordx4 %0, %1, off" PF4POL : "=v"(pf[8 + i]) : "v"(xp + (r * 2 + i) * D) : "memory");
      asm volatile("s_waitcnt vmcnt(0)" : "+v"(pf[0]), "+v"(pf[1]), "+v"(pf[2]), "+v"(pf[3]),
                   "+v"(pf[8]), "+v"(pf[9])::"memory");
    }
  }
  LDS_BARRIER();
  f32x16 oo = o[0];
  float lsum = l[0][0];
#pragma unroll
  for (int i = 1; i < 4; ++i) {
    const unsigned ra = lds0 + (wid * 4 + ((i + kq) & 3)) * K3_PART;
#pragma unroll
    for (int g = 0; g < 4; ++g) {
      const f32x4 v = *(const LAS f32x4*)(uintptr_t)(ra + g * 1024 + lane * 16);
      oo[4 * g] += v[0]; oo[4 * g + 1] += v[1]; oo[4 * g + 2] += v[2]; oo[4 * g + 3] += v[3];
    }
    lsum += *(const LAS float*)(uintptr_t)(ra + 4096 + lane * 4);
  }
  float rl = 1.f / lsum;
  bool bad = !(lsum < 1.0e30f && lsum > 1.0e-30f);
#pragma unroll
  for (int r = 0; r < 16; ++r) {
    oo[r] *= rl;
    bad |= !(fabsf(oo[r]) < 3.0e38f);
  }
  if (__builtin_expect(__any(bad), 0)) {
    const bf16x8 q0 = q[0][0], q1 = q[0][1];
    float m = -INFINITY, ls = 0.f;
    oo = zero;
    for (int t = 0; t < 64; ++t) {
      const bf16x8 a0 = LDF(kh + (size_t)t * 1024 + lane * 8), a1 = LDF(kh + (size_t)t * 1024 + 512 + lane * 8);
      const bf16x8 b0 = LDF(vh + (size_t)t * 1024 + lane * 8), b1 = LDF(vh + (size_t)t * 1024 + 512 + lane * 8);
      f32x16 s = MFMA32(a0, q0, zero);
      s = MFMA32(a1, q1, s);
      float tm = s[0];
#pragma unroll
      for (int r = 1; r < 16; ++r) tm = fmaxf(tm, s[r]);
      tm = xhalf_max(tm);
      const float mn = fmaxf(m, tm);
      const float alpha = ex2(m - mn);
      m = mn;
      u32x4 pa, pb;
      float ps = 0.f;
#pragma unroll
      for (int j = 0; j < 4; ++j) {
        pa[j] = pack2(ex2(s[2 * j] - mn), ex2(s[2 * j + 1] - mn));
        pb[j] = pack2(ex2(s[8 + 2 * j] - mn), ex2(s[8 + 2 * j + 1] - mn));
        ps += bflo(pa[j]) + bfhi(pa[j]) + bflo(pb[j]) + bfhi(pb[j]);
      }
      ps += __shfl_xor(ps, 32);
      ls = ls * alpha + ps;
#pragma unroll
      for (int r = 0; r < 16; ++r) oo[r] *= alpha;
      oo = MFMA32(b0, __builtin_bit_cast(bf16x8, pa), oo);
      oo = MFMA32(b1, __builtin_bit_cast(bf16x8, pb), oo);
    }
    rl = 1.f / ls;
#pragma unroll
    for (int r = 0; r < 16; ++r) oo[r] *= rl;
  }
  u16* op = of + ((size_t)((qt0 + kq) * 32 + head) * 2) * 512 + lane * 8;
#pragma unroll
  for (int s = 0; s < 2; ++s) {
    u32x4 w;
#pragma unroll
    for (int j = 0; j < 4; ++j) w[j] = pack2(oo[8 * s + 2 * j], oo[8 * s + 2 * j + 1]);
    st16(op + s * 512, w);
  }
  TS(3);
  TS_END(3, 0);
  PROF_END(3);
#undef PK
#undef EX
#undef LDF
}

constexpr int K4_LDS = 131072;
__global__ __launch_bounds__(512) void k4_oproj(const u16* __restrict__ of, const u16* __restrict__ wo,
                                                const u16* __restrict__ x, const float* __restrict__ modp,
                                                u16* __restrict__ x1) {
  PROF_BEGIN();
  extern __shared__ __attribute__((aligned(16))) char smem[];
  const unsigned lds0 = (unsigned)(uintptr_t)smem;
  const int tid = threadIdx.x, lane = tid & 63;
  const int wid = __builtin_amdgcn_readfirstlane(tid >> 6);
  const int bid = blockIdx.x, xcd = bid & 7, jj = bid >> 3;
  const int mt = (xcd >> 1) * 4 + (jj & 3), nt = (xcd & 1) * 8 + (jj >> 2);
  const int kh = wid >> 2, wt = wid & 3;
  const int kr = lane >> 3, pos = lane & 7;
  const unsigned wvoff = (unsigned)(((wt * 16 + kr) * D + (((pos >> 2) ^ ((kr >> 1) & 1)) * 32) + (pos & 3) * 8) * 2);
  const u16* wb = wo + (size_t)(kh * 512) * D + nt * 64;
  const u16* ob = of + ((size_t)((mt * 4 + wt) * 32 + kh * 16) * 2) * 512;
  const unsigned ovoff = lane * 16;
  u32x4 ofr[32];
  TS_DECL(); TS(0);
#pragma unroll
  for (int st = 0; st < 8; ++st) {
    glds16(wb + (size_t)(st * 64) * D, wvoff, lds0 + kh * 65536 + st * 8192 + wt * 2048);
    glds16(wb + (size_t)(st * 64 + 8) * D, wvoff, lds0 + kh * 65536 + st * 8192 + wt * 2048 + 1024);
#pragma unroll
    for (int i = 0; i < 4; ++i) {
      const u16* sb = ob + (size_t)(st * 4 + i) * 512;
      GLOAD16(ofr[st * 4 + i], ovoff, sb);
    }
  }
  const int erow = tid >> 3, ec = tid & 7;
  u32x4 xr[2];
  {
    const unsigned xv = (unsigned)((erow * D + ec * 8) * 2);
    const u16* xb0 = x + (size_t)(mt * 128) * D + nt * 64;
    const u16* xb1 = xb0 + (size_t)64 * D;
    GLOAD16(xr[0], xv, xb0);
    GLOAD16(xr[1], xv, xb1);
  }
  TS(1);
  const int h = lane >> 5, q4 = (lane & 15) >> 2, p4 = lane & 3, ch = (lane >> 4) & 1;
  const unsigned bb0 = lds0 + kh * 65536 + (4 * h + q4) * 128 + ((0 ^ ((q4 >> 1) & 1)) * 64) + ch * 32 + p4 * 8;
  const unsigned bb1 = lds0 + kh * 65536 + (4 * h + q4) * 128 + ((1 ^ ((q4 >> 1) & 1)) * 64) + ch * 32 + p4 * 8;
  f32x16 acc0 = (f32x16){}, acc1 = (f32x16){};
#pragma unroll
  for (int st = 0; st < 8; ++st) {
    asm volatile("s_waitcnt vmcnt(%4)\n\ts_barrier"
                 : "+v"(ofr[st * 4]), "+v"(ofr[st * 4 + 1]), "+v"(ofr[st * 4 + 2]), "+v"(ofr[st * 4 + 3])
                 : "i"((7 - st) * 6 + 2)
                 : "memory");
    if (st == 0) TS(2);
    if (st == 2) TS(3);
    if (st == 4) TS(4);
    if (st == 7) TS(5);
#pragma unroll
    for (int i = 0; i < 4; ++i) {
      const unsigned o = st * 8192 + i * 2048;
      const bf16x8 w0 = cat8(lds_tr(bb0 + o), lds_tr(bb0 + o + 1024));
      const bf16x8 w1 = cat8(lds_tr(bb1 + o), lds_tr(bb1 + o + 1024));
      const bf16x8 a = __builtin_bit_cast(bf16x8, ofr[st * 4 + i]);
      acc0 = MFMA32(w0, a, acc0);
      acc1 = MFMA32(w1, a, acc1);
    }
  }
  LDS_BARRIER();
  TS(6);
  {
    const unsigned wa = lds0 + kh * 34816 + (wt * 32 + (lane & 31)) * 272 + h * 16;
#pragma unroll
    for (int g = 0; g < 4; ++g) {
      *(LAS f32x4*)(uintptr_t)(wa + g * 32) = (f32x4){acc0[4 * g], acc0[4 * g + 1], acc0[4 * g + 2], acc0[4 * g + 3]};
      *(LAS f32x4*)(uintptr_t)(wa + 128 + g * 32) = (f32x4){acc1[4 * g], acc1[4 * g + 1], acc1[4 * g + 2], acc1[4 * g + 3]};
    }
  }
  asm volatile("s_waitcnt vmcnt(0) lgkmcnt(0)\n\ts_barrier" : "+v"(xr[0]), "+v"(xr[1])::"memory");
  TS(7);
  float ga[8];
  {
    const float* g0 = modp + 2048 + nt * 64 + ec * 8;
#pragma unroll
    for (int j = 0; j < 8; ++j) ga[j] = rbf(g0[j] + g0[6144 + j]);
  }
#pragma unroll
  for (int i = 0; i < 2; ++i) {
    const int row = erow + 64 * i;
    const unsigned ra = lds0 + row * 272 + ec * 32;
    const f32x4 a0 = *(const LAS f32x4*)(uintptr_t)(ra), a1 = *(const LAS f32x4*)(uintptr_t)(ra + 16);
    const f32x4 b0 = *(const LAS f32x4*)(uintptr_t)(ra + 34816), b1 = *(const LAS f32x4*)(uintptr_t)(ra + 34816 + 16);
    u32x4 o;
#pragma unroll
    for (int j = 0; j < 4; ++j) {
      const float lo = (j < 2 ? a0[2 * j] + b0[2 * j] : a1[2 * j - 4] + b1[2 * j - 4]);
      const float hi = (j < 2 ? a0[2 * j + 1] + b0[2 * j + 1] : a1[2 * j - 3] + b1[2 * j - 3]);
      o[j] = pack2(fmaf(ga[2 * j], lo, bflo(xr[i][j])), fmaf(ga[2 * j + 1], hi, bfhi(xr[i][j])));
    }
    st16(x1 + (size_t)(mt * 128 + row) * D + nt * 64 + ec * 8, o);
  }
  TS_END(4, 0);
  PROF_END(4);
}

#define GAS __attribute__((address_space(1)))
typedef GAS unsigned long long gu64;
#define RLX_AGENT __ATOMIC_RELAXED, __HIP_MEMORY_SCOPE_AGENT
constexpr unsigned SPIN_CAP = 1u << 17;
DI unsigned long long mk64(unsigned lo, unsigned hi) { return ((unsigned long long)hi << 32) | lo; }
template <int CTRL>
DI unsigned dpp_maxu(unsigned v) {
  const unsigned o = __builtin_amdgcn_update_dpp(0u, v, CTRL, 0xf, 0xf, true);
  return v > o ? v : o;
}
DI unsigned wmaxu(unsigned v) {
  v = dpp_maxu<0xB1>(v); v = dpp_maxu<0x4E>(v); v = dpp_maxu<0x141>(v); v = dpp_maxu<0x140>(v);
  auto r = __builtin_amdgcn_permlane16_swap(v, v, false, false);
  v = r[0] > r[1] ? r[0] : r[1];
  auto q = __builtin_amdgcn_permlane32_swap(v, v, false, false);
  return q[0] > q[1] ? q[0] : q[1];
}
DI f32x2 up2(unsigned w) { return (f32x2){bflo(w), bfhi(w)}; }
#define GLOADV(dst, ptr) asm volatile("global_load_dwordx4 %0, %1, off" : "=v"(dst) : "v"(ptr) : "memory")

__global__ __launch_bounds__(1024) void k5_tail(const u16* __restrict__ x1, const u16* __restrict__ modf,
                                                const u16* __restrict__ wr, const u16* __restrict__ wg,
                                                const u16* __restrict__ wu, const u16* __restrict__ wd,
                                                u16* __restrict__ out, unsigned long long* sync) {
  PROF_BEGIN();
  __shared__ __attribute__((aligned(16))) float s_row[8][4];
  __shared__ __attribute__((aligned(16))) float s_h[256];
  __shared__ __attribute__((aligned(16))) float s_a[256];
  __shared__ __attribute__((aligned(16))) float s_red[8][128];
  __shared__ __attribute__((aligned(16))) float s_red2[8][64];
  __shared__ int s_idx;
  __shared__ unsigned s_cnt;
  const int t = threadIdx.x, lane = t & 63;
  const int wid = __builtin_amdgcn_readfirstlane(t >> 6);
  const int b = blockIdx.x;
  gu64* g1 = (gu64*)sync;
  gu64* gp = (gu64*)sync + 4096;
  gu64* gy = (gu64*)sync + 4096 + 4 * FF;
  const bool has_gu = b < 224;
  const int cg = b % 56, ks = b / 56;
  const int dcg = b & 15, dks = b >> 4;
  TS_DECL(); TS(0);
  if (wid >= 8) {
    const int tt = t - 512, c8 = tt & 7, rg = tt >> 3;
    asm volatile("s_barrier" ::: "memory");
    f32x2 wf[8][4];
    u32x4 wdn[4];
    if (has_gu) {
      u32x4 wgu[8];
      const unsigned vo = (unsigned)((rg * FF + c8 * 8) * 2);
      const size_t base = (size_t)(ks * 256) * FF + cg * 64;
#define TDEPTH 3
#pragma unroll
      for (int i = 0; i < 8; ++i) {
        if (i >= TDEPTH) asm volatile("s_waitcnt vmcnt(%0)" ::"i"(TDEPTH - 1) : "memory");
        GLOAD16NT(wgu[i], vo, (i < 4 ? wg : wu) + base + (size_t)(64 * (i & 3)) * FF);
      }
      asm volatile("s_waitcnt vmcnt(0)"
                   : "+v"(wgu[0]), "+v"(wgu[1]), "+v"(wgu[2]), "+v"(wgu[3]), "+v"(wgu[4]), "+v"(wgu[5]), "+v"(wgu[6]), "+v"(wgu[7])::"memory");
#pragma unroll
      for (int i = 0; i < 8; ++i)
#pragma unroll
        for (int j = 0; j < 4; ++j) { wf[i][j] = up2(wgu[i][j]); asm volatile("" : "+v"(wf[i][j])); }
    }
    if (wid > 8) {
      const unsigned vo = (unsigned)(((rg - 8) * D + c8 * 8) * 2);
#pragma unroll
      for (int i = 0; i < 4; ++i) {
        if (i >= TDEPTH) asm volatile("s_waitcnt vmcnt(%0)" ::"i"(TDEPTH - 1) : "memory");
        GLOAD16NT(wdn[i], vo, wd + (size_t)(dks * 224 + 56 * i) * D + dcg * 64);
      }
    }
    LDS_BARRIER();
    if (has_gu) {
      f32x2 ag[4], au[4];
#pragma unroll
      for (int i = 0; i < 4; ++i) {
        const float h = s_h[rg + 64 * i];
#pragma unroll
        for (int j = 0; j < 4; ++j) {
          ag[j] = i == 0 ? wf[0][j] * h : wf[i][j] * h + ag[j];
          au[j] = i == 0 ? wf[4][j] * h : wf[4 + i][j] * h + au[j];
        }
      }
      float v8[8], v4[4];
#pragma unroll
      for (int j = 0; j < 8; ++j) v8[j] = swap32_add(ag[j >> 1][j & 1], au[j >> 1][j & 1]);
#pragma unroll
      for (int j = 0; j < 4; ++j) v4[j] = dpp_add<0x128>(swap16_add(v8[j], v8[4 + j]));
      if ((lane & 8) == 0)
        *(f32x4*)&s_red[wid - 8][(lane >> 5) * 64 + (lane & 7) * 8 + ((lane >> 4) & 1) * 4] = (f32x4){v4[0], v4[1], v4[2], v4[3]};
    }
    LDS_BARRIER();
    f32x2 wdf[4][4];
    if (wid == 8) {
      if (has_gu) {
        float sg = 0.f, su = 0.f;
#pragma unroll
        for (int w = 0; w < 8; ++w) { sg += s_red[w][lane]; su += s_red[w][64 + lane]; }
        __hip_atomic_store(gp + ks * FF + cg * 64 + lane, mk64(__float_as_uint(sg) | 1u, __float_as_uint(su)), RLX_AGENT);
      }
    } else {
      asm volatile("s_waitcnt vmcnt(0)" : "+v"(wdn[0]), "+v"(wdn[1]), "+v"(wdn[2]), "+v"(wdn[3])::"memory");
#pragma unroll
      for (int i = 0; i < 4; ++i)
#pragma unroll
        for (int j = 0; j < 4; ++j) { wdf[i][j] = up2(wdn[i][j]); asm volatile("" : "+v"(wdf[i][j])); }
    }
    LDS_BARRIER();
    if (wid > 8) {
      f32x2 a[4];
#pragma unroll
      for (int i = 0; i < 4; ++i) {
        const float h = s_a[rg - 8 + 56 * i];
#pragma unroll
        for (int j = 0; j < 4; ++j) a[j] = i == 0 ? wdf[0][j] * h : wdf[i][j] * h + a[j];
      }
      float v4[4], v2[2];
#pragma unroll
      for (int j = 0; j < 4; ++j) v4[j] = swap32_add(a[j >> 1][j & 1], a[2 + (j >> 1)][j & 1]);
#pragma unroll
      for (int j = 0; j < 2; ++j) v2[j] = dpp_add<0x128>(swap16_add(v4[j], v4[2 + j]));
      if ((lane & 8) == 0)
        *(f32x2*)&s_red2[wid - 8][(lane & 7) * 8 + (lane >> 5) * 4 + ((lane >> 4) & 1) * 2] = (f32x2){v2[0], v2[1]};
    }
    LDS_BARRIER();
    PROF_END(5);
    return;
  }
  const int row = b * 8 + wid;
  u32x4 xa, xb, g2a, g2b, sba, sbb, gba, gbb, wra, wrb;
  {
    const unsigned vo = lane * 16;
    const u16* xr = x1 + (size_t)row * D;
    GLOAD16(xa, vo, xr); GLOAD16(xb, vo, xr + 512);
    GLOAD16(g2a, vo, modf + 4096); GLOAD16(g2b, vo, modf + 4096 + 512);
    GLOAD16(sba, vo, modf + 3072); GLOAD16(sbb, vo, modf + 3072 + 512);
    GLOAD16(wra, vo, wr); GLOAD16(wrb, vo, wr + 512);
    GLOAD16(gba, vo, modf + 5120); GLOAD16(gbb, vo, modf + 5120 + 512);
  }
  const int col = has_gu ? ks * 256 + lane * 4 : dcg * 64 + (lane & 15) * 4;
  u32x2 g2s, sbs, gbs;
  if (wid == 0) {
    const unsigned vo = col * 2;
    asm volatile("global_load_dwordx2 %0, %1, %2" : "=v"(g2s) : "v"(vo), "s"(modf + 4096) : "memory");
    asm volatile("global_load_dwordx2 %0, %1, %2" : "=v"(sbs) : "v"(vo), "s"(modf + 3072) : "memory");
    asm volatile("global_load_dwordx2 %0, %1, %2" : "=v"(gbs) : "v"(vo), "s"(modf + 5120) : "memory");
  }
  if (t == 0) s_cnt = 0;
  LDS_BARRIER();
  asm volatile("s_waitcnt vmcnt(0)"
               : "+v"(xa), "+v"(xb), "+v"(g2a), "+v"(g2b), "+v"(sba), "+v"(sbb), "+v"(wra), "+v"(wrb), "+v"(gba), "+v"(gbb),
                 "+v"(g2s), "+v"(sbs), "+v"(gbs)::"memory");
  TS(1);
  f32x2 xv[8], hv[8];
#pragma unroll
  for (int j = 0; j < 4; ++j) { xv[j] = up2(xa[j]); xv[4 + j] = up2(xb[j]); }
  f32x2 s2 = ((xv[0] + xv[1]) + (xv[2] + xv[3])) + ((xv[4] + xv[5]) + (xv[6] + xv[7]));
  const float mean = wsum(s2[0] + s2[1]) * (1.f / D);
  f32x2 q2 = (f32x2){0.f, 0.f}, q3 = (f32x2){0.f, 0.f};
#pragma unroll
  for (int j = 0; j < 8; j += 2) {
    hv[j] = xv[j] - mean; q2 = hv[j] * hv[j] + q2;
    hv[j + 1] = xv[j + 1] - mean; q3 = hv[j + 1] * hv[j + 1] + q3;
  }
  q2 += q3;
  const float rstd = __builtin_amdgcn_rsqf(wsum(q2[0] + q2[1]) * (1.f / D) + 1e-5f);
  f32x2 r2 = (f32x2){0.f, 0.f}, r3 = (f32x2){0.f, 0.f};
#pragma unroll
  for (int j = 0; j < 8; ++j) {
    const f32x2 g = up2(j < 4 ? g2a[j] : g2b[j - 4]), sb = up2(j < 4 ? sba[j] : sbb[j - 4]);
    const f32x2 n = hv[j] * rstd;
    const f32x2 hh = (f32x2){fmaf(n[0], g[0], sb[0]), fmaf(n[1], g[1], sb[1])};
    hv[j] = up2(pack2(hh[0], hh[1]));
    if (j & 1) r3 = hv[j] * up2(j < 4 ? wra[j] : wrb[j - 4]) + r3;
    else r2 = hv[j] * up2(j < 4 ? wra[j] : wrb[j - 4]) + r2;
  }
  r2 += r3;
  const float rr = wsum(r2[0] + r2[1]);
  if (lane == 0) {
    *(f32x4*)s_row[wid] = (f32x4){rr, mean, rstd, 0.f};
    __hip_atomic_fetch_add(&s_cnt, 1u, __ATOMIC_RELEASE, __HIP_MEMORY_SCOPE_WORKGROUP);
  }
  float fx0 = 0.f, fx1 = 0.f, fh0 = 0.f, fh1 = 0.f, fg0 = 0.f, fg1 = 0.f, fsig = 0.f;
  if (wid == 0) {
    while (__hip_atomic_load(&s_cnt, __ATOMIC_ACQUIRE, __HIP_MEMORY_SCOPE_WORKGROUP) < 8u) {}
    TS(2);
    int wb = 0;
    float bk = -INFINITY;
#pragma unroll
    for (int w = 0; w < 8; ++w) {
      const float v = s_row[w][0];
      if (v > bk) { bk = v; wb = w; }
    }
    if (lane < 2) {
      const f32x4 v = *(const f32x4*)s_row[wb];
      const unsigned lo = __float_as_uint(lane == 0 ? v[0] : v[1]);
      const unsigned hi = (lane == 0 ? (unsigned)(b * 8 + wb) : __float_as_uint(v[2])) | 0x80000000u;
      __hip_atomic_store(g1 + b * 16 + lane, mk64(lo, hi), RLX_AGENT);
    }
  }
  u32x4 o0, o1;
#pragma unroll
  for (int j = 0; j < 8; ++j) {
    const f32x2 gb = up2(j < 4 ? gba[j] : gbb[j - 4]);
    const unsigned w = pack2(fmaf(gb[0], hv[j][0], xv[j][0]), fmaf(gb[1], hv[j][1], xv[j][1]));
    if (j < 4) o0[j] = w; else o1[j - 4] = w;
  }
  if (wid == 0) {
    u32x4 c[4];
    const unsigned vo = lane * 128;
    _Pragma("nounroll") for (unsigned spin = 0; spin < SPIN_CAP; ++spin) {
      asm volatile("global_load_dwordx4 %0, %4, %5 sc1\n\tglobal_load_dwordx4 %1, %4, %6 sc1\n\t"
                   "global_load_dwordx4 %2, %4, %7 sc1\n\tglobal_load_dwordx4 %3, %4, %8 sc1\n\ts_waitcnt vmcnt(0)"
                   : "=&v"(c[0]), "=&v"(c[1]), "=&v"(c[2]), "=&v"(c[3])
                   : "v"(vo), "s"(sync), "s"(sync + 1024), "s"(sync + 2048), "s"(sync + 3072)
                   : "memory");
      const unsigned m = (c[0][1] & c[0][3]) & (c[1][1] & c[1][3]) & (c[2][1] & c[2][3]) & (c[3][1] & c[3][3]);
      if (__all((int)m < 0)) break;
      __builtin_amdgcn_s_sleep(1);
    }
    TS(3);
    unsigned bk2 = 0, bi = 0, bm = 0, bs = 0, braw = 0;
#pragma unroll
    for (int i = 0; i < 4; ++i) {
      const unsigned raw = c[i][0];
      const float v0 = __uint_as_float(raw);
      const unsigned key = v0 == v0 ? (raw ^ ((unsigned)((int)raw >> 31) | 0x80000000u)) : 0u;
      const bool take = i == 0 || key > bk2;
      bk2 = take ? key : bk2;
      bi = take ? (c[i][1] & 0x7ffu) : bi;
      braw = take ? raw : braw;
      bm = take ? c[i][2] : bm;
      bs = take ? c[i][3] : bs;
    }
    const unsigned kmax = wmaxu(bk2);
    const int idx = __builtin_amdgcn_readfirstlane((int)~wmaxu(bk2 == kmax ? ~bi : 0u));
    const u32x2 xs = *(const u32x2*)(x1 + (size_t)idx * D + col);
    const int wl = (idx >> 3) & 63;
    const float sel_r = __uint_as_float(__builtin_amdgcn_readlane(braw, wl));
    const float sel_mean = __uint_as_float(__builtin_amdgcn_readlane(bm, wl));
    const float sel_rstd = __uint_as_float(__builtin_amdgcn_readlane(bs, wl) & 0x7fffffffu);
    float hs[4];
#pragma unroll
    for (int j = 0; j < 2; ++j) {
      const float n0 = (bflo(xs[j]) - sel_mean) * sel_rstd, n1 = (bfhi(xs[j]) - sel_mean) * sel_rstd;
      const unsigned w = pack2(fmaf(n0, bflo(g2s[j]), bflo(sbs[j])), fmaf(n1, bfhi(g2s[j]), bfhi(sbs[j])));
      hs[2 * j] = bflo(w); hs[2 * j + 1] = bfhi(w);
    }
    *(f32x4*)(s_h + lane * 4) = (f32x4){hs[0], hs[1], hs[2], hs[3]};
    if (lane == 0) s_idx = idx;
    TS(4);
    LDS_BARRIER();
    if (b >= 240) {
      const int src = (lane & 31) >> 1, e = lane & 1;
      const unsigned x0 = __shfl(xs[0], src), x1w = __shfl(xs[1], src);
      const unsigned gq0 = __shfl(gbs[0], src), gq1 = __shfl(gbs[1], src);
      const float h0 = __shfl(hs[0], src), h1 = __shfl(hs[1], src), h2 = __shfl(hs[2], src), h3 = __shfl(hs[3], src);
      const unsigned xw2 = e ? x1w : x0, gw2 = e ? gq1 : gq0;
      fx0 = bflo(xw2); fx1 = bfhi(xw2); fg0 = bflo(gw2); fg1 = bfhi(gw2);
      fh0 = e ? h2 : h0; fh1 = e ? h3 : h1;
      const float rs = rbf(sel_r);
      fsig = rbf(1.f / (1.f + __expf(-rs)));
    }
  } else {
    LDS_BARRIER();
  }
  TS(5);
  const int idx = __builtin_amdgcn_readfirstlane(s_idx);
  u16* orow = out + (size_t)row * D + lane * 8;
  if (wid >= 4 && row != idx) { *(u32x4*)orow = o0; *(u32x4*)(orow + 512) = o1; }
  asm volatile("s_barrier" ::: "memory");
  TS(6);
  if (wid < 4) {
    const int u = t < 224 ? t : 223;
    unsigned long long c[4];
    _Pragma("nounroll") for (unsigned spin = 0; spin < SPIN_CAP; ++spin) {
      bool ok = true;
#pragma unroll
      for (int i = 0; i < 4; ++i) {
        c[i] = __hip_atomic_load(gp + i * FF + dks * 224 + u, RLX_AGENT);
        ok &= (c[i] & 1ull) != 0;
      }
      if (__all(ok)) break;
      __builtin_amdgcn_s_sleep(1);
    }
    float sg = 0.f, su = 0.f;
#pragma unroll
    for (int i = 0; i < 4; ++i) { sg += __uint_as_float((unsigned)c[i]); su += __uint_as_float((unsigned)(c[i] >> 32)); }
    sg = rbf(sg); su = rbf(su);
    const float si = rbf(sg / (1.f + __expf(-sg)));
    s_a[t] = rbf(si * su);
  }
  TS(7);
  LDS_BARRIER();
  TS(8);
  if (wid > 0 && wid < 4 && row != idx) { *(u32x4*)orow = o0; *(u32x4*)(orow + 512) = o1; }
  asm volatile("s_barrier" ::: "memory");
  if (wid == 0) {
    if (lane < 32) {
      f32x2 y = (f32x2){0.f, 0.f};
#pragma unroll
      for (int w = 1; w < 8; ++w) y += *(const f32x2*)&s_red2[w][lane * 2];
      __hip_atomic_store(gy + dks * 512 + dcg * 32 + lane, mk64(__float_as_uint(y[0]) | 1u, __float_as_uint(y[1])), RLX_AGENT);
    }
    TS(9);
    if (row != idx) { *(u32x4*)orow = o0; *(u32x4*)(orow + 512) = o1; }
    if (b >= 240) {
      unsigned long long c[8];
      _Pragma("nounroll") for (unsigned spin = 0; spin < SPIN_CAP; ++spin) {
        bool ok = true;
#pragma unroll
        for (int i = 0; i < 8; ++i) {
          c[i] = __hip_atomic_load(gy + ((lane >> 5) * 8 + i) * 512 + dcg * 32 + (lane & 31), RLX_AGENT);
          ok &= (c[i] & 1ull) != 0;
        }
        if (__all(ok)) break;
        __builtin_amdgcn_s_sleep(1);
      }
      float y0 = 0.f, y1 = 0.f;
#pragma unroll
      for (int i = 0; i < 8; ++i) { y0 += __uint_as_float((unsigned)c[i]); y1 += __uint_as_float((unsigned)(c[i] >> 32)); }
      y0 = swap32_add(y0, y0); y1 = swap32_add(y1, y1);
      y0 = rbf(rbf(y0) * fsig); y1 = rbf(rbf(y1) * fsig);
      const unsigned w = pack2(fmaf(fg0, rbf(fh0 + y0), fx0), fmaf(fg1, rbf(fh1 + y1), fx1));
      if (lane < 32) *(unsigned*)(out + (size_t)idx * D + dcg * 64 + lane * 2) = w;
    }
  }
  TS(10);
  TS_END(5, 0);
  PROF_END(5);
}

extern "C" void kernel_launch(void* const* d_in, const int* in_sizes, int n_in, void* d_out, int out_size, void* d_ws,
                              size_t ws_size, hipStream_t stream) {
  const u16* x = (const u16*)d_in[0];
  const u16* wqkv = (const u16*)d_in[1];
  const u16* wo = (const u16*)d_in[2];
  const u16* wr = (const u16*)d_in[4];
  const u16* wg = (const u16*)d_in[5];
  const u16* wu = (const u16*)d_in[6];
  const u16* wd = (const u16*)d_in[7];
  const u16* c = (const u16*)d_in[8];
  const u16* wmod = (const u16*)d_in[9];
  char* ws = (char*)d_ws;
  float* modp = (float*)(ws + WS_MODP);
  u16* nbuf = (u16*)(ws + WS_N);
  u16* modf = (u16*)(ws + WS_MODF);
  u16* qf = (u16*)(ws + WS_Q);
  u16* kf = (u16*)(ws + WS_K);
  u16* vf = (u16*)(ws + WS_V);
  u16* of = (u16*)(ws + WS_O);
  u16* x1 = (u16*)(ws + WS_X1);
  u16* out = (u16*)d_out;
  (void)hipFuncSetAttribute((const void*)k2_qkv, hipFuncAttributeMaxDynamicSharedMemorySize, K2_LDS);
  (void)hipFuncSetAttribute((const void*)k3_attn, hipFuncAttributeMaxDynamicSharedMemorySize, K3_LDS);
  (void)hipFuncSetAttribute((const void*)k4_oproj, hipFuncAttributeMaxDynamicSharedMemorySize, K4_LDS);
  k1_mod_ln<<<256, 512, 65536, stream>>>(c, wmod, x, modp, nbuf, (u32x4*)(ws + WS_SYNC), wqkv);
  k2_qkv<<<256, 512, K2_LDS, stream>>>(nbuf, wqkv, modp, qf, kf, vf, modf);
  k3_attn<<<256, 512, K3_LDS, stream>>>(qf, kf, vf, of, wo, x);
  k4_oproj<<<256, 512, K4_LDS, stream>>>(of, wo, x, modp, x1);
  k5_tail<<<256, 1024, 0, stream>>>(x1, modf, wr, wg, wu, wd, out, (unsigned long long*)(ws + WS_SYNC));
}
```

```cpp
#include <hip/hip_runtime.h>
#include <hip/hip_bf16.h>
#include <stdint.h>

typedef unsigned short u16;
using bf16x8 = __attribute__((ext_vector_type(8))) short;
using s16x4 = __attribute__((ext_vector_type(4))) short;
using f32x16 = __attribute__((ext_vector_type(16))) float;
using f32x4 = __attribute__((ext_vector_type(4))) float;
using f32x2 = __attribute__((ext_vector_type(2))) float;
using u32x4 = __attribute__((ext_vector_type(4))) unsigned;
using u32x2 = __attribute__((ext_vector_type(2))) unsigned;
typedef __bf16 bf16x2_t __attribute__((ext_vector_type(2)));
#define LAS __attribute__((address_space(3)))
#define DI __device__ __forceinline__

constexpr int S = 2048, D = 1024, NH = 32, FF = 3584;
constexpr float QSCALE = 0.17677669529663687f * 1.4426950408889634f;

constexpr size_t WS_MODP = 0;
constexpr size_t WS_MODF = 98304;
constexpr size_t WS_SYNC = 131072;
constexpr int SY_ST = 0, SY_GC = 65536, SY_HC = SY_GC + 256, SY_GP = SY_HC + 8192, SY_GY = SY_GP + 4 * 3584,
              SY_OC = SY_GY + 8192, SY_END = SY_OC + 128 * 16;
constexpr int SYNC_PER_BLOCK = (SY_END / 2 + 255) / 256;
static_assert(WS_SYNC + 256 * SYNC_PER_BLOCK * 16 <= (1u << 20), "sync area");
constexpr size_t WS_Q = 1u << 20;
constexpr size_t WS_K = WS_Q + (4u << 20);
constexpr size_t WS_V = WS_K + (4u << 20);
constexpr size_t WS_O = WS_V + (4u << 20);
constexpr size_t WS_X1 = WS_O + (4u << 20);
constexpr size_t WS_N = WS_X1 + (4u << 20);

#define PROF_BEGIN()
#define PROF_END(kid)
#define TS_DECL()
#define TS(i)
#define TS_END(kid, w)

#define TR(base, w, kt, i)
#define TR_FLUSH(base, w)
#define TR_INIT(base, w)

DI float bf2f(u16 v) { return __uint_as_float((unsigned)v << 16); }
DI float bflo(unsigned w) { return __uint_as_float(w << 16); }
DI float bfhi(unsigned w) { return __uint_as_float(w & 0xffff0000u); }
DI unsigned pack2(float lo, float hi) {
  f32x2 v = {lo, hi};
  bf16x2_t b = __builtin_convertvector(v, bf16x2_t);
  return __builtin_bit_cast(unsigned, b);
}
DI float rbf(float x) { return bflo(pack2(x, 0.f)); }
#define MFMA32(a, b, c) __builtin_amdgcn_mfma_f32_32x32x16_bf16((a), (b), (c), 0, 0, 0)
#define MFMA16(a, b, c) __builtin_amdgcn_mfma_f32_16x16x32_bf16((a), (b), (c), 0, 0, 0)
#define SBAR() __builtin_amdgcn_sched_barrier(0)
#define LDS_BARRIER() asm volatile("s_waitcnt lgkmcnt(0)\n\ts_barrier" ::: "memory")

DI float wave_sum(float v) {
#pragma unroll
  for (int o = 32; o > 0; o >>= 1) v += __shfl_xor(v, o);
  return v;
}
template <int CTRL>
DI float dpp_add(float v) {
  return v + __uint_as_float(__builtin_amdgcn_update_dpp(0u, __float_as_uint(v), CTRL, 0xf, 0xf, true));
}
DI float swap32_add(float a, float b) {
  auto r = __builtin_amdgcn_permlane32_swap(__float_as_uint(a), __float_as_uint(b), false, false);
  return __uint_as_float(r[0]) + __uint_as_float(r[1]);
}
DI float swap16_add(float a, float b) {
  auto r = __builtin_amdgcn_permlane16_swap(__float_as_uint(a), __float_as_uint(b), false, false);
  return __uint_as_float(r[0]) + __uint_as_float(r[1]);
}
DI float wsum(float v) {
  v = dpp_add<0xB1>(v);
  v = dpp_add<0x4E>(v);
  v = dpp_add<0x141>(v);
  v = dpp_add<0x140>(v);
  v = swap16_add(v, v);
  return swap32_add(v, v);
}
DI unsigned ln_pair(float lo, float hi) {
  const unsigned l = pack2(lo, 0.f), b = __float_as_uint(hi);
  const int t = max((int)(b & 0x7fffffffu) - (int)l + 0x8000, 0);
  return (b & 0x80000000u) | ((unsigned)t & 0x7fff0000u) | l;
}
DI void glds16(const void* sbase, unsigned voff, unsigned lds_dst) {
  unsigned keep;
  asm volatile("s_mov_b32 %0, m0\n\ts_mov_b32 m0, %3\n\ts_nop 0\n\tglobal_load_lds_dwordx4 %1, %2\n\ts_mov_b32 m0, %0"
               : "=&s"(keep)
               : "v"(voff), "s"(sbase), "s"(lds_dst)
               : "memory");
}
DI void st16(void* p, u32x4 v) {
  asm volatile("s_nop 0\n\tglobal_store_dwordx4 %0, %1, off sc1\n\ts_nop 1" ::"v"(p), "v"(v) : "memory");
}
DI bf16x8 lds_b128(unsigned addr) { return *(const LAS bf16x8*)(uintptr_t)addr; }
typedef short v4i16_t __attribute__((ext_vector_type(4)));
DI s16x4 lds_tr(unsigned addr) {
  return __builtin_bit_cast(s16x4, __builtin_amdgcn_ds_read_tr16_b64_v4i16((LAS v4i16_t*)(uintptr_t)addr));
}
DI bf16x8 cat8(s16x4 lo, s16x4 hi) { return __builtin_shufflevector(lo, hi, 0, 1, 2, 3, 4, 5, 6, 7); }

#define WPFPOL ""
__global__ __launch_bounds__(512) void k1_mod_ln(const u16* __restrict__ c, const u16* __restrict__ wmod,
                                                 const u16* __restrict__ x, float* __restrict__ modp,
                                                 u16* __restrict__ nbuf, u32x4* __restrict__ sync,
                                                 const u16* __restrict__ wqkv) {
  PROF_BEGIN();
  extern __shared__ __attribute__((aligned(16))) char smem[];
  float (*red)[64] = (float (*)[64])smem;
  const int t = threadIdx.x, lane = t & 63, wid = t >> 6, bid = blockIdx.x;
  if (t < SYNC_PER_BLOCK) st16(sync + bid * SYNC_PER_BLOCK + t, (u32x4){0u, 0u, 0u, 0u});
  if (bid < 192) {
    const int cg = bid % 96, kh = bid / 96;
    const int c8 = t & 7, rg = t >> 3;
    u32x4 w[8];
    float sc[8];
#pragma unroll
    for (int i = 0; i < 8; ++i) {
      const int k = kh * 512 + rg + 64 * i;
      w[i] = __builtin_nontemporal_load((const u32x4*)(wmod + (size_t)k * 6144 + cg * 64 + c8 * 8));
      const float cv = bf2f(c[k]);
      sc[i] = rbf(cv / (1.f + __expf(-cv)));
    }
    float acc[8] = {0.f, 0.f, 0.f, 0.f, 0.f, 0.f, 0.f, 0.f};
#pragma unroll
    for (int i = 0; i < 8; ++i)
#pragma unroll
      for (int j = 0; j < 4; ++j) {
        acc[2 * j] = fmaf(sc[i], bflo(w[i][j]), acc[2 * j]);
        acc[2 * j + 1] = fmaf(sc[i], bfhi(w[i][j]), acc[2 * j + 1]);
      }
#pragma unroll
    for (int j = 0; j < 8; ++j) {
      acc[j] += __shfl_xor(acc[j], 8);
      acc[j] += __shfl_xor(acc[j], 16);
      acc[j] += __shfl_xor(acc[j], 32);
    }
    if (lane < 8) {
#pragma unroll
      for (int j = 0; j < 8; ++j) red[wid][lane * 8 + j] = acc[j];
    }
    __syncthreads();
    if (t < 64) {
      float s = 0.f;
#pragma unroll
      for (int w8 = 0; w8 < 8; ++w8) s += red[w8][t];
      modp[kh * 6144 + cg * 64 + t] = s;
    }
  } else {
    const int r0 = (bid - 192) * 32 + wid * 4;
    u32x4 a[4], b[4];
#pragma unroll
    for (int i = 0; i < 4; ++i) {
      a[i] = *(const u32x4*)(x + (size_t)(r0 + i) * D + lane * 8);
      b[i] = *(const u32x4*)(x + (size_t)(r0 + i) * D + 512 + lane * 8);
    }
#pragma unroll
    for (int i = 0; i < 4; ++i) {
      float v[16];
#pragma unroll
      for (int j = 0; j < 4; ++j) {
        v[2 * j] = bflo(a[i][j]); v[2 * j + 1] = bfhi(a[i][j]);
        v[8 + 2 * j] = bflo(b[i][j]); v[8 + 2 * j + 1] = bfhi(b[i][j]);
      }
      float s = 0.f;
#pragma unroll
      for (int j = 0; j < 16; ++j) s += v[j];
      const float mean = wsum(s) * (1.f / D);
      float q = 0.f;
#pragma unroll
      for (int j = 0; j < 16; ++j) { v[j] -= mean; q = fmaf(v[j], v[j], q); }
      const float rstd = __builtin_amdgcn_rsqf(wsum(q) * (1.f / D) + 1e-5f);
      u32x4 o0, o1;
#pragma unroll
      for (int j = 0; j < 4; ++j) {
        o0[j] = ln_pair(v[2 * j] * rstd, v[2 * j + 1] * rstd);
        o1[j] = ln_pair(v[8 + 2 * j] * rstd, v[8 + 2 * j + 1] * rstd);
      }
      const int sw = ((wid * 4 + i) ^ (lane & 31)) * 16;
      *(u32x4*)(smem + lane * 512 + sw) = o0;
      *(u32x4*)(smem + (64 + lane) * 512 + sw) = o1;
    }
    __syncthreads();
#pragma unroll
    for (int i = 0; i < 8; ++i) {
      const int c = wid * 16 + i * 2 + (lane >> 5);
      const u32x4 v = *(const u32x4*)(smem + c * 512 + (((lane ^ c) & 31) * 16));
      st16(nbuf + (size_t)(bid - 192) * 32768 + (wid * 8 + i) * 512 + lane * 8, v);
    }
  }
  {
    const u16* wp = wqkv + (size_t)((bid >> 3) * 32 + wid * 4 + (lane >> 5)) * 3072 + ((bid & 7) >> 1) * 256 + (lane & 31) * 8;
    u32x4 pf[6];
#pragma unroll
    for (int i = 0; i < 6; ++i)
      asm volatile("global_load_dwordx4 %0, %1, off" WPFPOL : "=v"(pf[i]) : "v"(wp + (i & 1) * 2 * 3072 + (i >> 1) * 1024) : "memory");
    asm volatile("s_waitcnt vmcnt(0)" : "+v"(pf[0]), "+v"(pf[1]), "+v"(pf[2]), "+v"(pf[3]), "+v"(pf[4]), "+v"(pf[5])::"memory");
  }
  PROF_END(1);
}

constexpr int K2_NB = 6, K2_BSLOT = 24576, K2_DW = 3, K2_DN = 6;
constexpr int K2_LDS_GS = K2_NB * K2_BSLOT, K2_LDS_TR = K2_LDS_GS + 8192, K2_LDS = K2_LDS_TR + 2048;

#define GLOAD16(dst, voff, sbase) \
  asm volatile("global_load_dwordx4 %0, %1, %2" : "=v"(dst) : "v"(voff), "s"(sbase) : "memory")

#define GLOAD16NT(dst, voff, sbase) \
  asm volatile("global_load_dwordx4 %0, %1, %2 nt" : "=v"(dst) : "v"(voff), "s"(sbase) : "memory")

struct K2Sched { int wn[33]; int ww[17]; };
constexpr int k2_younger(const int* seq, int n, int id) {
  int c = 0;
  for (int i = n - 1; i >= 0 && seq[i] != id; --i) ++c;
  return c;
}
constexpr K2Sched k2_make() {
  K2Sched s{};
  int seq[128] = {};
  int n = 0;
  seq[n++] = 200; seq[n++] = 201;
  for (int i = 0; i < 4; ++i) {
    if (i == 0) seq[n++] = 0;
    if (i < K2_DW) { seq[n++] = 100 + 3 * i; seq[n++] = 101 + 3 * i; seq[n++] = 102 + 3 * i; }
    if (2 * i + 1 < K2_DN) seq[n++] = 2 * i + 1;
    if (2 * i + 2 < K2_DN) seq[n++] = 2 * i + 2;
  }
  s.wn[32] = k2_younger(seq, n, 201);
  s.ww[16] = k2_younger(seq, n, 102);
  seq[n++] = K2_DN;
  for (int p = 0; p < 32; ++p) {
    const int t = p >> 1, j = p & 1;
    if (p + 1 <= 31) s.wn[p] = k2_younger(seq, n, p + 1);
    if (j == 1 && t + 1 <= 15) s.ww[t] = k2_younger(seq, n, 100 + 3 * (t + 1) + 2);
    if (p + 1 + K2_DN <= 31) seq[n++] = p + 1 + K2_DN;
    if (t + K2_DW <= 15) {
      if (j == 0) { seq[n++] = 100 + 3 * (t + K2_DW); seq[n++] = 101 + 3 * (t + K2_DW); }
      else seq[n++] = 102 + 3 * (t + K2_DW);
    }
  }
  return s;
}
constexpr K2Sched K2S = k2_make();

#define K2_WPOL ""
template <int KT, int PC>
DI void k2_issue_b(const u16* wq, unsigned wvoff, unsigned lds_dst) {
  if constexpr (KT <= 15) {
    const u16* sb = wq + (size_t)KT * 64 * 3072 + (PC == 2 ? 2048 - 512 : 0);
    asm volatile("s_nop 0\n\tglobal_load_lds_dwordx4 %0, %1 offset:%2" K2_WPOL
                 :
                 : "v"(wvoff), "s"(sb), "i"(PC == 0 ? 0 : PC == 1 ? 2048 : 1024), "{m0}"(lds_dst + (KT % K2_NB) * K2_BSLOT)
                 : "memory");
  }
}
template <int F>
DI void k2_issue_n(const u16* nb, unsigned nvoff, u32x4& r) {
  if constexpr (F <= 31)
    asm volatile("global_load_dwordx4 %0, %1, %2 offset:%3" : "=v"(r) : "v"(nvoff), "s"(nb + (F >> 1) * 2048), "i"((F & 1) * 1024) : "memory");
}
struct K2GS { f32x4 g0, g1, s0, s1; };
DI K2GS k2_gs(unsigned gsa) {
  K2GS r;
  r.g0 = *(const LAS f32x4*)(uintptr_t)(gsa); r.g1 = *(const LAS f32x4*)(uintptr_t)(gsa + 16);
  r.s0 = *(const LAS f32x4*)(uintptr_t)(gsa + 4096); r.s1 = *(const LAS f32x4*)(uintptr_t)(gsa + 4096 + 16);
  return r;
}
template <int WAIT>
DI u32x4 k2_xform(u32x4& x, const K2GS& q) {
  asm volatile("s_waitcnt vmcnt(%1)" : "+v"(x) : "i"(WAIT) : "memory");
  unsigned t0, t1, t2, t3, o0, o1, o2, o3;
  asm volatile(
      "v_lshlrev_b32 %4, 16, %24\n\tv_lshlrev_b32 %5, 16, %25\n\tv_lshlrev_b32 %6, 16, %26\n\tv_lshlrev_b32 %7, 16, %27\n\t"
      "v_fma_f32 %0, %24, %9, %17\n\tv_fma_f32 %1, %25, %11, %19\n\tv_fma_f32 %2, %26, %13, %21\n\tv_fma_f32 %3, %27, %15, %23\n\t"
      "v_fma_f32 %4, %4, %8, %16\n\tv_fma_f32 %5, %5, %10, %18\n\tv_fma_f32 %6, %6, %12, %20\n\tv_fma_f32 %7, %7, %14, %22\n\t"
      "v_cvt_pk_bf16_f32 %0, %4, %0\n\tv_cvt_pk_bf16_f32 %1, %5, %1\n\tv_cvt_pk_bf16_f32 %2, %6, %2\n\tv_cvt_pk_bf16_f32 %3, %7, %3\n\t"
      "s_nop 1"
      : "=&v"(o0), "=&v"(o1), "=&v"(o2), "=&v"(o3), "=&v"(t0), "=&v"(t1), "=&v"(t2), "=&v"(t3)
      : "v"(q.g0[0]), "v"(q.g0[1]), "v"(q.g0[2]), "v"(q.g0[3]), "v"(q.g1[0]), "v"(q.g1[1]), "v"(q.g1[2]), "v"(q.g1[3]),
        "v"(q.s0[0]), "v"(q.s0[1]), "v"(q.s0[2]), "v"(q.s0[3]), "v"(q.s1[0]), "v"(q.s1[1]), "v"(q.s1[2]), "v"(q.s1[3]),
        "v"(x[0]), "v"(x[1]), "v"(x[2]), "v"(x[3]));
  return (u32x4){o0, o1, o2, o3};
}

struct K2State {
  f32x16 acc[2][3];
  bf16x8 fb[2][6];
  u32x4 nr[K2_DN];
  u32x4 hf[2];
  K2GS gs;
};
template <int P>
DI bf16x8 k2_ldb(const unsigned (&bb)[2][2], int hd, int sg) {
  constexpr int slot = (P >> 1) % K2_NB;
  const unsigned a = bb[hd][slot / 3] + (slot % 3) * K2_BSLOT + (P & 1) * 6144 + (sg == 0 ? 0 : sg == 1 ? 2048 : 1024);
  return cat8(lds_tr(a), lds_tr(a + 512));
}
template <int P>
DI void k2_phase(K2State& st, const unsigned (&bb)[2][2], unsigned gsa, const u16* wq, unsigned wvoff, unsigned wdst,
                 const u16* nb, unsigned nvoff, unsigned lds0) {
  constexpr int C = P & 1, N = C ^ 1, T = P >> 1, J = P & 1;
  constexpr bool NEXT = P < 31;
  const bf16x8 hf = __builtin_bit_cast(bf16x8, st.hf[C]);
#define LDB2(I0) do { if constexpr (NEXT) { st.fb[N][I0] = k2_ldb<P + 1>(bb, (I0) / 3, (I0) % 3); \
                                            st.fb[N][I0 + 1] = k2_ldb<P + 1>(bb, (I0 + 1) / 3, (I0 + 1) % 3); } } while (0)
#define MM(I) do { SBAR();                                                                                   \
    if ((I) % 3 == 2) st.acc[(I) / 3][2] = MFMA32(hf, st.fb[C][I], st.acc[(I) / 3][2]);                      \
    else st.acc[(I) / 3][(I) % 3] = MFMA32(st.fb[C][I], hf, st.acc[(I) / 3][(I) % 3]);                       \
    SBAR(); } while (0)
  if constexpr (J == 0) TR(lds0 + K2_LDS_TR, 0, T, 0);
  MM(0);
  if constexpr (NEXT) st.hf[N] = k2_xform<K2S.wn[P]>(st.nr[(P + 1) % K2_DN], st.gs);
  MM(1);
  if constexpr (J == 1 && NEXT) {
    TR(lds0 + K2_LDS_TR, 0, T, 1);
    asm volatile("s_waitcnt vmcnt(%0)" ::"i"(K2S.ww[T]) : "memory");
    TR(lds0 + K2_LDS_TR, 0, T, 2);
    asm volatile("s_barrier" ::: "memory");
    TR(lds0 + K2_LDS_TR, 0, T, 3);
  }
  LDB2(0);
  k2_issue_n<P + 1 + K2_DN>(nb, nvoff, st.nr[(P + 1) % K2_DN]);
  MM(2);
  LDB2(2);
  k2_issue_b<T + K2_DW, J == 0 ? 0 : 2>(wq, wvoff, wdst);
  MM(3);
  LDB2(4);
  if constexpr (J == 0) k2_issue_b<T + K2_DW, 1>(wq, wvoff, wdst);
  MM(4);
  if constexpr (P < 30) st.gs = k2_gs(gsa + ((P + 2) >> 1) * 256 + (P & 1) * 64);
  MM(5);
#undef LDB2
#undef MM
}
template <int P>
DI void k2_phases(K2State& st, const unsigned (&bb)[2][2], unsigned gsa, const u16* wq, unsigned wvoff, unsigned wdst,
                  const u16* nb, unsigned nvoff, unsigned lds0) {
  k2_phase<P>(st, bb, gsa, wq, wvoff, wdst, nb, nvoff, lds0);
  if constexpr (P < 31) k2_phases<P + 1>(st, bb, gsa, wq, wvoff, wdst, nb, nvoff, lds0);
}

__global__ __launch_bounds__(512) void k2_qkv(const u16* __restrict__ nf, const u16* __restrict__ wqkv,
                                              const float* __restrict__ modp,
                                              u16* __restrict__ qf, u16* __restrict__ kf, u16* __restrict__ vf,
                                              u16* __restrict__ modf) {
  PROF_BEGIN();
  extern __shared__ __attribute__((aligned(16))) char smem[];
  const unsigned lds0 = (unsigned)(uintptr_t)smem;
  const int tid = threadIdx.x, lane = tid & 63;
  const int wid = __builtin_amdgcn_readfirstlane(tid >> 6);
  const int bid = blockIdx.x, xcd = bid & 7, jj = bid >> 3;
  const int mt = (xcd & 1) * 8 + (jj & 7), nt = (xcd >> 1) * 4 + (jj >> 3);
  const int wm = wid & 3, kh = wid >> 2;
  TR_INIT(lds0 + K2_LDS_TR, 0);
  TR(lds0 + K2_LDS_TR, 0, 16, 0);
  K2State st;
  f32x4 mp0, mp1;
  {
    const unsigned vo = (tid & 255) * 16;
    const float* mb = modp + (kh == 0 ? 1024 : 0);
    GLOAD16(mp0, vo, mb);
    GLOAD16(mp1, vo, mb + 6144);
  }
  const int kr = lane >> 3, pos = lane & 7;
  const unsigned wvoff = (unsigned)(((wid * 8 + kr) * 3072 + (((pos >> 2) ^ ((kr >> 1) & 1)) * 32) + (pos & 3) * 8) * 2);
  const u16* wq = wqkv + nt * 64;
  const unsigned wdst = lds0 + wid * 3072;
  const u16* nb = nf + (size_t)(mt * 4 + wm) * 32768 + kh * 1024;
  const unsigned nvoff = lane * 16;
#define WT(KT) do { if constexpr (KT < K2_DW) { k2_issue_b<KT, 0>(wq, wvoff, wdst); k2_issue_b<KT, 1>(wq, wvoff, wdst); \
                                                k2_issue_b<KT, 2>(wq, wvoff, wdst); } } while (0)
#define NT(F) do { if constexpr (F < K2_DN) k2_issue_n<F>(nb, nvoff, st.nr[(F) % K2_DN]); } while (0)
  NT(0); WT(0); NT(1); NT(2); WT(1); NT(3); NT(4); WT(2); NT(5); NT(6); WT(3); NT(7); NT(8);
#undef WT
#undef NT
  TR(lds0 + K2_LDS_TR, 0, 16, 1);
  asm volatile("s_waitcnt vmcnt(%2)" : "+v"(mp0), "+v"(mp1) : "i"(K2S.wn[32]) : "memory");
  {
    f32x4 v;
#pragma unroll
    for (int j = 0; j < 4; ++j) {
      v[j] = rbf(mp0[j] + mp1[j]);
      if (kh == 0) v[j] = rbf(1.f + v[j]);
    }
    *(LAS f32x4*)(uintptr_t)(lds0 + K2_LDS_GS + tid * 16) = v;
  }
  const int h = lane >> 5, q4 = (lane & 15) >> 2, p4 = lane & 3, ch = (lane >> 4) & 1;
  unsigned bb[2][2];
#pragma unroll
  for (int hd = 0; hd < 2; ++hd) {
    bb[hd][0] = lds0 + kh * 12288 + h * 3072 + q4 * 128 + (((hd ^ kh) ^ ((q4 >> 1) & 1)) * 64) + ch * 32 + p4 * 8;
    bb[hd][1] = bb[hd][0] + 3 * K2_BSLOT;
    asm volatile("" : "+v"(bb[hd][0]), "+v"(bb[hd][1]));
  }
  unsigned gsa = lds0 + K2_LDS_GS + (32 * kh + 8 * h) * 4;
  asm volatile("" : "+v"(gsa));
#pragma unroll
  for (int a = 0; a < 2; ++a)
#pragma unroll
    for (int b = 0; b < 3; ++b) st.acc[a][b] = (f32x16){};
  asm volatile("s_waitcnt vmcnt(%0) lgkmcnt(0)\n\ts_barrier" ::"i"(K2S.ww[16]) : "memory");
  TR(lds0 + K2_LDS_TR, 0, 16, 2);
  st.gs = k2_gs(gsa);
#pragma unroll
  for (int i = 0; i < 6; ++i) st.fb[0][i] = k2_ldb<0>(bb, i / 3, i % 3);
  st.hf[0] = k2_xform<K2S.ww[16]>(st.nr[0], st.gs);
  k2_issue_n<K2_DN>(nb, nvoff, st.nr[0]);
  st.gs = k2_gs(gsa + 64);
  k2_phases<0>(st, bb, gsa, wq, wvoff, wdst, nb, nvoff, lds0);
  LDS_BARRIER();
#pragma unroll
  for (int sg = 0; sg < 3; ++sg)
#pragma unroll
    for (int g = 0; g < 4; ++g)
      *(LAS f32x4*)(uintptr_t)(lds0 + (wid ^ 4) * 12288 + sg * 4096 + g * 1024 + lane * 16) =
          (f32x4){st.acc[1][sg][4 * g], st.acc[1][sg][4 * g + 1], st.acc[1][sg][4 * g + 2], st.acc[1][sg][4 * g + 3]};
  LDS_BARRIER();
  const size_t off = ((size_t)((nt * 2 + kh) * 64 + mt * 4 + wm) * 2) * 512 + lane * 8;
#pragma unroll
  for (int sg = 0; sg < 3; ++sg) {
    u16* dst = (sg == 0 ? qf : sg == 1 ? kf : vf) + off;
    const float sc = sg == 0 ? QSCALE : 1.f;
#pragma unroll
    for (int s = 0; s < 2; ++s) {
      const f32x4 a = *(const LAS f32x4*)(uintptr_t)(lds0 + wid * 12288 + sg * 4096 + s * 2048 + lane * 16);
      const f32x4 b = *(const LAS f32x4*)(uintptr_t)(lds0 + wid * 12288 + sg * 4096 + s * 2048 + 1024 + lane * 16);
      u32x4 o;
#pragma unroll
      for (int j = 0; j < 4; ++j) {
        const float lo = st.acc[0][sg][8 * s + 2 * j] + (j < 2 ? a[2 * j] : b[2 * j - 4]);
        const float hi = st.acc[0][sg][8 * s + 2 * j + 1] + (j < 2 ? a[2 * j + 1] : b[2 * j - 3]);
        o[j] = pack2(lo * sc, hi * sc);
      }
      st16(dst + s * 512, o);
    }
  }
  if (bid >= 2 && bid < 6 && tid < 256) {
    const f32x4 p0 = *(const f32x4*)(modp + bid * 1024 + tid * 4), p1 = *(const f32x4*)(modp + 6144 + bid * 1024 + tid * 4);
    float v[4];
#pragma unroll
    for (int j = 0; j < 4; ++j) { v[j] = rbf(p0[j] + p1[j]); if (bid == 4) v[j] += 1.f; }
    *(u32x2*)(modf + bid * 1024 + tid * 4) = (u32x2){pack2(v[0], v[1]), pack2(v[2], v[3])};
  }
  PROF_END(2);
}

#define GAS __attribute__((address_space(1)))
typedef GAS unsigned long long gu64;
typedef GAS unsigned gu32;
#define RLX_AGENT __ATOMIC_RELAXED, __HIP_MEMORY_SCOPE_AGENT
constexpr unsigned SPIN_CAP = 1u << 18;
DI unsigned long long mk64(unsigned lo, unsigned hi) { return ((unsigned long long)hi << 32) | lo; }
template <int CTRL>
DI unsigned dpp_maxu(unsigned v) {
  const unsigned o = __builtin_amdgcn_update_dpp(0u, v, CTRL, 0xf, 0xf, true);
  return v > o ? v : o;
}
DI unsigned rowmaxu(unsigned v) {
  v = dpp_maxu<0xB1>(v); v = dpp_maxu<0x4E>(v); v = dpp_maxu<0x141>(v); return dpp_maxu<0x140>(v);
}
DI float sum8(float v) { return dpp_add<0x141>(dpp_add<0x4E>(dpp_add<0xB1>(v))); }
DI float sumrows(float v) { v = swap16_add(v, v); return swap32_add(v, v); }
DI f32x2 up2(unsigned w) { return (f32x2){bflo(w), bfhi(w)}; }
DI unsigned okey(float v) {
  const unsigned raw = __float_as_uint(v);
  return v == v ? (raw ^ ((unsigned)((int)raw >> 31) | 0x80000000u)) : 0u;
}
#define GLOADC(dst, voff, sbase, off) \
  asm volatile("global_load_dwordx4 %0, %1, %2 offset:%3 sc1" : "=v"(dst) : "v"(voff), "s"(sbase), "i"(off) : "memory")
template <class T> DI T ldsr(unsigned a) { return *(const LAS T*)(uintptr_t)a; }
template <class T> DI void ldsw(unsigned a, T v) { *(LAS T*)(uintptr_t)a = v; }
DI float ex2(float x) { return __builtin_amdgcn_exp2f(x); }
DI float xhalf_max(float v) {
  auto rr = __builtin_amdgcn_permlane32_swap(__float_as_uint(v), __float_as_uint(v), false, false);
  return fmaxf(__uint_as_float(rr[0]), __uint_as_float(rr[1]));
}
DI void lds_arrive(unsigned a) {
  asm volatile("s_waitcnt lgkmcnt(0)" ::: "memory");
  if ((threadIdx.x & 63) == 0) __hip_atomic_fetch_add((LAS unsigned*)(uintptr_t)a, 1u, __ATOMIC_RELAXED, __HIP_MEMORY_SCOPE_WORKGROUP);
}
DI void lds_await(unsigned a, unsigned n) {
  while (__hip_atomic_load((LAS unsigned*)(uintptr_t)a, __ATOMIC_RELAXED, __HIP_MEMORY_SCOPE_WORKGROUP) < n) __builtin_amdgcn_s_sleep(2);
  asm volatile("" ::: "memory");
}

#define K4_R 8
#define HD0 14
#define HD0R 8
#define HD1 16
#define HD2 HD1
#define HD3 HD1
#define HD4 HD1
constexpr int K3_CNT = 0;
constexpr int K4_VEC = 64;
constexpr int K4_ROW = K4_VEC + 1280;
constexpr int K4_BEST = K4_ROW + 2048;
constexpr int K4_FIN = K4_BEST + 64;
constexpr int K4_H = K4_FIN + 512;
constexpr int K4_A = K4_H + 1024;
constexpr int K4_RED = K4_A + 1024;
constexpr int K4_RED2 = K4_RED + 2048;
constexpr int K4_IMG = 10240;
constexpr int K3_LDS = 163840, K3_PART = 4096 + 256, K3_R = K3_LDS - 12 * K3_PART;
static_assert(K4_RED2 + 1024 <= K4_IMG && K4_IMG + 12 * 8192 <= K3_R && K4_IMG + 131072 <= K3_LDS, "LDS map");

__global__ __launch_bounds__(512) void k3_tail(const u16* __restrict__ qf, const u16* __restrict__ kf,
                                               const u16* __restrict__ vf, u16* of, const u16* __restrict__ wo,
                                               const u16* __restrict__ x, const float* __restrict__ modp,
                                               const u16* __restrict__ wr, const u16* __restrict__ wg,
                                               const u16* __restrict__ wu, const u16* __restrict__ wd,
                                               u16* __restrict__ out, unsigned long long* sync) {
  PROF_BEGIN();
  extern __shared__ __attribute__((aligned(16))) char smem[];
  const unsigned lds0 = (unsigned)(uintptr_t)smem;
  const int tid = threadIdx.x, lane = tid & 63;
  const int wid = __builtin_amdgcn_readfirstlane(tid >> 6);
  const int b = blockIdx.x, xcd = b & 7, jj = b >> 3;
  const int qg = wid >> 2, kq = wid & 3;
  const int mt = (xcd >> 1) * 4 + (jj & 3), nt = (xcd & 1) * 8 + (jj >> 2);
  const bool has_gu = b < 224;
  const int cg = b % 56, ks = has_gu ? b / 56 : 0;
  const int dcg = b & 15, dks = b >> 4;
  const int kr = lane >> 3, pos = lane & 7;
  u32x4 wgu[16], wdn[7];
  TS_DECL(); TS(0);
  {
    const int head = xcd * 4 + (jj >> 3), qb = jj & 7;
    const int qt0 = qb * 8 + qg * 4;
    const u16* kh = kf + (size_t)head * 65536;
    const u16* vh = vf + (size_t)head * 65536;
    const u16* kp = kh + kq * 16384 + lane * 8;
    const u16* vp = vh + kq * 16384 + lane * 8;
#define LDF(ptr) (*(const bf16x8*)(ptr))
    bf16x8 ka0 = LDF(kp), ka1 = LDF(kp + 512);
    bf16x8 q[4][2];
#pragma unroll
    for (int i = 0; i < 4; ++i) {
      const u16* qp = qf + ((size_t)(head * 64 + qt0 + ((i + kq) & 3)) * 2) * 512 + lane * 8;
      q[i][0] = LDF(qp); q[i][1] = LDF(qp + 512);
    }
    bf16x8 va0 = LDF(vp), va1 = LDF(vp + 512);
    bf16x8 kb0 = LDF(kp + 1024), kb1 = LDF(kp + 1024 + 512);
    bf16x8 vb0, vb1 = va1;
    if (tid == 0) ldsw(lds0 + K3_CNT, (u32x2){0u, 0u});
    LDS_BARRIER();
    bf16x8 ones;
    {
      const short o = ((((lane & 15) >> 2) & 1) == ((lane >> 4) & 1)) ? (short)0x3F80 : (short)0;
#pragma unroll
      for (int j = 0; j < 8; ++j) ones[j] = o;
    }
    const f32x16 zero = (f32x16){};
    f32x16 o[4] = {zero, zero, zero, zero};
    f32x4 l[4] = {(f32x4){}, (f32x4){}, (f32x4){}, (f32x4){}};
    f32x16 sA = MFMA32(ka0, q[0][0], zero), sB;
    sA = MFMA32(ka1, q[0][1], sA);
    u32x4 p0, p1 = (u32x4){0u, 0u, 0u, 0u};
#define PK(S_, i) pack2(S_[i], S_[i + 1])
#define EX(S_, i) S_[i] = ex2(S_[i])
#define PIN(x_) asm volatile("" : "+v"(x_))
#define UNIT(SC, SN, KN0, KN1, N, C, V0C, P, V1P, LD_A, LD_B)                               \
  do {                                                                                     \
    SN = MFMA32(KN0, q[N][0], zero); SBAR();                                               \
    EX(SC, 0); EX(SC, 1); EX(SC, 2); p0[0] = PK(SC, 0); PIN(p0); SBAR();                   \
    o[P] = MFMA32(V1P, __builtin_bit_cast(bf16x8, p1), o[P]); SBAR();                      \
    LD_A;                                                                                  \
    EX(SC, 3); EX(SC, 4); EX(SC, 5); p0[1] = PK(SC, 2); PIN(p0); SBAR();                   \
    l[P] = MFMA16(ones, __builtin_bit_cast(bf16x8, p1), l[P]); SBAR();                     \
    EX(SC, 6); EX(SC, 7); p0[2] = PK(SC, 4); p0[3] = PK(SC, 6); PIN(p0); SBAR();           \
    SN = MFMA32(KN1, q[N][1], SN); SBAR();                                                 \
    LD_B;                                                                                  \
    EX(SC, 8); EX(SC, 9); EX(SC, 10); p1[0] = PK(SC, 8); PIN(p1); SBAR();                  \
    o[C] = MFMA32(V0C, __builtin_bit_cast(bf16x8, p0), o[C]); SBAR();                      \
    EX(SC, 11); EX(SC, 12); EX(SC, 13); p1[1] = PK(SC, 10); PIN(p1); SBAR();               \
    l[C] = MFMA16(ones, __builtin_bit_cast(bf16x8, p0), l[C]); SBAR();                     \
    EX(SC, 14); EX(SC, 15); p1[2] = PK(SC, 12); p1[3] = PK(SC, 14); PIN(p1); SBAR();       \
  } while (0)
#pragma unroll 1
    for (int j = 0; j < 16; j += 2) {
      const u16* kn = kp + ((j + 2) & 15) * 1024;
      const u16* vn = vp + (j + 1) * 1024;
      const u16* vn2 = vp + ((j + 2) & 15) * 1024;
      UNIT(sA, sB, ka0, ka1, 1, 0, va0, 3, vb1, vb0 = LDF(vn), vb1 = LDF(vn + 512));
      UNIT(sB, sA, ka0, ka1, 2, 1, va0, 0, va1, (void)0, (void)0);
      UNIT(sA, sB, ka0, ka1, 3, 2, va0, 1, va1, (void)0, (void)0);
      UNIT(sB, sA, kb0, kb1, 0, 3, va0, 2, va1, ka0 = LDF(kn), ka1 = LDF(kn + 512));
      UNIT(sA, sB, kb0, kb1, 1, 0, vb0, 3, va1, va0 = LDF(vn2), va1 = LDF(vn2 + 512));
      UNIT(sB, sA, kb0, kb1, 2, 1, vb0, 0, vb1, (void)0, (void)0);
      UNIT(sA, sB, kb0, kb1, 3, 2, vb0, 1, vb1, (void)0, (void)0);
      UNIT(sB, sA, ka0, ka1, 0, 3, vb0, 2, vb1, kb0 = LDF(kn + 1024), kb1 = LDF(kn + 1024 + 512));
    }
    o[3] = MFMA32(vb1, __builtin_bit_cast(bf16x8, p1), o[3]);
    l[3] = MFMA16(ones, __builtin_bit_cast(bf16x8, p1), l[3]);
#undef UNIT
    TS(1);
    if (qg == 1) lds_await(lds0 + K3_CNT + 4, 4u);
#pragma unroll
    for (int i = 1; i < 4; ++i) {
      const unsigned wa = lds0 + K3_R + (((i + kq) & 3) * 3 + i - 1) * K3_PART;
#pragma unroll
      for (int g = 0; g < 4; ++g)
        ldsw(wa + g * 1024 + lane * 16, (f32x4){o[i][4 * g], o[i][4 * g + 1], o[i][4 * g + 2], o[i][4 * g + 3]});
      ldsw(wa + 4096 + lane * 4, l[i][0]);
    }
    lds_arrive(lds0 + K3_CNT);
    lds_await(lds0 + K3_CNT, qg == 0 ? 4u : 8u);
    f32x16 oo = o[0];
    float lsum = l[0][0];
#pragma unroll
    for (int i = 0; i < 3; ++i) {
      const unsigned ra = lds0 + K3_R + (kq * 3 + i) * K3_PART;
#pragma unroll
      for (int g = 0; g < 4; ++g) {
        const f32x4 v = ldsr<f32x4>(ra + g * 1024 + lane * 16);
        oo[4 * g] += v[0]; oo[4 * g + 1] += v[1]; oo[4 * g + 2] += v[2]; oo[4 * g + 3] += v[3];
      }
      lsum += ldsr<float>(ra + 4096 + lane * 4);
    }
    PIN(oo); PIN(lsum);
    lds_arrive(lds0 + K3_CNT + 4);
    float rl = 1.f / lsum;
    bool bad = !(lsum < 1.0e30f && lsum > 1.0e-30f);
#pragma unroll
    for (int r = 0; r < 16; ++r) {
      oo[r] *= rl;
      bad |= !(fabsf(oo[r]) < 3.0e38f);
    }
    if (__builtin_expect(__any(bad), 0)) {
      const bf16x8 q0 = q[0][0], q1 = q[0][1];
      float m = -INFINITY, ls = 0.f;
      oo = zero;
      for (int t = 0; t < 64; ++t) {
        const bf16x8 a0 = LDF(kh + (size_t)t * 1024 + lane * 8), a1 = LDF(kh + (size_t)t * 1024 + 512 + lane * 8);
        const bf16x8 b0 = LDF(vh + (size_t)t * 1024 + lane * 8), b1 = LDF(vh + (size_t)t * 1024 + 512 + lane * 8);
        f32x16 s = MFMA32(a0, q0, zero);
        s = MFMA32(a1, q1, s);
        float tm = s[0];
#pragma unroll
        for (int r = 1; r < 16; ++r) tm = fmaxf(tm, s[r]);
        tm = xhalf_max(tm);
        const float mn = fmaxf(m, tm);
        const float alpha = ex2(m - mn);
        m = mn;
        u32x4 pa, pb;
        float ps = 0.f;
#pragma unroll
        for (int j = 0; j < 4; ++j) {
          pa[j] = pack2(ex2(s[2 * j] - mn), ex2(s[2 * j + 1] - mn));
          pb[j] = pack2(ex2(s[8 + 2 * j] - mn), ex2(s[8 + 2 * j + 1] - mn));
          ps += bflo(pa[j]) + bfhi(pa[j]) + bflo(pb[j]) + bfhi(pb[j]);
        }
        ps += __shfl_xor(ps, 32);
        ls = ls * alpha + ps;
#pragma unroll
        for (int r = 0; r < 16; ++r) oo[r] *= alpha;
        oo = MFMA32(b0, __builtin_bit_cast(bf16x8, pa), oo);
        oo = MFMA32(b1, __builtin_bit_cast(bf16x8, pb), oo);
      }
      rl = 1.f / ls;
#pragma unroll
      for (int r = 0; r < 16; ++r) oo[r] *= rl;
    }
    u16* op = of + ((size_t)((qt0 + kq) * 32 + head) * 2) * 512 + lane * 8;
#pragma unroll
    for (int s = 0; s < 2; ++s) {
      u32x4 w;
#pragma unroll
      for (int j = 0; j < 4; ++j) w[j] = pack2(oo[8 * s + 2 * j], oo[8 * s + 2 * j + 1]);
      st16(op + s * 512, w);
    }
    asm volatile("s_waitcnt vmcnt(0)" ::: "memory");
    if (lane == 0) __hip_atomic_fetch_add((gu32*)(sync + SY_OC + ((qt0 + kq) * 2 + (head >> 4)) * 16), 1u, RLX_AGENT);
    TS(2);
#undef PK
#undef EX
#undef LDF
  }
  const unsigned wvoff = (unsigned)(((kq * 8 + kr) * D + (((pos >> 2) ^ ((kr >> 1) & 1)) * 32) + (pos & 3) * 8) * 2);
  const u16* wb = wo + nt * 64;
#define WDMA(P, HF)                                                                                  \
  asm volatile("s_nop 0\n\tglobal_load_lds_dwordx4 %0, %1"                                           \
               :                                                                                     \
               : "v"(wvoff), "s"(wb + (size_t)(((P) & 1) * 512 + ((P) >> 1) * 64 + (HF) * 32) * D),  \
                 "{m0}"(lds0 + K4_IMG + (P) * 8192 + (HF) * 4096 + kq * 1024)                        \
               : "memory")
  if (qg == 0) {
    if (wid == 0) {
      const float* mp = modp + 2048 + nt * 64 + lane;
      float v[4];
#pragma unroll
      for (int i = 0; i < 4; ++i) v[i] = rbf(mp[i * 1024] + mp[6144 + i * 1024]);
      const float w = bf2f(wr[nt * 64 + lane]);
      const float g2 = rbf(1.f + v[2]), gw = g2 * w;
      const unsigned va = lds0 + K4_VEC + lane * 4;
      ldsw(va, v[0]); ldsw(va + 256, g2); ldsw(va + 512, v[1]); ldsw(va + 768, v[3]); ldsw(va + 1024, gw);
      const float gs = wsum(gw), cs = wsum(v[1] * w);
      if (lane == 0) __hip_atomic_store((gu64*)sync + SY_GC + mt * 16 + nt, mk64(__float_as_uint(gs) | 1u, __float_as_uint(cs)), RLX_AGENT);
    }
#define SH_D 6
#define PACE() asm volatile("s_waitcnt vmcnt(%0)" ::"i"(SH_D - 1) : "memory")
    const int rg = tid >> 3;
    const unsigned vgu = (unsigned)((rg * FF + pos * 8) * 2), vdn = (unsigned)((rg * D + pos * 8) * 2);
    const size_t gbase = (size_t)(ks * 256) * FF + cg * 64;
#pragma unroll
    for (int i = 0; i < 16; ++i) { PACE(); GLOAD16NT(wgu[i], vgu, (i < 8 ? wg : wu) + gbase + (size_t)(32 * (i & 7)) * FF); }
#pragma unroll
    for (int i = 0; i < 7; ++i) { PACE(); GLOAD16NT(wdn[i], vdn, wd + (size_t)(dks * 224 + 32 * i) * D + dcg * 64); }
#pragma unroll
    for (int p = 0; p < 24; ++p) { PACE(); WDMA(p >> 1, p & 1); }
    {
      u32x4 pf[12];
#pragma unroll
      for (int p = 24; p < 32; ++p) { PACE(); GLOAD16(pf[p - 24], wvoff, wb + (size_t)(((p >> 1) & 1) * 512 + (p >> 2) * 64 + (p & 1) * 32) * D); }
#pragma unroll
      for (int i = 0; i < 4; ++i) { PACE(); GLOAD16(pf[8 + i], vdn, x + (size_t)(mt * 128 + 32 * i) * D + nt * 64); }
      asm volatile("s_waitcnt vmcnt(0)" : "+v"(pf[0]), "+v"(pf[1]), "+v"(pf[2]), "+v"(pf[3]), "+v"(pf[4]), "+v"(pf[5]), "+v"(pf[6]),
                   "+v"(pf[7]), "+v"(pf[8]), "+v"(pf[9]), "+v"(pf[10]), "+v"(pf[11])::"memory");
    }
    asm volatile("" : "+v"(wgu[0]), "+v"(wgu[1]), "+v"(wgu[2]), "+v"(wgu[3]), "+v"(wgu[4]), "+v"(wgu[5]), "+v"(wgu[6]), "+v"(wgu[7]),
                      "+v"(wgu[8]), "+v"(wgu[9]), "+v"(wgu[10]), "+v"(wgu[11]), "+v"(wgu[12]), "+v"(wgu[13]), "+v"(wgu[14]), "+v"(wgu[15]),
                      "+v"(wdn[0]), "+v"(wdn[1]), "+v"(wdn[2]), "+v"(wdn[3]), "+v"(wdn[4]), "+v"(wdn[5]), "+v"(wdn[6])::"memory");
    TS(3);
    lds_await(lds0 + K3_CNT + 4, 8u);
#pragma unroll
    for (int p = 24; p < 32; ++p) WDMA(p >> 1, p & 1);
    asm volatile("s_waitcnt vmcnt(0)" ::: "memory");
  } else if (wid == 4) {
    const unsigned vo = (unsigned)((SY_OC + (mt * 8 + (lane & 7)) * 16) * 8);
    __builtin_amdgcn_s_sleep(HD0);
    _Pragma("nounroll") for (unsigned spin = 0; spin < SPIN_CAP; ++spin) {
      unsigned c;
      asm volatile("global_load_dword %0, %1, %2 sc1\n\ts_waitcnt vmcnt(0)" : "=v"(c) : "v"(vo), "s"(sync) : "memory");
      if (__all(c >= 16u)) break;
      __builtin_amdgcn_s_sleep(HD0R);
    }
    TS(3);
  }
#undef WDMA
  LDS_BARRIER();
  TS(4);
  const int erow = tid >> 3, ec = tid & 7;
  float xv[2][8];
  {
    const int kh = qg, wt = kq;
    const u16* ob = of + ((size_t)((mt * 4 + wt) * 32 + kh * 16) * 2) * 512;
    const unsigned ovoff = lane * 16;
    u32x4 ofr[K4_R], xr[2];
#define OLOAD(F) GLOADC(ofr[(F) % K4_R], ovoff, ob + ((F) >> 2) * 2048, ((F) & 3) * 1024)
#pragma unroll
    for (int f = 0; f < K4_R; ++f) OLOAD(f);
    const int h = lane >> 5, q4 = (lane & 15) >> 2, p4 = lane & 3, ch = (lane >> 4) & 1;
    unsigned bb[2][2];
#pragma unroll
    for (int c = 0; c < 2; ++c) {
      bb[c][0] = lds0 + K4_IMG + kh * 8192 + (4 * h + q4) * 128 + ((c ^ ((q4 >> 1) & 1)) * 64) + ch * 32 + p4 * 8;
      bb[c][1] = bb[c][0] + 65536;
      asm volatile("" : "+v"(bb[c][0]), "+v"(bb[c][1]));
    }
#define WFRAG(C, F) cat8(lds_tr(bb[C][(F) >> 4] + (((F) >> 2) & 3) * 16384 + ((F) & 3) * 2048), \
                         lds_tr(bb[C][(F) >> 4] + (((F) >> 2) & 3) * 16384 + ((F) & 3) * 2048 + 1024))
    f32x16 acc0 = (f32x16){}, acc1 = (f32x16){};
    bf16x8 w0 = WFRAG(0, 0), w1 = WFRAG(1, 0);
#pragma unroll
    for (int f = 0; f < 32; ++f) {
      const int last = f - 1 + K4_R < 31 ? f - 1 + K4_R : 31;
      bf16x8 n0 = w0, n1 = w1;
      if (f < 31) { n0 = WFRAG(0, f + 1); n1 = WFRAG(1, f + 1); }
      asm volatile("s_waitcnt vmcnt(%1)" : "+v"(ofr[f % K4_R]) : "i"(last - f + (f - 1 + K4_R >= 31 ? 2 : 0)) : "memory");
      const bf16x8 a = __builtin_bit_cast(bf16x8, ofr[f % K4_R]);
      acc0 = MFMA32(w0, a, acc0);
      acc1 = MFMA32(w1, a, acc1);
      SBAR();
      if (f + K4_R <= 31) OLOAD(f + K4_R);
      if (f + K4_R == 31) {
        const unsigned xo = (unsigned)((erow * D + ec * 8) * 2);
        const u16* xb0 = x + (size_t)(mt * 128) * D + nt * 64;
        GLOAD16(xr[0], xo, xb0);
        GLOAD16(xr[1], xo, xb0 + (size_t)64 * D);
      }
      w0 = n0; w1 = n1;
    }
#undef OLOAD
#undef WFRAG
    LDS_BARRIER();
    TS(5);
    {
      const unsigned wa = lds0 + K4_IMG + kh * 34816 + (wt * 32 + (lane & 31)) * 272 + h * 16;
#pragma unroll
      for (int g = 0; g < 4; ++g) {
        ldsw(wa + g * 32, (f32x4){acc0[4 * g], acc0[4 * g + 1], acc0[4 * g + 2], acc0[4 * g + 3]});
        ldsw(wa + 128 + g * 32, (f32x4){acc1[4 * g], acc1[4 * g + 1], acc1[4 * g + 2], acc1[4 * g + 3]});
      }
    }
    asm volatile("s_waitcnt vmcnt(0) lgkmcnt(0)\n\ts_barrier" : "+v"(xr[0]), "+v"(xr[1])::"memory");
    const unsigned va = lds0 + K4_VEC + ec * 32;
    const f32x4 ga0 = ldsr<f32x4>(va), ga1 = ldsr<f32x4>(va + 16);
    const f32x4 gw0 = ldsr<f32x4>(va + 1024), gw1 = ldsr<f32x4>(va + 1024 + 16);
    float sm[2], sq[2], sd[2];
#pragma unroll
    for (int i = 0; i < 2; ++i) {
      const unsigned ra = lds0 + K4_IMG + (erow + 64 * i) * 272 + ec * 32;
      const f32x4 a0 = ldsr<f32x4>(ra), a1 = ldsr<f32x4>(ra + 16);
      const f32x4 b0 = ldsr<f32x4>(ra + 34816), b1 = ldsr<f32x4>(ra + 34816 + 16);
#pragma unroll
      for (int j = 0; j < 4; ++j) {
        const float lo = (j < 2 ? a0[2 * j] + b0[2 * j] : a1[2 * j - 4] + b1[2 * j - 4]);
        const float hi = (j < 2 ? a0[2 * j + 1] + b0[2 * j + 1] : a1[2 * j - 3] + b1[2 * j - 3]);
        const float gl = j < 2 ? ga0[2 * j] : ga1[2 * j - 4], gh = j < 2 ? ga0[2 * j + 1] : ga1[2 * j - 3];
        const unsigned w = pack2(fmaf(gl, lo, bflo(xr[i][j])), fmaf(gh, hi, bfhi(xr[i][j])));
        xv[i][2 * j] = bflo(w); xv[i][2 * j + 1] = bfhi(w);
      }
      sm[i] = sum8(((xv[i][0] + xv[i][1]) + (xv[i][2] + xv[i][3])) + ((xv[i][4] + xv[i][5]) + (xv[i][6] + xv[i][7]))) * (1.f / 64);
      float q = 0.f, d = 0.f;
#pragma unroll
      for (int j = 0; j < 8; ++j) {
        const float e = xv[i][j] - sm[i];
        q = fmaf(e, e, q);
        d = fmaf(e, j < 4 ? gw0[j] : gw1[j - 4], d);
      }
      sq[i] = sum8(q); sd[i] = sum8(d);
    }
    if (ec < 2) {
      const u32x4 rec = {__float_as_uint(ec ? sm[1] : sm[0]) | 1u, __float_as_uint(ec ? sq[1] : sq[0]),
                         __float_as_uint(ec ? sd[1] : sd[0]) | 1u, 0u};
      st16(sync + SY_ST + ((size_t)(mt * 16 + nt) * 128 + erow + 64 * ec) * 2, rec);
    }
  }
  TS(6);
  {
    const int r16 = lane & 15, sg = lane >> 4;
    const unsigned vo = (unsigned)(sg * 8192 + (wid * 16 + r16) * 16), vg = (unsigned)((SY_GC + mt * 16) * 8 + sg * 32);
    const unsigned long long* sb = sync + SY_ST + (size_t)mt * 4096;
    u32x4 c[4], g[2];
    __builtin_amdgcn_s_sleep(HD1);
    _Pragma("nounroll") for (unsigned spin = 0; spin < SPIN_CAP; ++spin) {
      GLOADC(c[0], vo, sb, 0); GLOADC(c[1], vo, sb, 2048); GLOADC(c[2], vo, sb + 512, 0); GLOADC(c[3], vo, sb + 512, 2048);
      GLOADC(g[0], vg, sync, 0); GLOADC(g[1], vg, sync, 16);
      asm volatile("s_waitcnt vmcnt(0)" : "+v"(c[0]), "+v"(c[1]), "+v"(c[2]), "+v"(c[3]), "+v"(g[0]), "+v"(g[1])::"memory");
      const unsigned m = (c[0][0] & c[0][2]) & (c[1][0] & c[1][2]) & (c[2][0] & c[2][2]) & (c[3][0] & c[3][2]) &
                         (g[0][0] & g[0][2]) & (g[1][0] & g[1][2]);
      if (__all(m & 1u)) break;
      __builtin_amdgcn_s_sleep(4);
    }
    TS(7);
#define F(x_) __uint_as_float(x_)
    const float mean = sumrows((F(c[0][0]) + F(c[1][0])) + (F(c[2][0]) + F(c[3][0]))) * (1.f / 16);
    float q = 0.f, d = 0.f;
#pragma unroll
    for (int i = 0; i < 4; ++i) {
      const float e = F(c[i][0]) - mean;
      q += fmaf(64.f * e, e, F(c[i][1]));
      d += fmaf(e, F(g[i >> 1][(i & 1) * 2]), F(c[i][2]));
    }
    const float cs = sumrows((F(g[0][1]) + F(g[0][3])) + (F(g[1][1]) + F(g[1][3])));
#undef F
    const float rstd = __builtin_amdgcn_rsqf(sumrows(q) * (1.f / D) + 1e-5f);
    const float rr = fmaf(rstd, sumrows(d), cs);
    const unsigned key = okey(rr), kmax = rowmaxu(key);
    const unsigned rbest = ~rowmaxu(key == kmax ? ~(unsigned)(wid * 16 + r16) : 0u);
    if (lane < 16) ldsw(lds0 + K4_ROW + (wid * 16 + r16) * 16, (f32x4){mean, rstd, rr, 0.f});
    if (lane == 0) ldsw(lds0 + K4_BEST + wid * 8, (u32x2){kmax, rbest});
  }
  LDS_BARRIER();
  TS(8);
  unsigned bkey, brow;
  {
    const u32x4 e0 = ldsr<u32x4>(lds0 + K4_BEST), e1 = ldsr<u32x4>(lds0 + K4_BEST + 16);
    const u32x4 e2 = ldsr<u32x4>(lds0 + K4_BEST + 32), e3 = ldsr<u32x4>(lds0 + K4_BEST + 48);
    const unsigned kk[8] = {e0[0], e0[2], e1[0], e1[2], e2[0], e2[2], e3[0], e3[2]};
    const unsigned rs[8] = {e0[1], e0[3], e1[1], e1[3], e2[1], e2[3], e3[1], e3[3]};
    bkey = kk[0]; brow = rs[0];
#pragma unroll
    for (int w = 1; w < 8; ++w) { const bool tk = kk[w] > bkey; bkey = tk ? kk[w] : bkey; brow = tk ? rs[w] : brow; }
  }
  {
    const unsigned va = lds0 + K4_VEC + ec * 32;
    const f32x4 g0 = ldsr<f32x4>(va + 256), g1 = ldsr<f32x4>(va + 256 + 16);
    const f32x4 s0 = ldsr<f32x4>(va + 512), s1 = ldsr<f32x4>(va + 512 + 16);
    const f32x4 b0 = ldsr<f32x4>(va + 768), b1 = ldsr<f32x4>(va + 768 + 16);
    u32x4 hw[2];
#pragma unroll
    for (int i = 0; i < 2; ++i) {
      const f32x2 ms = ldsr<f32x2>(lds0 + K4_ROW + (erow + 64 * i) * 16);
#pragma unroll
      for (int j = 0; j < 4; ++j) {
        const float n0 = (xv[i][2 * j] - ms[0]) * ms[1], n1 = (xv[i][2 * j + 1] - ms[0]) * ms[1];
        hw[i][j] = pack2(fmaf(n0, j < 2 ? g0[2 * j] : g1[2 * j - 4], j < 2 ? s0[2 * j] : s1[2 * j - 4]),
                         fmaf(n1, j < 2 ? g0[2 * j + 1] : g1[2 * j - 3], j < 2 ? s0[2 * j + 1] : s1[2 * j - 3]));
      }
    }
    if (erow == (int)(brow & 63u)) {
      const bool hi = brow >= 64u;
      const u32x4 hc = hi ? hw[1] : hw[0];
      unsigned long long* hp = sync + SY_HC + mt * 512 + nt * 32 + ec * 4;
      st16(hp, (u32x4){hc[0], bkey | 1u, hc[1], bkey | 1u});
      st16(hp + 2, (u32x4){hc[2], bkey | 1u, hc[3], bkey | 1u});
      float xc[8];
#pragma unroll
      for (int j = 0; j < 8; ++j) xc[j] = hi ? xv[1][j] : xv[0][j];
      ldsw(lds0 + K4_FIN + ec * 32, (f32x4){xc[0], xc[1], xc[2], xc[3]});
      ldsw(lds0 + K4_FIN + ec * 32 + 16, (f32x4){xc[4], xc[5], xc[6], xc[7]});
      ldsw(lds0 + K4_FIN + 256 + ec * 32, (f32x4){bflo(hc[0]), bfhi(hc[0]), bflo(hc[1]), bfhi(hc[1])});
      ldsw(lds0 + K4_FIN + 256 + ec * 32 + 16, (f32x4){bflo(hc[2]), bfhi(hc[2]), bflo(hc[3]), bfhi(hc[3])});
    }
#pragma unroll
    for (int i = 0; i < 2; ++i) {
      const int row = erow + 64 * i;
      u32x4 o;
#pragma unroll
      for (int j = 0; j < 4; ++j)
        o[j] = pack2(fmaf(j < 2 ? b0[2 * j] : b1[2 * j - 4], bflo(hw[i][j]), xv[i][2 * j]),
                     fmaf(j < 2 ? b0[2 * j + 1] : b1[2 * j - 3], bfhi(hw[i][j]), xv[i][2 * j + 1]));
      if (row != (int)brow) st16(out + (size_t)(mt * 128 + row) * D + nt * 64 + ec * 8, o);
    }
  }
  TS(9);
  if (wid > 4) {
    asm volatile("s_waitcnt lgkmcnt(0)" ::: "memory");
    PROF_END(3);
    return;
  }
  gu64* gp = (gu64*)sync + SY_GP;
  gu64* gy = (gu64*)sync + SY_GY;
  if (wid == 4) {
    int win;
    {
      const unsigned vo = (unsigned)(ks * 1024 + lane * 16);
      const unsigned long long* hb = sync + SY_HC;
      u32x4 c[16];
      __builtin_amdgcn_s_sleep(HD2);
      _Pragma("nounroll") for (unsigned spin = 0; spin < SPIN_CAP; ++spin) {
#pragma unroll
        for (int i = 0; i < 16; ++i) GLOADC(c[i], vo, hb + i * 512, 0);
        asm volatile("s_waitcnt vmcnt(0)"
                     : "+v"(c[0]), "+v"(c[1]), "+v"(c[2]), "+v"(c[3]), "+v"(c[4]), "+v"(c[5]), "+v"(c[6]), "+v"(c[7]),
                       "+v"(c[8]), "+v"(c[9]), "+v"(c[10]), "+v"(c[11]), "+v"(c[12]), "+v"(c[13]), "+v"(c[14]), "+v"(c[15])::"memory");
        unsigned m = 1u;
#pragma unroll
        for (int i = 0; i < 16; ++i) m &= c[i][1] & c[i][3];
        if (__all(m & 1u)) break;
        __builtin_amdgcn_s_sleep(4);
      }
      TS(10);
      unsigned k0 = c[0][1], h0 = c[0][0], h1 = c[0][2];
      win = 0;
#pragma unroll
      for (int i = 1; i < 16; ++i) {
        const bool tk = c[i][1] > k0;
        k0 = tk ? c[i][1] : k0; h0 = tk ? c[i][0] : h0; h1 = tk ? c[i][2] : h1; win = tk ? i : win;
      }
      ldsw(lds0 + K4_H + lane * 16, (f32x4){bflo(h0), bfhi(h0), bflo(h1), bfhi(h1)});
      win = __builtin_amdgcn_readfirstlane(win);
    }
    LDS_BARRIER();
    const f32x2 fx = ldsr<f32x2>(lds0 + K4_FIN + (lane & 31) * 8), fg = ldsr<f32x2>(lds0 + K4_VEC + 768 + (lane & 31) * 8);
    const f32x2 fh = ldsr<f32x2>(lds0 + K4_FIN + 256 + (lane & 31) * 8);
    const float fsig = rbf(1.f / (1.f + __expf(-rbf(ldsr<float>(lds0 + K4_ROW + brow * 16 + 8)))));
    unsigned* orow = (unsigned*)(out + (size_t)(mt * 128 + brow) * D + nt * 64 + (lane & 31) * 2);
    if (win != mt && lane < 32) *orow = pack2(fmaf(fg[0], fh[0], fx[0]), fmaf(fg[1], fh[1], fx[1]));
    LDS_BARRIER();
    if (has_gu) {
      float sg = 0.f, su = 0.f;
#pragma unroll
      for (int w = 0; w < 4; ++w) {
        sg += ldsr<float>(lds0 + K4_RED + w * 512 + lane * 4);
        su += ldsr<float>(lds0 + K4_RED + w * 512 + 256 + lane * 4);
      }
      __hip_atomic_store(gp + ks * FF + cg * 64 + lane, mk64(__float_as_uint(sg) | 1u, __float_as_uint(su)), RLX_AGENT);
    }
    TS(11);
    {
      const unsigned v0 = (unsigned)(dks * 1792 + lane * 16), v1 = (unsigned)(dks * 1792 + (lane < 48 ? lane + 64 : 111) * 16);
      const unsigned long long* gb = sync + SY_GP;
      u32x4 c[8];
      __builtin_amdgcn_s_sleep(HD3);
      _Pragma("nounroll") for (unsigned spin = 0; spin < SPIN_CAP; ++spin) {
#pragma unroll
        for (int i = 0; i < 4; ++i) { GLOADC(c[i], v0, gb + i * FF, 0); GLOADC(c[4 + i], v1, gb + i * FF, 0); }
        asm volatile("s_waitcnt vmcnt(0)"
                     : "+v"(c[0]), "+v"(c[1]), "+v"(c[2]), "+v"(c[3]), "+v"(c[4]), "+v"(c[5]), "+v"(c[6]), "+v"(c[7])::"memory");
        unsigned m = 1u;
#pragma unroll
        for (int i = 0; i < 8; ++i) m &= c[i][0] & c[i][2];
        if (__all(m & 1u)) break;
        __builtin_amdgcn_s_sleep(4);
      }
      TS(12);
#pragma unroll
      for (int r = 0; r < 2; ++r) {
        float a[2];
#pragma unroll
        for (int e = 0; e < 2; ++e) {
          float sg = 0.f, su = 0.f;
#pragma unroll
          for (int i = 0; i < 4; ++i) { sg += __uint_as_float(c[r * 4 + i][2 * e]); su += __uint_as_float(c[r * 4 + i][2 * e + 1]); }
          sg = rbf(sg); su = rbf(su);
          a[e] = rbf(rbf(sg / (1.f + __expf(-sg))) * su);
        }
        ldsw(lds0 + K4_A + (r * 64 + lane) * 8, (f32x2){a[0], a[1]});
      }
    }
    LDS_BARRIER();
    asm volatile("s_barrier" ::: "memory");
    if (lane < 32) {
      f32x2 y = (f32x2){0.f, 0.f};
#pragma unroll
      for (int w = 0; w < 4; ++w) y += ldsr<f32x2>(lds0 + K4_RED2 + w * 256 + lane * 8);
      __hip_atomic_store(gy + dks * 512 + dcg * 32 + lane, mk64(__float_as_uint(y[0]) | 1u, __float_as_uint(y[1])), RLX_AGENT);
    }
    TS(13);
    if (win == mt) {
      unsigned long long c[8];
      __builtin_amdgcn_s_sleep(HD4);
      _Pragma("nounroll") for (unsigned spin = 0; spin < SPIN_CAP; ++spin) {
        bool ok = true;
#pragma unroll
        for (int i = 0; i < 8; ++i) {
          c[i] = __hip_atomic_load(gy + ((lane >> 5) * 8 + i) * 512 + nt * 32 + (lane & 31), RLX_AGENT);
          ok &= (c[i] & 1ull) != 0;
        }
        if (__all(ok)) break;
        __builtin_amdgcn_s_sleep(4);
      }
      float y0 = 0.f, y1 = 0.f;
#pragma unroll
      for (int i = 0; i < 8; ++i) { y0 += __uint_as_float((unsigned)c[i]); y1 += __uint_as_float((unsigned)(c[i] >> 32)); }
      y0 = swap32_add(y0, y0); y1 = swap32_add(y1, y1);
      y0 = rbf(rbf(y0) * fsig); y1 = rbf(rbf(y1) * fsig);
      if (lane < 32) *orow = pack2(fmaf(fg[0], rbf(fh[0] + y0), fx[0]), fmaf(fg[1], rbf(fh[1] + y1), fx[1]));
    }
    TS(14);
    TS_END(3, 4);
    PROF_END(3);
    return;
  }
  {
    f32x2 wf[16][4];
#pragma unroll
    for (int i = 0; i < 16; ++i)
#pragma unroll
      for (int j = 0; j < 4; ++j) { wf[i][j] = up2(wgu[i][j]); asm volatile("" : "+v"(wf[i][j])); }
    LDS_BARRIER();
    f32x2 ag[4], au[4];
#pragma unroll
    for (int i = 0; i < 8; ++i) {
      const float h = ldsr<float>(lds0 + K4_H + (erow + 32 * i) * 4);
#pragma unroll
      for (int j = 0; j < 4; ++j) {
        ag[j] = i == 0 ? wf[0][j] * h : wf[i][j] * h + ag[j];
        au[j] = i == 0 ? wf[8][j] * h : wf[8 + i][j] * h + au[j];
      }
    }
    float v8[8], v4[4];
#pragma unroll
    for (int j = 0; j < 8; ++j) v8[j] = swap32_add(ag[j >> 1][j & 1], au[j >> 1][j & 1]);
#pragma unroll
    for (int j = 0; j < 4; ++j) v4[j] = dpp_add<0x128>(swap16_add(v8[j], v8[4 + j]));
    if ((lane & 8) == 0)
      ldsw(lds0 + K4_RED + wid * 512 + ((lane >> 5) * 64 + (lane & 7) * 8 + ((lane >> 4) & 1) * 4) * 4,
           (f32x4){v4[0], v4[1], v4[2], v4[3]});
  }
  LDS_BARRIER();
  {
    f32x2 wdf[7][4];
#pragma unroll
    for (int i = 0; i < 7; ++i)
#pragma unroll
      for (int j = 0; j < 4; ++j) { wdf[i][j] = up2(wdn[i][j]); asm volatile("" : "+v"(wdf[i][j])); }
    asm volatile("s_barrier" ::: "memory");
    f32x2 a[4];
#pragma unroll
    for (int i = 0; i < 7; ++i) {
      const float h = ldsr<float>(lds0 + K4_A + (erow + 32 * i) * 4);
#pragma unroll
      for (int j = 0; j < 4; ++j) a[j] = i == 0 ? wdf[0][j] * h : wdf[i][j] * h + a[j];
    }
    float v4[4], v2[2];
#pragma unroll
    for (int j = 0; j < 4; ++j) v4[j] = swap32_add(a[j >> 1][j & 1], a[2 + (j >> 1)][j & 1]);
#pragma unroll
    for (int j = 0; j < 2; ++j) v2[j] = dpp_add<0x128>(swap16_add(v4[j], v4[2 + j]));
    if ((lane & 8) == 0)
      ldsw(lds0 + K4_RED2 + wid * 256 + ((lane & 7) * 8 + (lane >> 5) * 4 + ((lane >> 4) & 1) * 2) * 4, (f32x2){v2[0], v2[1]});
  }
  LDS_BARRIER();
  TS_END(4, 0);
  PROF_END(3);
}

extern "C" void kernel_launch(void* const* d_in, const int* in_sizes, int n_in, void* d_out, int out_size, void* d_ws,
                              size_t ws_size, hipStream_t stream) {
  const u16* x = (const u16*)d_in[0];
  const u16* wqkv = (const u16*)d_in[1];
  const u16* wo = (const u16*)d_in[2];
  const u16* wr = (const u16*)d_in[4];
  const u16* wg = (const u16*)d_in[5];
  const u16* wu = (const u16*)d_in[6];
  const u16* wd = (const u16*)d_in[7];
  const u16* c = (const u16*)d_in[8];
  const u16* wmod = (const u16*)d_in[9];
  char* ws = (char*)d_ws;
  float* modp = (float*)(ws + WS_MODP);
  u16* nbuf = (u16*)(ws + WS_N);
  u16* modf = (u16*)(ws + WS_MODF);
  u16* qf = (u16*)(ws + WS_Q);
  u16* kf = (u16*)(ws + WS_K);
  u16* vf = (u16*)(ws + WS_V);
  u16* of = (u16*)(ws + WS_O);
  u16* out = (u16*)d_out;
  (void)hipFuncSetAttribute((const void*)k2_qkv, hipFuncAttributeMaxDynamicSharedMemorySize, K2_LDS);
  (void)hipFuncSetAttribute((const void*)k3_tail, hipFuncAttributeMaxDynamicSharedMemorySize, K3_LDS);
  k1_mod_ln<<<256, 512, 65536, stream>>>(c, wmod, x, modp, nbuf, (u32x4*)(ws + WS_SYNC), wqkv);
  k2_qkv<<<256, 512, K2_LDS, stream>>>(nbuf, wqkv, modp, qf, kf, vf, modf);
  k3_tail<<<256, 512, K3_LDS, stream>>>(qf, kf, vf, of, wo, x, modp, wr, wg, wu, wd, out, (unsigned long long*)(ws + WS_SYNC));
}
```

```cpp
#include <hip/hip_runtime.h>
#include <hip/hip_bf16.h>
#include <stdint.h>

typedef unsigned short u16;
using bf16x8 = __attribute__((ext_vector_type(8))) short;
using s16x4 = __attribute__((ext_vector_type(4))) short;
using f32x16 = __attribute__((ext_vector_type(16))) float;
using f32x4 = __attribute__((ext_vector_type(4))) float;
using f32x2 = __attribute__((ext_vector_type(2))) float;
using u32x4 = __attribute__((ext_vector_type(4))) unsigned;
using u32x2 = __attribute__((ext_vector_type(2))) unsigned;
typedef __bf16 bf16x2_t __attribute__((ext_vector_type(2)));
#define LAS __attribute__((address_space(3)))
#define DI __device__ __forceinline__

constexpr int S = 2048, D = 1024, NH = 32, FF = 3584;
constexpr float QSCALE = 0.17677669529663687f * 1.4426950408889634f;

constexpr size_t WS_MODP = 0;
constexpr size_t WS_MODF = 98304;
constexpr size_t WS_SYNC = 131072;
constexpr int SY_ST = 0, SY_GC = 65536, SY_HC = SY_GC + 256, SY_GP = SY_HC + 8192, SY_GY = SY_GP + 4 * 3584,
              SY_OC = SY_GY + 8192, SY_KV = SY_OC + 64 * 16, SY_END = SY_KV + 128 * 16;
constexpr int SYNC_PER_BLOCK = (SY_END / 2 + 255) / 256;
static_assert(WS_SYNC + 256 * SYNC_PER_BLOCK * 16 <= (1u << 20), "sync area");
constexpr size_t WS_Q = 1u << 20;
constexpr size_t WS_K = WS_Q + (4u << 20);
constexpr size_t WS_V = WS_K + (4u << 20);
constexpr size_t WS_O = WS_V + (4u << 20);
constexpr size_t WS_X1 = WS_O + (4u << 20);
constexpr size_t WS_N = WS_X1 + (4u << 20);

#define PROF_BEGIN()
#define PROF_END(kid)
#define TS_DECL()
#define TS(i)
#define TS_END(kid, w)

#define TR(base, w, kt, i)
#define TR_FLUSH(base, w)
#define TR_INIT(base, w)

DI float bf2f(u16 v) { return __uint_as_float((unsigned)v << 16); }
DI float bflo(unsigned w) { return __uint_as_float(w << 16); }
DI float bfhi(unsigned w) { return __uint_as_float(w & 0xffff0000u); }
DI unsigned pack2(float lo, float hi) {
  f32x2 v = {lo, hi};
  bf16x2_t b = __builtin_convertvector(v, bf16x2_t);
  return __builtin_bit_cast(unsigned, b);
}
DI float rbf(float x) { return bflo(pack2(x, 0.f)); }
#define MFMA32(a, b, c) __builtin_amdgcn_mfma_f32_32x32x16_bf16((a), (b), (c), 0, 0, 0)
#define MFMA16(a, b, c) __builtin_amdgcn_mfma_f32_16x16x32_bf16((a), (b), (c), 0, 0, 0)
#define SBAR() __builtin_amdgcn_sched_barrier(0)
#define LDS_BARRIER() asm volatile("s_waitcnt lgkmcnt(0)\n\ts_barrier" ::: "memory")

DI float wave_sum(float v) {
#pragma unroll
  for (int o = 32; o > 0; o >>= 1) v += __shfl_xor(v, o);
  return v;
}
template <int CTRL>
DI float dpp_add(float v) {
  return v + __uint_as_float(__builtin_amdgcn_update_dpp(0u, __float_as_uint(v), CTRL, 0xf, 0xf, true));
}
DI float swap32_add(float a, float b) {
  auto r = __builtin_amdgcn_permlane32_swap(__float_as_uint(a), __float_as_uint(b), false, false);
  return __uint_as_float(r[0]) + __uint_as_float(r[1]);
}
DI float swap16_add(float a, float b) {
  auto r = __builtin_amdgcn_permlane16_swap(__float_as_uint(a), __float_as_uint(b), false, false);
  return __uint_as_float(r[0]) + __uint_as_float(r[1]);
}
DI float wsum(float v) {
  v = dpp_add<0xB1>(v);
  v = dpp_add<0x4E>(v);
  v = dpp_add<0x141>(v);
  v = dpp_add<0x140>(v);
  v = swap16_add(v, v);
  return swap32_add(v, v);
}
DI unsigned ln_pair(float lo, float hi) {
  const unsigned l = pack2(lo, 0.f), b = __float_as_uint(hi);
  const int t = max((int)(b & 0x7fffffffu) - (int)l + 0x8000, 0);
  return (b & 0x80000000u) | ((unsigned)t & 0x7fff0000u) | l;
}
DI void glds16(const void* sbase, unsigned voff, unsigned lds_dst) {
  unsigned keep;
  asm volatile("s_mov_b32 %0, m0\n\ts_mov_b32 m0, %3\n\ts_nop 0\n\tglobal_load_lds_dwordx4 %1, %2\n\ts_mov_b32 m0, %0"
               : "=&s"(keep)
               : "v"(voff), "s"(sbase), "s"(lds_dst)
               : "memory");
}
DI void st16(void* p, u32x4 v) {
  asm volatile("s_nop 0\n\tglobal_store_dwordx4 %0, %1, off sc1\n\ts_nop 1" ::"v"(p), "v"(v) : "memory");
}
DI bf16x8 lds_b128(unsigned addr) { return *(const LAS bf16x8*)(uintptr_t)addr; }
typedef short v4i16_t __attribute__((ext_vector_type(4)));
DI s16x4 lds_tr(unsigned addr) {
  return __builtin_bit_cast(s16x4, __builtin_amdgcn_ds_read_tr16_b64_v4i16((LAS v4i16_t*)(uintptr_t)addr));
}
DI bf16x8 cat8(s16x4 lo, s16x4 hi) { return __builtin_shufflevector(lo, hi, 0, 1, 2, 3, 4, 5, 6, 7); }

#define WPFPOL ""
__global__ __launch_bounds__(512) void k1_mod_ln(const u16* __restrict__ c, const u16* __restrict__ wmod,
                                                 const u16* __restrict__ x, float* __restrict__ modp,
                                                 u16* __restrict__ nbuf, u32x4* __restrict__ sync,
                                                 const u16* __restrict__ wqkv) {
  PROF_BEGIN();
  extern __shared__ __attribute__((aligned(16))) char smem[];
  float (*red)[64] = (float (*)[64])smem;
  const int t = threadIdx.x, lane = t & 63, wid = t >> 6, bid = blockIdx.x;
  if (t < SYNC_PER_BLOCK) st16(sync + bid * SYNC_PER_BLOCK + t, (u32x4){0u, 0u, 0u, 0u});
  if (bid < 192) {
    const int cg = bid % 96, kh = bid / 96;
    const int c8 = t & 7, rg = t >> 3;
    u32x4 w[8];
    float sc[8];
#pragma unroll
    for (int i = 0; i < 8; ++i) {
      const int k = kh * 512 + rg + 64 * i;
      w[i] = __builtin_nontemporal_load((const u32x4*)(wmod + (size_t)k * 6144 + cg * 64 + c8 * 8));
      const float cv = bf2f(c[k]);
      sc[i] = rbf(cv / (1.f + __expf(-cv)));
    }
    float acc[8] = {0.f, 0.f, 0.f, 0.f, 0.f, 0.f, 0.f, 0.f};
#pragma unroll
    for (int i = 0; i < 8; ++i)
#pragma unroll
      for (int j = 0; j < 4; ++j) {
        acc[2 * j] = fmaf(sc[i], bflo(w[i][j]), acc[2 * j]);
        acc[2 * j + 1] = fmaf(sc[i], bfhi(w[i][j]), acc[2 * j + 1]);
      }
#pragma unroll
    for (int j = 0; j < 8; ++j) {
      acc[j] += __shfl_xor(acc[j], 8);
      acc[j] += __shfl_xor(acc[j], 16);
      acc[j] += __shfl_xor(acc[j], 32);
    }
    if (lane < 8) {
#pragma unroll
      for (int j = 0; j < 8; ++j) red[wid][lane * 8 + j] = acc[j];
    }
    __syncthreads();
    if (t < 64) {
      float s = 0.f;
#pragma unroll
      for (int w8 = 0; w8 < 8; ++w8) s += red[w8][t];
      modp[kh * 6144 + cg * 64 + t] = s;
    }
  } else {
    const int r0 = (bid - 192) * 32 + wid * 4;
    u32x4 a[4], b[4];
#pragma unroll
    for (int i = 0; i < 4; ++i) {
      a[i] = *(const u32x4*)(x + (size_t)(r0 + i) * D + lane * 8);
      b[i] = *(const u32x4*)(x + (size_t)(r0 + i) * D + 512 + lane * 8);
    }
#pragma unroll
    for (int i = 0; i < 4; ++i) {
      float v[16];
#pragma unroll
      for (int j = 0; j < 4; ++j) {
        v[2 * j] = bflo(a[i][j]); v[2 * j + 1] = bfhi(a[i][j]);
        v[8 + 2 * j] = bflo(b[i][j]); v[8 + 2 * j + 1] = bfhi(b[i][j]);
      }
      float s = 0.f;
#pragma unroll
      for (int j = 0; j < 16; ++j) s += v[j];
      const float mean = wsum(s) * (1.f / D);
      float q = 0.f;
#pragma unroll
      for (int j = 0; j < 16; ++j) { v[j] -= mean; q = fmaf(v[j], v[j], q); }
      const float rstd = __builtin_amdgcn_rsqf(wsum(q) * (1.f / D) + 1e-5f);
      u32x4 o0, o1;
#pragma unroll
      for (int j = 0; j < 4; ++j) {
        o0[j] = ln_pair(v[2 * j] * rstd, v[2 * j + 1] * rstd);
        o1[j] = ln_pair(v[8 + 2 * j] * rstd, v[8 + 2 * j + 1] * rstd);
      }
      const int sw = ((wid * 4 + i) ^ (lane & 31)) * 16;
      *(u32x4*)(smem + lane * 512 + sw) = o0;
      *(u32x4*)(smem + (64 + lane) * 512 + sw) = o1;
    }
    __syncthreads();
#pragma unroll
    for (int i = 0; i < 8; ++i) {
      const int c = wid * 16 + i * 2 + (lane >> 5);
      const u32x4 v = *(const u32x4*)(smem + c * 512 + (((lane ^ c) & 31) * 16));
      st16(nbuf + (size_t)(bid - 192) * 32768 + (wid * 8 + i) * 512 + lane * 8, v);
    }
  }
  {
    const u16* wp = wqkv + (size_t)((bid >> 3) * 32 + wid * 4 + (lane >> 5)) * 3072 + ((bid & 7) >> 1) * 256 + (lane & 31) * 8;
    u32x4 pf[6];
#pragma unroll
    for (int i = 0; i < 6; ++i)
      asm volatile("global_load_dwordx4 %0, %1, off" WPFPOL : "=v"(pf[i]) : "v"(wp + (i & 1) * 2 * 3072 + (i >> 1) * 1024) : "memory");
    asm volatile("s_waitcnt vmcnt(0)" : "+v"(pf[0]), "+v"(pf[1]), "+v"(pf[2]), "+v"(pf[3]), "+v"(pf[4]), "+v"(pf[5])::"memory");
  }
  PROF_END(1);
}

constexpr int K2_NB = 6, K2_BSLOT = 24576, K2_DW = 3, K2_DN = 6;
constexpr int K2_LDS_GS = K2_NB * K2_BSLOT, K2_LDS_TR = K2_LDS_GS + 8192, K2_LDS = K2_LDS_TR + 2048;

#define GLOAD16(dst, voff, sbase) \
  asm volatile("global_load_dwordx4 %0, %1, %2" : "=v"(dst) : "v"(voff), "s"(sbase) : "memory")

#define GLOAD16NT(dst, voff, sbase) \
  asm volatile("global_load_dwordx4 %0, %1, %2 nt" : "=v"(dst) : "v"(voff), "s"(sbase) : "memory")

#define K2_TP0 6
#define K2_TN 0
#define K2_TS 2
constexpr int k2_touch(int p) { return p >= K2_TP0 && (p - K2_TP0) % K2_TS == 0 && (p - K2_TP0) / K2_TS < K2_TN ? (p - K2_TP0) / K2_TS : -1; }
struct K2Sched { int wn[33]; int ww[17]; };
constexpr int k2_younger(const int* seq, int n, int id) {
  int c = 0;
  for (int i = n - 1; i >= 0 && seq[i] != id; --i) ++c;
  return c;
}
constexpr K2Sched k2_make() {
  K2Sched s{};
  int seq[128] = {};
  int n = 0;
  seq[n++] = 200; seq[n++] = 201;
  for (int i = 0; i < 4; ++i) {
    if (i == 0) seq[n++] = 0;
    if (i < K2_DW) { seq[n++] = 100 + 3 * i; seq[n++] = 101 + 3 * i; seq[n++] = 102 + 3 * i; }
    if (2 * i + 1 < K2_DN) seq[n++] = 2 * i + 1;
    if (2 * i + 2 < K2_DN) seq[n++] = 2 * i + 2;
  }
  s.wn[32] = k2_younger(seq, n, 201);
  s.ww[16] = k2_younger(seq, n, 102);
  seq[n++] = K2_DN;
  for (int p = 0; p < 32; ++p) {
    const int t = p >> 1, j = p & 1;
    if (p + 1 <= 31) s.wn[p] = k2_younger(seq, n, p + 1);
    if (j == 1 && t + 1 <= 15) s.ww[t] = k2_younger(seq, n, 100 + 3 * (t + 1) + 2);
    if (p + 1 + K2_DN <= 31) seq[n++] = p + 1 + K2_DN;
    if (t + K2_DW <= 15) {
      if (j == 0) { seq[n++] = 100 + 3 * (t + K2_DW); seq[n++] = 101 + 3 * (t + K2_DW); }
      else seq[n++] = 102 + 3 * (t + K2_DW);
    }
    if (k2_touch(p) >= 0) seq[n++] = 300 + p;
  }
  return s;
}
constexpr K2Sched K2S = k2_make();

#define K2_WPOL ""
template <int KT, int PC>
DI void k2_issue_b(const u16* wq, unsigned wvoff, unsigned lds_dst) {
  if constexpr (KT <= 15) {
    const u16* sb = wq + (size_t)KT * 64 * 3072 + (PC == 2 ? 2048 - 512 : 0);
    asm volatile("s_nop 0\n\tglobal_load_lds_dwordx4 %0, %1 offset:%2" K2_WPOL
                 :
                 : "v"(wvoff), "s"(sb), "i"(PC == 0 ? 0 : PC == 1 ? 2048 : 1024), "{m0}"(lds_dst + (KT % K2_NB) * K2_BSLOT)
                 : "memory");
  }
}
template <int F>
DI void k2_issue_n(const u16* nb, unsigned nvoff, u32x4& r) {
  if constexpr (F <= 31)
    asm volatile("global_load_dwordx4 %0, %1, %2 offset:%3" : "=v"(r) : "v"(nvoff), "s"(nb + (F >> 1) * 2048), "i"((F & 1) * 1024) : "memory");
}
struct K2GS { f32x4 g0, g1, s0, s1; };
DI K2GS k2_gs(unsigned gsa) {
  K2GS r;
  r.g0 = *(const LAS f32x4*)(uintptr_t)(gsa); r.g1 = *(const LAS f32x4*)(uintptr_t)(gsa + 16);
  r.s0 = *(const LAS f32x4*)(uintptr_t)(gsa + 4096); r.s1 = *(const LAS f32x4*)(uintptr_t)(gsa + 4096 + 16);
  return r;
}
template <int WAIT>
DI u32x4 k2_xform(u32x4& x, const K2GS& q) {
  asm volatile("s_waitcnt vmcnt(%1)" : "+v"(x) : "i"(WAIT) : "memory");
  unsigned t0, t1, t2, t3, o0, o1, o2, o3;
  asm volatile(
      "v_lshlrev_b32 %4, 16, %24\n\tv_lshlrev_b32 %5, 16, %25\n\tv_lshlrev_b32 %6, 16, %26\n\tv_lshlrev_b32 %7, 16, %27\n\t"
      "v_fma_f32 %0, %24, %9, %17\n\tv_fma_f32 %1, %25, %11, %19\n\tv_fma_f32 %2, %26, %13, %21\n\tv_fma_f32 %3, %27, %15, %23\n\t"
      "v_fma_f32 %4, %4, %8, %16\n\tv_fma_f32 %5, %5, %10, %18\n\tv_fma_f32 %6, %6, %12, %20\n\tv_fma_f32 %7, %7, %14, %22\n\t"
      "v_cvt_pk_bf16_f32 %0, %4, %0\n\tv_cvt_pk_bf16_f32 %1, %5, %1\n\tv_cvt_pk_bf16_f32 %2, %6, %2\n\tv_cvt_pk_bf16_f32 %3, %7, %3\n\t"
      "s_nop 1"
      : "=&v"(o0), "=&v"(o1), "=&v"(o2), "=&v"(o3), "=&v"(t0), "=&v"(t1), "=&v"(t2), "=&v"(t3)
      : "v"(q.g0[0]), "v"(q.g0[1]), "v"(q.g0[2]), "v"(q.g0[3]), "v"(q.g1[0]), "v"(q.g1[1]), "v"(q.g1[2]), "v"(q.g1[3]),
        "v"(q.s0[0]), "v"(q.s0[1]), "v"(q.s0[2]), "v"(q.s0[3]), "v"(q.s1[0]), "v"(q.s1[1]), "v"(q.s1[2]), "v"(q.s1[3]),
        "v"(x[0]), "v"(x[1]), "v"(x[2]), "v"(x[3]));
  return (u32x4){o0, o1, o2, o3};
}

struct K2State {
  f32x16 acc[2][3];
  bf16x8 fb[2][6];
  u32x4 nr[K2_DN];
  u32x4 hf[2];
  K2GS gs;
  unsigned tv[2], tdummy;
  const u16* tb[4];
};
template <int P>
DI bf16x8 k2_ldb(const unsigned (&bb)[2][2], int hd, int sg) {
  constexpr int slot = (P >> 1) % K2_NB;
  const unsigned a = bb[hd][slot / 3] + (slot % 3) * K2_BSLOT + (P & 1) * 6144 + (sg == 0 ? 0 : sg == 1 ? 2048 : 1024);
  return cat8(lds_tr(a), lds_tr(a + 512));
}
template <int P>
DI void k2_phase(K2State& st, const unsigned (&bb)[2][2], unsigned gsa, const u16* wq, unsigned wvoff, unsigned wdst,
                 const u16* nb, unsigned nvoff, unsigned lds0) {
  constexpr int C = P & 1, N = C ^ 1, T = P >> 1, J = P & 1;
  constexpr bool NEXT = P < 31;
  const bf16x8 hf = __builtin_bit_cast(bf16x8, st.hf[C]);
#define LDB2(I0) do { if constexpr (NEXT) { st.fb[N][I0] = k2_ldb<P + 1>(bb, (I0) / 3, (I0) % 3); \
                                            st.fb[N][I0 + 1] = k2_ldb<P + 1>(bb, (I0 + 1) / 3, (I0 + 1) % 3); } } while (0)
#define MM(I) do { SBAR();                                                                                   \
    if ((I) % 3 == 2) st.acc[(I) / 3][2] = MFMA32(hf, st.fb[C][I], st.acc[(I) / 3][2]);                      \
    else st.acc[(I) / 3][(I) % 3] = MFMA32(st.fb[C][I], hf, st.acc[(I) / 3][(I) % 3]);                       \
    SBAR(); } while (0)
  if constexpr (J == 0) TR(lds0 + K2_LDS_TR, 0, T, 0);
  MM(0);
  if constexpr (NEXT) st.hf[N] = k2_xform<K2S.wn[P]>(st.nr[(P + 1) % K2_DN], st.gs);
  MM(1);
  if constexpr (J == 1 && NEXT) {
    TR(lds0 + K2_LDS_TR, 0, T, 1);
    asm volatile("s_waitcnt vmcnt(%0)" ::"i"(K2S.ww[T]) : "memory");
    TR(lds0 + K2_LDS_TR, 0, T, 2);
    asm volatile("s_barrier" ::: "memory");
    TR(lds0 + K2_LDS_TR, 0, T, 3);
  }
  LDB2(0);
  k2_issue_n<P + 1 + K2_DN>(nb, nvoff, st.nr[(P + 1) % K2_DN]);
  MM(2);
  LDB2(2);
  k2_issue_b<T + K2_DW, J == 0 ? 0 : 2>(wq, wvoff, wdst);
  MM(3);
  LDB2(4);
  if constexpr (J == 0) k2_issue_b<T + K2_DW, 1>(wq, wvoff, wdst);
  MM(4);
  if constexpr (k2_touch(P) >= 0)
    asm volatile("global_load_dword %0, %1, %2" : "+v"(st.tdummy) : "v"(st.tv[k2_touch(P) >> 1]), "s"(st.tb[k2_touch(P)]) : "memory");
  if constexpr (P < 30) st.gs = k2_gs(gsa + ((P + 2) >> 1) * 256 + (P & 1) * 64);
  MM(5);
#undef LDB2
#undef MM
}
template <int P>
DI void k2_phases(K2State& st, const unsigned (&bb)[2][2], unsigned gsa, const u16* wq, unsigned wvoff, unsigned wdst,
                  const u16* nb, unsigned nvoff, unsigned lds0) {
  k2_phase<P>(st, bb, gsa, wq, wvoff, wdst, nb, nvoff, lds0);
  if constexpr (P < 31) k2_phases<P + 1>(st, bb, gsa, wq, wvoff, wdst, nb, nvoff, lds0);
}

#define GAS __attribute__((address_space(1)))
typedef GAS unsigned long long gu64;
typedef GAS unsigned gu32;
#define RLX_AGENT __ATOMIC_RELAXED, __HIP_MEMORY_SCOPE_AGENT
constexpr unsigned SPIN_CAP = 1u << 18;
DI unsigned long long mk64(unsigned lo, unsigned hi) { return ((unsigned long long)hi << 32) | lo; }
template <int CTRL>
DI unsigned dpp_maxu(unsigned v) {
  const unsigned o = __builtin_amdgcn_update_dpp(0u, v, CTRL, 0xf, 0xf, true);
  return v > o ? v : o;
}
DI unsigned rowmaxu(unsigned v) {
  v = dpp_maxu<0xB1>(v); v = dpp_maxu<0x4E>(v); v = dpp_maxu<0x141>(v); return dpp_maxu<0x140>(v);
}
DI float sum8(float v) { return dpp_add<0x141>(dpp_add<0x4E>(dpp_add<0xB1>(v))); }
DI float sumrows(float v) { v = swap16_add(v, v); return swap32_add(v, v); }
DI f32x2 up2(unsigned w) { return (f32x2){bflo(w), bfhi(w)}; }
DI unsigned okey(float v) {
  const unsigned raw = __float_as_uint(v);
  return v == v ? (raw ^ ((unsigned)((int)raw >> 31) | 0x80000000u)) : 0u;
}
#define GLOADC(dst, voff, sbase, off) \
  asm volatile("global_load_dwordx4 %0, %1, %2 offset:%3 sc1" : "=v"(dst) : "v"(voff), "s"(sbase), "i"(off) : "memory")
template <class T> DI T ldsr(unsigned a) { return *(const LAS T*)(uintptr_t)a; }
template <class T> DI void ldsw(unsigned a, T v) { *(LAS T*)(uintptr_t)a = v; }
DI float ex2(float x) { return __builtin_amdgcn_exp2f(x); }
DI float xhalf_max(float v) {
  auto rr = __builtin_amdgcn_permlane32_swap(__float_as_uint(v), __float_as_uint(v), false, false);
  return fmaxf(__uint_as_float(rr[0]), __uint_as_float(rr[1]));
}
DI void lds_arrive(unsigned a) {
  asm volatile("s_waitcnt lgkmcnt(0)" ::: "memory");
  if ((threadIdx.x & 63) == 0) __hip_atomic_fetch_add((LAS unsigned*)(uintptr_t)a, 1u, __ATOMIC_RELAXED, __HIP_MEMORY_SCOPE_WORKGROUP);
}
DI void lds_await(unsigned a, unsigned n) {
  while (__hip_atomic_load((LAS unsigned*)(uintptr_t)a, __ATOMIC_RELAXED, __HIP_MEMORY_SCOPE_WORKGROUP) < n) __builtin_amdgcn_s_sleep(2);
  asm volatile("" ::: "memory");
}

#define K4_R 8
#define HD0 14
#define HD0R 8
#define HD1 16
#define HD2 HD1
#define HD3 HD1
#define HD4 HD1
#define FT(i)
#define GT(i) TS(i)
#define KVF_AT 1
#define HDK 10
#define HDKR 8
#define SH_D 4
#define SH_W1 8
#define SH_L1 4
#define VECW 0
#define SH_END 24
#define HD0B 0
constexpr int K4_SM = 153600;
constexpr int K4_ROW = K4_SM;
constexpr int K4_VEC = K4_ROW + 2048;
constexpr int K4_BEST = K4_VEC + 1280;
constexpr int K4_FIN = K4_BEST + 64;
constexpr int K4_H = K4_FIN + 512;
constexpr int K4_A = K4_H + 1024;
constexpr int K4_RED = K4_A + 1024;
constexpr int K4_RED2 = K4_RED + 2048;
constexpr int K3_CNT = K4_RED2 + 1024;
constexpr int K4_IMG = 0;
constexpr int K3_QX = 49152;
constexpr int K3_LDS = 163840, K3_PART = 4096 + 256;
static_assert(K4_VEC >= K2_LDS_GS + 8192 && K3_CNT + 64 <= K3_LDS && 12 * K3_PART <= 65536 && 98304 + 12 * K3_PART <= K4_SM, "LDS map");
constexpr unsigned POISON = 0xffffffffu;

__global__ __launch_bounds__(512) void k2_main(const u16* __restrict__ nf, const u16* __restrict__ wqkv,
                                               const float* __restrict__ modp, u16* kvf, u16* of,
                                               const u16* __restrict__ wo, const u16* __restrict__ x,
                                               const u16* __restrict__ wr, const u16* __restrict__ wg,
                                               const u16* __restrict__ wu, const u16* __restrict__ wd,
                                               u16* __restrict__ out, unsigned long long* sync) {
  PROF_BEGIN();
  extern __shared__ __attribute__((aligned(16))) char smem[];
  const unsigned lds0 = (unsigned)(uintptr_t)smem;
  const int tid = threadIdx.x, lane = tid & 63;
  const int wid = __builtin_amdgcn_readfirstlane(tid >> 6);
  const int b = blockIdx.x, xcd = b & 7, jj = b >> 3;
  const int mt = (xcd & 1) * 8 + (jj & 7), nt = (xcd >> 1) * 4 + (jj >> 3);
  const int qg = wid >> 2, kq = wid & 3;
  const int kr = lane >> 3, pos = lane & 7;
  const bool has_gu = b < 224;
  const int cg = b % 56, ks = has_gu ? b / 56 : 0;
  const int dcg = b & 15, dks = b >> 4;
  asm volatile("" : "+s"(kvf), "+s"(of), "+s"(wo), "+s"(x), "+s"(wr), "+s"(wg), "+s"(wu), "+s"(wd), "+s"(out), "+s"(sync));
  TS_DECL(); TS(0);
  unsigned tdummy;
  u32x4 fq[2], fk[2], fv[2];
  {
    const int wm = kq, kh = qg;
    K2State st;
    if (tid < 16) ldsw(lds0 + K3_CNT + tid * 4, 0u);
    st.tv[0] = (unsigned)((lane >> 1) * FF * 2 + (lane & 1) * 64);
    st.tv[1] = (unsigned)((lane >> 1) * D * 2 + (lane & 1) * 64);
    st.tdummy = 0u;
    st.tb[0] = (qg ? wu : wg) + (size_t)(ks * 256 + kq * 64) * FF + cg * 64;
    st.tb[1] = st.tb[0] + (size_t)32 * FF;
    st.tb[2] = wd + (size_t)(dks * 224 + (wid < 7 ? wid * 28 : 192)) * D + dcg * 64;
    st.tb[3] = wo + (size_t)((mt & 3) * 256 + wid * 32) * D + nt * 64;
    f32x4 mp0, mp1;
    {
      const unsigned vo = (tid & 255) * 16;
      const float* mb = modp + (kh == 0 ? 1024 : 0);
      GLOAD16(mp0, vo, mb);
      GLOAD16(mp1, vo, mb + 6144);
    }
    const unsigned wvoff = (unsigned)(((wid * 8 + kr) * 3072 + (((pos >> 2) ^ ((kr >> 1) & 1)) * 32) + (pos & 3) * 8) * 2);
    const u16* wq = wqkv + nt * 64;
    const unsigned wdst = lds0 + wid * 3072;
    const u16* nb = nf + (size_t)(mt * 4 + wm) * 32768 + kh * 1024;
    const unsigned nvoff = lane * 16;
#define WT(KT) do { if constexpr (KT < K2_DW) { k2_issue_b<KT, 0>(wq, wvoff, wdst); k2_issue_b<KT, 1>(wq, wvoff, wdst); \
                                                  k2_issue_b<KT, 2>(wq, wvoff, wdst); } } while (0)
#define NT(F) do { if constexpr (F < K2_DN) k2_issue_n<F>(nb, nvoff, st.nr[(F) % K2_DN]); } while (0)
    NT(0); WT(0); NT(1); NT(2); WT(1); NT(3); NT(4); WT(2); NT(5); NT(6); WT(3); NT(7); NT(8);
#undef WT
#undef NT
    TR(lds0 + K2_LDS_TR, 0, 16, 1);
    asm volatile("s_waitcnt vmcnt(%2)" : "+v"(mp0), "+v"(mp1) : "i"(K2S.wn[32]) : "memory");
    {
      f32x4 v;
#pragma unroll
      for (int j = 0; j < 4; ++j) {
        v[j] = rbf(mp0[j] + mp1[j]);
        if (kh == 0) v[j] = rbf(1.f + v[j]);
      }
      *(LAS f32x4*)(uintptr_t)(lds0 + K2_LDS_GS + tid * 16) = v;
    }
    const int h = lane >> 5, q4 = (lane & 15) >> 2, p4 = lane & 3, ch = (lane >> 4) & 1;
    unsigned bb[2][2];
#pragma unroll
    for (int hd = 0; hd < 2; ++hd) {
      bb[hd][0] = lds0 + kh * 12288 + h * 3072 + q4 * 128 + (((hd ^ kh) ^ ((q4 >> 1) & 1)) * 64) + ch * 32 + p4 * 8;
      bb[hd][1] = bb[hd][0] + 3 * K2_BSLOT;
      asm volatile("" : "+v"(bb[hd][0]), "+v"(bb[hd][1]));
    }
    unsigned gsa = lds0 + K2_LDS_GS + (32 * kh + 8 * h) * 4;
    asm volatile("" : "+v"(gsa));
#pragma unroll
    for (int a = 0; a < 2; ++a)
#pragma unroll
      for (int b = 0; b < 3; ++b) st.acc[a][b] = (f32x16){};
    asm volatile("s_waitcnt vmcnt(%0) lgkmcnt(0)\n\ts_barrier" ::"i"(K2S.ww[16]) : "memory");
    TR(lds0 + K2_LDS_TR, 0, 16, 2);
    TS(1);
    st.gs = k2_gs(gsa);
#pragma unroll
    for (int i = 0; i < 6; ++i) st.fb[0][i] = k2_ldb<0>(bb, i / 3, i % 3);
    st.hf[0] = k2_xform<K2S.ww[16]>(st.nr[0], st.gs);
    k2_issue_n<K2_DN>(nb, nvoff, st.nr[0]);
    st.gs = k2_gs(gsa + 64);
    k2_phases<0>(st, bb, gsa, wq, wvoff, wdst, nb, nvoff, lds0);
    TS(2);
    tdummy = st.tdummy;
    const unsigned ibo = lds0 + (qg == 1 ? 0 : 98304) + kq * 12288 + lane * 16;
    const unsigned ibm = lds0 + (qg == 0 ? 0 : 98304) + kq * 12288 + lane * 16;
#pragma unroll
    for (int sg = 0; sg < 3; ++sg)
#pragma unroll
      for (int g = 0; g < 4; ++g)
        ldsw(ibo + sg * 4096 + g * 1024,
             (f32x4){st.acc[1][sg][4 * g], st.acc[1][sg][4 * g + 1], st.acc[1][sg][4 * g + 2], st.acc[1][sg][4 * g + 3]});
    FT(9);
    LDS_BARRIER();
    FT(10);
    f32x4 in[12];
#pragma unroll
    for (int i = 0; i < 12; ++i) in[i] = ldsr<f32x4>(ibm + (i < 8 ? 4096 + i * 1024 : (i - 8) * 1024));
#pragma unroll
    for (int i = 0; i < 3; ++i) {
      const int sg = i < 2 ? i + 1 : 0;
      const float sc = sg == 0 ? QSCALE : 1.f;
#pragma unroll
      for (int s = 0; s < 2; ++s) {
        const f32x4 a = in[i * 4 + s * 2], c = in[i * 4 + s * 2 + 1];
        u32x4 o;
#pragma unroll
        for (int j = 0; j < 4; ++j) {
          const float lo = st.acc[0][sg][8 * s + 2 * j] + (j < 2 ? a[2 * j] : c[2 * j - 4]);
          const float hi = st.acc[0][sg][8 * s + 2 * j + 1] + (j < 2 ? a[2 * j + 1] : c[2 * j - 3]);
          o[j] = pack2(lo * sc, hi * sc);
        }
        if (sg == 0) { fq[s] = o; ldsw(lds0 + K3_QX + wid * 2048 + s * 1024 + lane * 16, o); }
        else {
          if (sg == 1) fk[s] = o; else fv[s] = o;
          st16(kvf + (size_t)(sg - 1) * (2u << 20) + ((size_t)((nt * 2 + qg) * 64 + mt * 4 + kq) * 2 + s) * 512 + lane * 8, o);
        }
      }
    }
  }
  u16* const op = of + ((size_t)(((mt * 4 + kq) * 2 + qg) * 16 + nt) * 2) * 512 + lane * 8;
  const int erow = tid >> 3, ec = tid & 7;
  u32x4 wgu[16], wdn[7];
  u32x4 xr[2];
  const unsigned wvoff = (unsigned)(((kq * 8 + kr) * D + (((pos >> 2) ^ ((kr >> 1) & 1)) * 32) + (pos & 3) * 8) * 2);
  const u16* wb = wo + nt * 64;
#define WDMA(P, HF)                                                                                  \
  asm volatile("s_nop 0\n\tglobal_load_lds_dwordx4 %0, %1"                                           \
               :                                                                                     \
               : "v"(wvoff), "s"(wb + (size_t)((P) * 64 + (HF) * 32) * D),                           \
                 "{m0}"(lds0 + K4_IMG + (HF) * 65536 + (P) * 4096 + kq * 1024)                       \
               : "memory")
  const unsigned vgu = (unsigned)((erow * FF + pos * 8) * 2), vdn = (unsigned)((erow * D + pos * 8) * 2);
  const size_t gbase = (size_t)(ks * 256) * FF + cg * 64;
#define SDMA(U)                                                                                      \
  do {                                                                                               \
    if (stage)                                                                                       \
      asm volatile("s_nop 0\n\tglobal_load_lds_dwordx4 %0, %1 nt"                                    \
                   :                                                                                 \
                   : "v"(sv), "s"(sp + ((U) == 3 ? s3 : (U) * ss)), "{m0}"(sl + (U) * 4096)          \
                   : "memory");                                                                      \
  } while (0)
  {
    const int head = nt * 2 + qg;
    const __amdgpu_buffer_rsrc_t rs = __builtin_amdgcn_make_buffer_rsrc((void*)kvf, 0, 8 << 20, 0x00020000);
    const int vk = lane * 16, vv = lane * 16 + (4 << 20);
    const int hb = head * 131072 + kq * 2048;
#define LDK(so, c) __builtin_bit_cast(bf16x8, __builtin_amdgcn_raw_buffer_load_b128(rs, vk + (c), (so), 16))
#define LDV(so, c) __builtin_bit_cast(bf16x8, __builtin_amdgcn_raw_buffer_load_b128(rs, vv + (c), (so), 16))
#define TOFF(i) (hb + ((mt + (i)) & 15) * 8192)
    FT(11);
    LDS_BARRIER();
    bf16x8 q[4][2];
    q[0][0] = __builtin_bit_cast(bf16x8, fq[0]); q[0][1] = __builtin_bit_cast(bf16x8, fq[1]);
#pragma unroll
    for (int i = 1; i < 4; ++i) {
      const unsigned qa = lds0 + K3_QX + (qg * 4 + ((i + kq) & 3)) * 2048 + lane * 16;
      q[i][0] = lds_b128(qa); q[i][1] = lds_b128(qa + 1024);
    }
    bf16x8 ka0 = __builtin_bit_cast(bf16x8, fk[0]), ka1 = __builtin_bit_cast(bf16x8, fk[1]);
    bf16x8 va0 = __builtin_bit_cast(bf16x8, fv[0]), va1 = __builtin_bit_cast(bf16x8, fv[1]);
    bf16x8 kb0, kb1, vb0, vb1;
    bf16x8 ones;
    {
      const short o = ((((lane & 15) >> 2) & 1) == ((lane >> 4) & 1)) ? (short)0x3F80 : (short)0;
#pragma unroll
      for (int j = 0; j < 8; ++j) ones[j] = o;
    }
    const f32x16 zero = (f32x16){};
    f32x16 o[4] = {zero, zero, zero, zero};
    f32x4 l[4] = {(f32x4){}, (f32x4){}, (f32x4){}, (f32x4){}};
    f32x16 sA = MFMA32(ka0, q[0][0], zero), sB;
    sA = MFMA32(ka1, q[0][1], sA);
    asm volatile("s_waitcnt lgkmcnt(0)\n\ts_barrier"
                 : "+v"(q[1][0]), "+v"(q[1][1]), "+v"(q[2][0]), "+v"(q[2][1]), "+v"(q[3][0]), "+v"(q[3][1])::"memory");
    TS(3);
    u32x4 p0, p1 = (u32x4){0u, 0u, 0u, 0u};
#define PK(S_, i) pack2(S_[i], S_[i + 1])
#define EX(S_, i) S_[i] = ex2(S_[i])
#define PIN(x_) asm volatile("" : "+v"(x_))
#define UNIT(SC, SN, KN0, KN1, N, C, V0C, P, V1P, LD_A, LD_B)                               \
  do {                                                                                     \
    SN = MFMA32(KN0, q[N][0], zero); SBAR();                                               \
    EX(SC, 0); EX(SC, 1); EX(SC, 2); p0[0] = PK(SC, 0); PIN(p0); SBAR();                   \
    o[P] = MFMA32(V1P, __builtin_bit_cast(bf16x8, p1), o[P]); SBAR();                      \
    LD_A;                                                                                  \
    EX(SC, 3); EX(SC, 4); EX(SC, 5); p0[1] = PK(SC, 2); PIN(p0); SBAR();                   \
    l[P] = MFMA16(ones, __builtin_bit_cast(bf16x8, p1), l[P]); SBAR();                     \
    EX(SC, 6); EX(SC, 7); p0[2] = PK(SC, 4); p0[3] = PK(SC, 6); PIN(p0); SBAR();           \
    SN = MFMA32(KN1, q[N][1], SN); SBAR();                                                 \
    LD_B;                                                                                  \
    EX(SC, 8); EX(SC, 9); EX(SC, 10); p1[0] = PK(SC, 8); PIN(p1); SBAR();                  \
    o[C] = MFMA32(V0C, __builtin_bit_cast(bf16x8, p0), o[C]); SBAR();                      \
    EX(SC, 11); EX(SC, 12); EX(SC, 13); p1[1] = PK(SC, 10); PIN(p1); SBAR();               \
    l[C] = MFMA16(ones, __builtin_bit_cast(bf16x8, p0), l[C]); SBAR();                     \
    EX(SC, 14); EX(SC, 15); p1[2] = PK(SC, 12); p1[3] = PK(SC, 14); PIN(p1); SBAR();       \
  } while (0)
    const unsigned fl = (unsigned)((SY_KV + (head * 4 + kq) * 16) * 8);
#define KVFLAG()                                                                                       \
  do {                                                                                                 \
    asm volatile("s_waitcnt vmcnt(0)" : "+v"(tdummy)::"memory");                                       \
    if (lane == 0) __hip_atomic_store((gu32*)((char*)sync + fl) + mt, 1u, RLX_AGENT);                  \
    FT(13);                                                                                            \
    SBAR();                                                                                            \
  } while (0)
    UNIT(sA, sB, ka0, ka1, 1, 0, va0, 3, va1  , (void)0, (void)0);
    KVFLAG();
    UNIT(sB, sA, ka0, ka1, 2, 1, va0, 0, va1, (void)0, (void)0);
    UNIT(sA, sB, ka0, ka1, 3, 2, va0, 1, va1, (void)0, (void)0);
    {
      const unsigned vo = fl + (lane & 15) * 4;
      if (HDK) __builtin_amdgcn_s_sleep(HDK);
      _Pragma("nounroll") for (unsigned spin = 0; spin < SPIN_CAP; ++spin) {
        unsigned c;
        asm volatile("global_load_dword %0, %1, %2 sc1\n\ts_waitcnt vmcnt(0)" : "=v"(c) : "v"(vo), "s"(sync) : "memory");
        if (__all(c != 0u)) break;
        __builtin_amdgcn_s_sleep(HDKR);
      }
    }
    TS(4);
    kb0 = LDK(TOFF(1), 0); kb1 = LDK(TOFF(1), 1024); SBAR();
    vb0 = LDV(TOFF(1), 0); vb1 = LDV(TOFF(1), 1024); SBAR();
    ka0 = LDK(TOFF(2), 0); ka1 = LDK(TOFF(2), 1024); SBAR();
    UNIT(sB, sA, kb0, kb1, 0, 3, va0, 2, va1, (void)0, (void)0);
    UNIT(sA, sB, kb0, kb1, 1, 0, vb0, 3, va1, va0 = LDV(TOFF(2), 0), va1 = LDV(TOFF(2), 1024));
    UNIT(sB, sA, kb0, kb1, 2, 1, vb0, 0, vb1, (void)0, (void)0);
    UNIT(sA, sB, kb0, kb1, 3, 2, vb0, 1, vb1, (void)0, (void)0);
    UNIT(sB, sA, ka0, ka1, 0, 3, vb0, 2, vb1, kb0 = LDK(TOFF(3), 0), kb1 = LDK(TOFF(3), 1024));
#pragma unroll 1
    for (int j = 2; j < 16; j += 2) {
      const int t1 = TOFF(j + 1), t2 = TOFF(j + 2), t3 = TOFF(j + 3);
      const bool stage = qg == 0 && j < 14;
      const unsigned sv = j < 10 ? vgu : vdn, sl = lds0 + (j - 2) * 8192 + kq * 1024;
      const int ss = j < 10 ? 32 * FF : 32 * D, s3 = j == 12 ? 2 * ss : 3 * ss;
      const u16* sp = (j < 6 ? wg + gbase : j < 10 ? wu + gbase : wd + (size_t)(dks * 224) * D + dcg * 64) + ((j - 2) & 2) * 2 * ss;
      UNIT(sA, sB, ka0, ka1, 1, 0, va0, 3, vb1, vb0 = LDV(t1, 0), vb1 = LDV(t1, 1024));
      UNIT(sB, sA, ka0, ka1, 2, 1, va0, 0, va1, SDMA(0), (void)0);
      UNIT(sA, sB, ka0, ka1, 3, 2, va0, 1, va1, SDMA(1), (void)0);
      UNIT(sB, sA, kb0, kb1, 0, 3, va0, 2, va1, ka0 = LDK(t2, 0), ka1 = LDK(t2, 1024));
      UNIT(sA, sB, kb0, kb1, 1, 0, vb0, 3, va1, va0 = LDV(t2, 0), va1 = LDV(t2, 1024));
      UNIT(sB, sA, kb0, kb1, 2, 1, vb0, 0, vb1, SDMA(2), (void)0);
      UNIT(sA, sB, kb0, kb1, 3, 2, vb0, 1, vb1, SDMA(3), (void)0);
      UNIT(sB, sA, ka0, ka1, 0, 3, vb0, 2, vb1, kb0 = LDK(t3, 0), kb1 = LDK(t3, 1024));
    }
    o[3] = MFMA32(vb1, __builtin_bit_cast(bf16x8, p1), o[3]);
    l[3] = MFMA16(ones, __builtin_bit_cast(bf16x8, p1), l[3]);
#undef UNIT
    TS(5);
    if (qg == 0) {
      asm volatile("s_waitcnt vmcnt(6)" ::: "memory");
#pragma unroll
      for (int i = 0; i < 16; ++i) wgu[i] = ldsr<u32x4>(lds0 + i * 4096 + kq * 1024 + lane * 16);
#pragma unroll
      for (int i = 0; i < 7; ++i) wdn[i] = ldsr<u32x4>(lds0 + (16 + i) * 4096 + kq * 1024 + lane * 16);
      asm volatile("" : "+v"(wgu[0]), "+v"(wgu[1]), "+v"(wgu[2]), "+v"(wgu[3]), "+v"(wgu[4]), "+v"(wgu[5]), "+v"(wgu[6]), "+v"(wgu[7]),
                        "+v"(wgu[8]), "+v"(wgu[9]), "+v"(wgu[10]), "+v"(wgu[11]), "+v"(wgu[12]), "+v"(wgu[13]), "+v"(wgu[14]), "+v"(wgu[15]));
      asm volatile("" : "+v"(wdn[0]), "+v"(wdn[1]), "+v"(wdn[2]), "+v"(wdn[3]), "+v"(wdn[4]), "+v"(wdn[5]), "+v"(wdn[6]));
    }
    {
      const unsigned xo = (unsigned)((erow * D + ec * 8) * 2);
      const u16* xb0 = x + (size_t)(mt * 128) * D + nt * 64;
      GLOAD16(xr[0], xo, xb0);
      GLOAD16(xr[1], xo, xb0 + (size_t)64 * D);
    }
    const unsigned rb = lds0 + 98304;
    if (qg == 1) lds_await(lds0 + K3_CNT + 4, 4u);
#pragma unroll
    for (int i = 1; i < 4; ++i) {
      const unsigned wa = rb + (((i + kq) & 3) * 3 + i - 1) * K3_PART;
#pragma unroll
      for (int g = 0; g < 4; ++g)
        ldsw(wa + g * 1024 + lane * 16, (f32x4){o[i][4 * g], o[i][4 * g + 1], o[i][4 * g + 2], o[i][4 * g + 3]});
      ldsw(wa + 4096 + lane * 4, l[i][0]);
    }
    lds_arrive(lds0 + K3_CNT);
    lds_await(lds0 + K3_CNT, qg == 0 ? 4u : 8u);
    f32x16 oo = o[0];
    float lsum = l[0][0];
#pragma unroll
    for (int i = 0; i < 3; ++i) {
      const unsigned ra = rb + (kq * 3 + i) * K3_PART;
#pragma unroll
      for (int g = 0; g < 4; ++g) {
        const f32x4 v = ldsr<f32x4>(ra + g * 1024 + lane * 16);
        oo[4 * g] += v[0]; oo[4 * g + 1] += v[1]; oo[4 * g + 2] += v[2]; oo[4 * g + 3] += v[3];
      }
      lsum += ldsr<float>(ra + 4096 + lane * 4);
    }
    PIN(oo); PIN(lsum);
    float rl = 1.f / lsum;
    bool bad = !(lsum < 1.0e30f && lsum > 1.0e-30f);
#pragma unroll
    for (int r = 0; r < 16; ++r) {
      oo[r] *= rl;
      bad |= !(fabsf(oo[r]) < 3.0e38f);
    }
    if (__builtin_expect(__any(bad), 0)) {
      const bf16x8 q0 = q[0][0], q1 = q[0][1];
      float m = -INFINITY, ls = 0.f;
      oo = zero;
      for (int t = 0; t < 64; ++t) {
        const int so = head * 131072 + t * 2048;
        const bf16x8 a0 = LDK(so, 0), a1 = LDK(so, 1024), b0 = LDV(so, 0), b1 = LDV(so, 1024);
        f32x16 s = MFMA32(a0, q0, zero);
        s = MFMA32(a1, q1, s);
        float tm = s[0];
#pragma unroll
        for (int r = 1; r < 16; ++r) tm = fmaxf(tm, s[r]);
        tm = xhalf_max(tm);
        const float mn = fmaxf(m, tm);
        const float alpha = ex2(m - mn);
        m = mn;
        u32x4 pa, pb;
        float ps = 0.f;
#pragma unroll
        for (int j = 0; j < 4; ++j) {
          pa[j] = pack2(ex2(s[2 * j] - mn), ex2(s[2 * j + 1] - mn));
          pb[j] = pack2(ex2(s[8 + 2 * j] - mn), ex2(s[8 + 2 * j + 1] - mn));
          ps += bflo(pa[j]) + bfhi(pa[j]) + bflo(pb[j]) + bfhi(pb[j]);
        }
        ps += __shfl_xor(ps, 32);
        ls = ls * alpha + ps;
#pragma unroll
        for (int r = 0; r < 16; ++r) oo[r] *= alpha;
        oo = MFMA32(b0, __builtin_bit_cast(bf16x8, pa), oo);
        oo = MFMA32(b1, __builtin_bit_cast(bf16x8, pb), oo);
      }
      rl = 1.f / ls;
#pragma unroll
      for (int r = 0; r < 16; ++r) oo[r] *= rl;
    }
#pragma unroll
    for (int s = 0; s < 2; ++s) {
      u32x4 w;
#pragma unroll
      for (int j = 0; j < 4; ++j) w[j] = min(pack2(oo[8 * s + 2 * j], oo[8 * s + 2 * j + 1]), 0xfffeffffu);
      st16(op + s * 512, w);
    }
    if (qg == 1) lds_arrive(lds0 + K3_CNT + 16);
    else {
      st16(op + 16384, (u32x4){POISON, POISON, POISON, POISON});
      st16(op + 16384 + 512, (u32x4){POISON, POISON, POISON, POISON});
    }
#undef PK
#undef EX
#undef LDK
#undef LDV
#undef TOFF
  }
  if (qg == 0) {
#define PACE() asm volatile("s_waitcnt vmcnt(%0)" ::"i"(SH_D - 1) : "memory")
#pragma unroll
    for (int p = 0; p < SH_L1; ++p) WDMA(p & 15, p >> 4);
    asm volatile("s_waitcnt vmcnt(%2)" : "+v"(xr[0]), "+v"(xr[1]) : "i"(SH_L1) : "memory");
    if (lane == 0) __hip_atomic_fetch_add((gu32*)(sync + SY_OC + (mt * 4 + kq) * 16), 1u, RLX_AGENT);
    lds_arrive(lds0 + K3_CNT + 4);
    TS(6);
    float mv[8];
    unsigned wrw;
    {
      const float* mp = modp + 2048 + nt * 64;
#pragma unroll
      for (int i = 0; i < 8; ++i)
        asm volatile("global_load_dword %0, %1, %2" : "=v"(mv[i]) : "v"(lane * 4), "s"(mp + (i >> 2) * 6144 + (i & 3) * 1024) : "memory");
      asm volatile("global_load_ushort %0, %1, %2" : "=v"(wrw) : "v"(lane * 2), "s"(wr + nt * 64) : "memory");
    }
#pragma unroll
    for (int p = SH_L1; p < SH_END; ++p) { PACE(); WDMA(p & 15, p >> 4); }
    if (SH_END == 24) {
      u32x4 pf[8];
#pragma unroll
      for (int p = 8; p < 16; ++p) { PACE(); GLOAD16(pf[p - 8], wvoff, wb + (size_t)(p * 64 + 32) * D); }
      asm volatile("s_waitcnt vmcnt(0)" : "+v"(pf[0]), "+v"(pf[1]), "+v"(pf[2]), "+v"(pf[3]), "+v"(pf[4]), "+v"(pf[5]), "+v"(pf[6]),
                   "+v"(pf[7])::"memory");
    }
    FT(15);
    asm volatile("s_waitcnt vmcnt(0)" : "+v"(mv[0]), "+v"(mv[1]), "+v"(mv[2]), "+v"(mv[3]), "+v"(mv[4]), "+v"(mv[5]), "+v"(mv[6]), "+v"(mv[7]), "+v"(wrw)::"memory");
    if (wid == 0) {
      float v[4];
#pragma unroll
      for (int i = 0; i < 4; ++i) v[i] = rbf(mv[i] + mv[4 + i]);
      const float w = bflo(wrw);
      const float g2 = rbf(1.f + v[2]), gw = g2 * w;
      const unsigned va = lds0 + K4_VEC + lane * 4;
      ldsw(va, v[0]); ldsw(va + 256, g2); ldsw(va + 512, v[1]); ldsw(va + 768, v[3]); ldsw(va + 1024, gw);
      const float gs = wsum(gw), cs = wsum(v[1] * w);
      if (lane == 0) __hip_atomic_store((gu64*)sync + SY_GC + mt * 16 + nt, mk64(__float_as_uint(gs) | 1u, __float_as_uint(cs)), RLX_AGENT);
    }
    {
      const unsigned vo = (unsigned)((SY_OC + (mt * 4 + kq) * 16) * 8);
      _Pragma("nounroll") for (unsigned spin = 0; spin < SPIN_CAP; ++spin) {
        unsigned c;
        asm volatile("global_load_dword %0, %1, %2 sc1\n\ts_waitcnt vmcnt(0)" : "=v"(c) : "v"(vo), "s"(sync) : "memory");
        if (__all(c >= 16u)) break;
        __builtin_amdgcn_s_sleep(HD0R);
      }
    }
    lds_arrive(lds0 + K3_CNT + 8);
  } else {
    TS(6);
  }
  lds_await(lds0 + K3_CNT + 8, 4u);
  FT(12);
  float xv[2][8];
  {
    const int kh = qg, wt = kq, h = lane >> 5;
    unsigned bb[2];
    {
      const int q4 = (lane & 15) >> 2, p4 = lane & 3, ch = (lane >> 4) & 1;
#pragma unroll
      for (int c = 0; c < 2; ++c)
        bb[c] = lds0 + K4_IMG + qg * 32768 + (4 * h + q4) * 128 + ((c ^ ((q4 >> 1) & 1)) * 64) + ch * 32 + p4 * 8;
    }
#define WFRAG(C, F) cat8(lds_tr(bb[C] + (F) * 2048), lds_tr(bb[C] + (F) * 2048 + 1024))
    const u16* ob = of + (size_t)(mt * 4 + kq) * 32768 + qg * 8192;
    const unsigned ovoff = lane * 16;
    f32x16 acc0 = (f32x16){}, acc1 = (f32x16){};
    u32x4 ofr[16];
#pragma unroll
    for (int f = 0; f < 16; ++f) GLOADC(ofr[f], ovoff, ob + (f >> 2) * 2048, (f & 3) * 1024);
#pragma unroll
    for (int c = 0; c < 4; ++c) {
      bf16x8 w[4][2];
#pragma unroll
      for (int i = 0; i < 4; ++i) { w[i][0] = WFRAG(0, 4 * c + i); w[i][1] = WFRAG(1, 4 * c + i); }
      asm volatile("s_waitcnt vmcnt(%4)" : "+v"(ofr[4 * c]), "+v"(ofr[4 * c + 1]), "+v"(ofr[4 * c + 2]), "+v"(ofr[4 * c + 3]) : "i"(12 - 4 * c) : "memory");
#pragma unroll
      for (int i = 0; i < 4; ++i) {
        const bf16x8 a = __builtin_bit_cast(bf16x8, ofr[4 * c + i]);
        acc0 = MFMA32(w[i][0], a, acc0);
        acc1 = MFMA32(w[i][1], a, acc1);
      }
      SBAR();
    }
    TS(7);
    if (qg == 0) {
      lds_await(lds0 + K3_CNT + 16, 4u);
#pragma unroll
      for (int p = 8; p < 16; ++p) WDMA(p, 1);
      __builtin_amdgcn_s_sleep(HD0);
    } else if (HD0B) {
      __builtin_amdgcn_s_sleep(HD0B);
    }
#undef WDMA
    const u16* ol = ob + 16384;
    bb[0] += 65536; bb[1] += 65536;
#pragma unroll
    for (int f = 0; f < 16; ++f) GLOADC(ofr[f], ovoff, ol + (f >> 2) * 2048, (f & 3) * 1024);
    if (qg == 0) {
      asm volatile("s_waitcnt vmcnt(16)" ::: "memory");
      lds_arrive(lds0 + K3_CNT + 20);
    } else {
      lds_await(lds0 + K3_CNT + 20, 4u);
    }
#pragma unroll
    for (int c = 0; c < 4; ++c) {
      bf16x8 w[4][2];
#pragma unroll
      for (int i = 0; i < 4; ++i) { w[i][0] = WFRAG(0, 4 * c + i); w[i][1] = WFRAG(1, 4 * c + i); }
      asm volatile("s_waitcnt vmcnt(%4)" : "+v"(ofr[4 * c]), "+v"(ofr[4 * c + 1]), "+v"(ofr[4 * c + 2]), "+v"(ofr[4 * c + 3]) : "i"(12 - 4 * c) : "memory");
      _Pragma("nounroll") for (unsigned spin = 0; spin < SPIN_CAP; ++spin) {
        unsigned m = 0u;
#pragma unroll
        for (int i = 0; i < 4; ++i) m = max(max(max(m, ofr[4 * c + i][0]), ofr[4 * c + i][1]), max(ofr[4 * c + i][2], ofr[4 * c + i][3]));
        if (__builtin_expect(!__any(m == POISON), 1)) break;
        __builtin_amdgcn_s_sleep(HD0R);
        asm volatile("s_waitcnt vmcnt(0)" ::: "memory");
#pragma unroll
        for (int f = 4 * c; f < 16; ++f)
          asm volatile("global_load_dwordx4 %0, %1, %2 offset:%3 sc1" : "+v"(ofr[f]) : "v"(ovoff), "s"(ol + (f >> 2) * 2048), "i"((f & 3) * 1024) : "memory");
        asm volatile("s_waitcnt vmcnt(%4)" : "+v"(ofr[4 * c]), "+v"(ofr[4 * c + 1]), "+v"(ofr[4 * c + 2]), "+v"(ofr[4 * c + 3]) : "i"(12 - 4 * c) : "memory");
      }
#pragma unroll
      for (int i = 0; i < 4; ++i) {
        const bf16x8 a = __builtin_bit_cast(bf16x8, ofr[4 * c + i]);
        acc0 = MFMA32(w[i][0], a, acc0);
        acc1 = MFMA32(w[i][1], a, acc1);
      }
      SBAR();
    }
#undef WFRAG
    LDS_BARRIER();
    TS(8);
    {
      const unsigned wa = lds0 + K4_IMG + kh * 34816 + (wt * 32 + (lane & 31)) * 272 + h * 16;
#pragma unroll
      for (int g = 0; g < 4; ++g) {
        ldsw(wa + g * 32, (f32x4){acc0[4 * g], acc0[4 * g + 1], acc0[4 * g + 2], acc0[4 * g + 3]});
        ldsw(wa + 128 + g * 32, (f32x4){acc1[4 * g], acc1[4 * g + 1], acc1[4 * g + 2], acc1[4 * g + 3]});
      }
    }
    asm volatile("s_waitcnt vmcnt(0) lgkmcnt(0)\n\ts_barrier" : "+v"(xr[0]), "+v"(xr[1])::"memory");
    const unsigned va = lds0 + K4_VEC + ec * 32;
    const f32x4 ga0 = ldsr<f32x4>(va), ga1 = ldsr<f32x4>(va + 16);
    const f32x4 gw0 = ldsr<f32x4>(va + 1024), gw1 = ldsr<f32x4>(va + 1024 + 16);
    float sm[2], sq[2], sd[2];
#pragma unroll
    for (int i = 0; i < 2; ++i) {
      const unsigned ra = lds0 + K4_IMG + (erow + 64 * i) * 272 + ec * 32;
      const f32x4 a0 = ldsr<f32x4>(ra), a1 = ldsr<f32x4>(ra + 16);
      const f32x4 b0 = ldsr<f32x4>(ra + 34816), b1 = ldsr<f32x4>(ra + 34816 + 16);
#pragma unroll
      for (int j = 0; j < 4; ++j) {
        const float lo = (j < 2 ? a0[2 * j] + b0[2 * j] : a1[2 * j - 4] + b1[2 * j - 4]);
        const float hi = (j < 2 ? a0[2 * j + 1] + b0[2 * j + 1] : a1[2 * j - 3] + b1[2 * j - 3]);
        const float gl = j < 2 ? ga0[2 * j] : ga1[2 * j - 4], gh = j < 2 ? ga0[2 * j + 1] : ga1[2 * j - 3];
        const unsigned w = pack2(fmaf(gl, lo, bflo(xr[i][j])), fmaf(gh, hi, bfhi(xr[i][j])));
        xv[i][2 * j] = bflo(w); xv[i][2 * j + 1] = bfhi(w);
      }
      sm[i] = sum8(((xv[i][0] + xv[i][1]) + (xv[i][2] + xv[i][3])) + ((xv[i][4] + xv[i][5]) + (xv[i][6] + xv[i][7]))) * (1.f / 64);
      float q = 0.f, d = 0.f;
#pragma unroll
      for (int j = 0; j < 8; ++j) {
        const float e = xv[i][j] - sm[i];
        q = fmaf(e, e, q);
        d = fmaf(e, j < 4 ? gw0[j] : gw1[j - 4], d);
      }
      sq[i] = sum8(q); sd[i] = sum8(d);
    }
    if (ec < 2) {
      const u32x4 rec = {__float_as_uint(ec ? sm[1] : sm[0]) | 1u, __float_as_uint(ec ? sq[1] : sq[0]),
                         __float_as_uint(ec ? sd[1] : sd[0]) | 1u, 0u};
      st16(sync + SY_ST + ((size_t)(mt * 16 + nt) * 128 + erow + 64 * ec) * 2, rec);
    }
  }
  GT(9);
  {
    const int r16 = lane & 15, sg = lane >> 4;
    const unsigned vo = (unsigned)(sg * 8192 + (wid * 16 + r16) * 16), vg = (unsigned)((SY_GC + mt * 16) * 8 + sg * 32);
    const unsigned long long* sb = sync + SY_ST + (size_t)mt * 4096;
    u32x4 c[4], g[2];
    __builtin_amdgcn_s_sleep(HD1);
    _Pragma("nounroll") for (unsigned spin = 0; spin < SPIN_CAP; ++spin) {
      GLOADC(c[0], vo, sb, 0); GLOADC(c[1], vo, sb, 2048); GLOADC(c[2], vo, sb + 512, 0); GLOADC(c[3], vo, sb + 512, 2048);
      GLOADC(g[0], vg, sync, 0); GLOADC(g[1], vg, sync, 16);
      asm volatile("s_waitcnt vmcnt(0)" : "+v"(c[0]), "+v"(c[1]), "+v"(c[2]), "+v"(c[3]), "+v"(g[0]), "+v"(g[1])::"memory");
      const unsigned m = (c[0][0] & c[0][2]) & (c[1][0] & c[1][2]) & (c[2][0] & c[2][2]) & (c[3][0] & c[3][2]) &
                         (g[0][0] & g[0][2]) & (g[1][0] & g[1][2]);
      if (__all(m & 1u)) break;
      __builtin_amdgcn_s_sleep(4);
    }
    GT(10);
#define F(x_) __uint_as_float(x_)
    const float mean = sumrows((F(c[0][0]) + F(c[1][0])) + (F(c[2][0]) + F(c[3][0]))) * (1.f / 16);
    float q = 0.f, d = 0.f;
#pragma unroll
    for (int i = 0; i < 4; ++i) {
      const float e = F(c[i][0]) - mean;
      q += fmaf(64.f * e, e, F(c[i][1]));
      d += fmaf(e, F(g[i >> 1][(i & 1) * 2]), F(c[i][2]));
    }
    const float cs = sumrows((F(g[0][1]) + F(g[0][3])) + (F(g[1][1]) + F(g[1][3])));
#undef F
    const float rstd = __builtin_amdgcn_rsqf(sumrows(q) * (1.f / D) + 1e-5f);
    const float rr = fmaf(rstd, sumrows(d), cs);
    const unsigned key = okey(rr), kmax = rowmaxu(key);
    const unsigned rbest = ~rowmaxu(key == kmax ? ~(unsigned)(wid * 16 + r16) : 0u);
    if (lane < 16) ldsw(lds0 + K4_ROW + (wid * 16 + r16) * 16, (f32x4){mean, rstd, rr, 0.f});
    if (lane == 0) ldsw(lds0 + K4_BEST + wid * 8, (u32x2){kmax, rbest});
  }
  LDS_BARRIER();
  GT(11);
  unsigned bkey, brow;
  {
    const u32x4 e0 = ldsr<u32x4>(lds0 + K4_BEST), e1 = ldsr<u32x4>(lds0 + K4_BEST + 16);
    const u32x4 e2 = ldsr<u32x4>(lds0 + K4_BEST + 32), e3 = ldsr<u32x4>(lds0 + K4_BEST + 48);
    const unsigned kk[8] = {e0[0], e0[2], e1[0], e1[2], e2[0], e2[2], e3[0], e3[2]};
    const unsigned rs[8] = {e0[1], e0[3], e1[1], e1[3], e2[1], e2[3], e3[1], e3[3]};
    bkey = kk[0]; brow = rs[0];
#pragma unroll
    for (int w = 1; w < 8; ++w) { const bool tk = kk[w] > bkey; bkey = tk ? kk[w] : bkey; brow = tk ? rs[w] : brow; }
  }
  {
    const unsigned va = lds0 + K4_VEC + ec * 32;
    const f32x4 g0 = ldsr<f32x4>(va + 256), g1 = ldsr<f32x4>(va + 256 + 16);
    const f32x4 s0 = ldsr<f32x4>(va + 512), s1 = ldsr<f32x4>(va + 512 + 16);
    const f32x4 b0 = ldsr<f32x4>(va + 768), b1 = ldsr<f32x4>(va + 768 + 16);
    u32x4 hw[2];
#pragma unroll
    for (int i = 0; i < 2; ++i) {
      const f32x2 ms = ldsr<f32x2>(lds0 + K4_ROW + (erow + 64 * i) * 16);
#pragma unroll
      for (int j = 0; j < 4; ++j) {
        const float n0 = (xv[i][2 * j] - ms[0]) * ms[1], n1 = (xv[i][2 * j + 1] - ms[0]) * ms[1];
        hw[i][j] = pack2(fmaf(n0, j < 2 ? g0[2 * j] : g1[2 * j - 4], j < 2 ? s0[2 * j] : s1[2 * j - 4]),
                         fmaf(n1, j < 2 ? g0[2 * j + 1] : g1[2 * j - 3], j < 2 ? s0[2 * j + 1] : s1[2 * j - 3]));
      }
    }
    if (erow == (int)(brow & 63u)) {
      const bool hi = brow >= 64u;
      const u32x4 hc = hi ? hw[1] : hw[0];
      unsigned long long* hp = sync + SY_HC + mt * 512 + nt * 32 + ec * 4;
      st16(hp, (u32x4){hc[0], bkey | 1u, hc[1], bkey | 1u});
      st16(hp + 2, (u32x4){hc[2], bkey | 1u, hc[3], bkey | 1u});
      float xc[8];
#pragma unroll
      for (int j = 0; j < 8; ++j) xc[j] = hi ? xv[1][j] : xv[0][j];
      ldsw(lds0 + K4_FIN + ec * 32, (f32x4){xc[0], xc[1], xc[2], xc[3]});
      ldsw(lds0 + K4_FIN + ec * 32 + 16, (f32x4){xc[4], xc[5], xc[6], xc[7]});
      ldsw(lds0 + K4_FIN + 256 + ec * 32, (f32x4){bflo(hc[0]), bfhi(hc[0]), bflo(hc[1]), bfhi(hc[1])});
      ldsw(lds0 + K4_FIN + 256 + ec * 32 + 16, (f32x4){bflo(hc[2]), bfhi(hc[2]), bflo(hc[3]), bfhi(hc[3])});
    }
#pragma unroll
    for (int i = 0; i < 2; ++i) {
      const int row = erow + 64 * i;
      u32x4 o;
#pragma unroll
      for (int j = 0; j < 4; ++j)
        o[j] = pack2(fmaf(j < 2 ? b0[2 * j] : b1[2 * j - 4], bflo(hw[i][j]), xv[i][2 * j]),
                     fmaf(j < 2 ? b0[2 * j + 1] : b1[2 * j - 3], bfhi(hw[i][j]), xv[i][2 * j + 1]));
      if (row != (int)brow) st16(out + (size_t)(mt * 128 + row) * D + nt * 64 + ec * 8, o);
    }
  }

  if (wid > 4) {
    asm volatile("s_waitcnt lgkmcnt(0)" ::: "memory");
    PROF_END(3);
    return;
  }
  gu64* gp = (gu64*)sync + SY_GP;
  gu64* gy = (gu64*)sync + SY_GY;
  if (wid == 4) {
    int win;
    {
      const unsigned vo = (unsigned)(ks * 1024 + lane * 16);
      const unsigned long long* hb = sync + SY_HC;
      u32x4 c[16];
      __builtin_amdgcn_s_sleep(HD2);
      _Pragma("nounroll") for (unsigned spin = 0; spin < SPIN_CAP; ++spin) {
#pragma unroll
        for (int i = 0; i < 16; ++i) GLOADC(c[i], vo, hb + i * 512, 0);
        asm volatile("s_waitcnt vmcnt(0)"
                     : "+v"(c[0]), "+v"(c[1]), "+v"(c[2]), "+v"(c[3]), "+v"(c[4]), "+v"(c[5]), "+v"(c[6]), "+v"(c[7]),
                       "+v"(c[8]), "+v"(c[9]), "+v"(c[10]), "+v"(c[11]), "+v"(c[12]), "+v"(c[13]), "+v"(c[14]), "+v"(c[15])::"memory");
        unsigned m = 1u;
#pragma unroll
        for (int i = 0; i < 16; ++i) m &= c[i][1] & c[i][3];
        if (__all(m & 1u)) break;
        __builtin_amdgcn_s_sleep(4);
      }
      GT(12);
      unsigned k0 = c[0][1], h0 = c[0][0], h1 = c[0][2];
      win = 0;
#pragma unroll
      for (int i = 1; i < 16; ++i) {
        const bool tk = c[i][1] > k0;
        k0 = tk ? c[i][1] : k0; h0 = tk ? c[i][0] : h0; h1 = tk ? c[i][2] : h1; win = tk ? i : win;
      }
      ldsw(lds0 + K4_H + lane * 16, (f32x4){bflo(h0), bfhi(h0), bflo(h1), bfhi(h1)});
      win = __builtin_amdgcn_readfirstlane(win);
    }
    LDS_BARRIER();
    const f32x2 fx = ldsr<f32x2>(lds0 + K4_FIN + (lane & 31) * 8), fg = ldsr<f32x2>(lds0 + K4_VEC + 768 + (lane & 31) * 8);
    const f32x2 fh = ldsr<f32x2>(lds0 + K4_FIN + 256 + (lane & 31) * 8);
    const float fsig = rbf(1.f / (1.f + __expf(-rbf(ldsr<float>(lds0 + K4_ROW + brow * 16 + 8)))));
    unsigned* orow = (unsigned*)(out + (size_t)(mt * 128 + brow) * D + nt * 64 + (lane & 31) * 2);
    if (win != mt && lane < 32) *orow = pack2(fmaf(fg[0], fh[0], fx[0]), fmaf(fg[1], fh[1], fx[1]));
    LDS_BARRIER();
    if (has_gu) {
      float sg = 0.f, su = 0.f;
#pragma unroll
      for (int w = 0; w < 4; ++w) {
        sg += ldsr<float>(lds0 + K4_RED + w * 512 + lane * 4);
        su += ldsr<float>(lds0 + K4_RED + w * 512 + 256 + lane * 4);
      }
      __hip_atomic_store(gp + ks * FF + cg * 64 + lane, mk64(__float_as_uint(sg) | 1u, __float_as_uint(su)), RLX_AGENT);
    }

    {
      const unsigned v0 = (unsigned)(dks * 1792 + lane * 16), v1 = (unsigned)(dks * 1792 + (lane < 48 ? lane + 64 : 111) * 16);
      const unsigned long long* gb = sync + SY_GP;
      u32x4 c[8];
      __builtin_amdgcn_s_sleep(HD3);
      _Pragma("nounroll") for (unsigned spin = 0; spin < SPIN_CAP; ++spin) {
#pragma unroll
        for (int i = 0; i < 4; ++i) { GLOADC(c[i], v0, gb + i * FF, 0); GLOADC(c[4 + i], v1, gb + i * FF, 0); }
        asm volatile("s_waitcnt vmcnt(0)"
                     : "+v"(c[0]), "+v"(c[1]), "+v"(c[2]), "+v"(c[3]), "+v"(c[4]), "+v"(c[5]), "+v"(c[6]), "+v"(c[7])::"memory");
        unsigned m = 1u;
#pragma unroll
        for (int i = 0; i < 8; ++i) m &= c[i][0] & c[i][2];
        if (__all(m & 1u)) break;
        __builtin_amdgcn_s_sleep(4);
      }
      GT(13);
#pragma unroll
      for (int r = 0; r < 2; ++r) {
        float a[2];
#pragma unroll
        for (int e = 0; e < 2; ++e) {
          float sg = 0.f, su = 0.f;
#pragma unroll
          for (int i = 0; i < 4; ++i) { sg += __uint_as_float(c[r * 4 + i][2 * e]); su += __uint_as_float(c[r * 4 + i][2 * e + 1]); }
          sg = rbf(sg); su = rbf(su);
          a[e] = rbf(rbf(sg / (1.f + __expf(-sg))) * su);
        }
        ldsw(lds0 + K4_A + (r * 64 + lane) * 8, (f32x2){a[0], a[1]});
      }
    }
    LDS_BARRIER();
    asm volatile("s_barrier" ::: "memory");
    if (lane < 32) {
      f32x2 y = (f32x2){0.f, 0.f};
#pragma unroll
      for (int w = 0; w < 4; ++w) y += ldsr<f32x2>(lds0 + K4_RED2 + w * 256 + lane * 8);
      __hip_atomic_store(gy + dks * 512 + dcg * 32 + lane, mk64(__float_as_uint(y[0]) | 1u, __float_as_uint(y[1])), RLX_AGENT);
    }
    TS(14);
    if (win == mt) {
      unsigned long long c[8];
      __builtin_amdgcn_s_sleep(HD4);
      _Pragma("nounroll") for (unsigned spin = 0; spin < SPIN_CAP; ++spin) {
        bool ok = true;
#pragma unroll
        for (int i = 0; i < 8; ++i) {
          c[i] = __hip_atomic_load(gy + ((lane >> 5) * 8 + i) * 512 + nt * 32 + (lane & 31), RLX_AGENT);
          ok &= (c[i] & 1ull) != 0;
        }
        if (__all(ok)) break;
        __builtin_amdgcn_s_sleep(4);
      }
      float y0 = 0.f, y1 = 0.f;
#pragma unroll
      for (int i = 0; i < 8; ++i) { y0 += __uint_as_float((unsigned)c[i]); y1 += __uint_as_float((unsigned)(c[i] >> 32)); }
      y0 = swap32_add(y0, y0); y1 = swap32_add(y1, y1);
      y0 = rbf(rbf(y0) * fsig); y1 = rbf(rbf(y1) * fsig);
      if (lane < 32) *orow = pack2(fmaf(fg[0], rbf(fh[0] + y0), fx[0]), fmaf(fg[1], rbf(fh[1] + y1), fx[1]));
    }
    TS(15);
    TS_END(3, 4);
    PROF_END(3);
    return;
  }
  {
    f32x2 wf[16][4];
#pragma unroll
    for (int i = 0; i < 16; ++i)
#pragma unroll
      for (int j = 0; j < 4; ++j) { wf[i][j] = up2(wgu[i][j]); asm volatile("" : "+v"(wf[i][j])); }
    LDS_BARRIER();
    f32x2 ag[4], au[4];
#pragma unroll
    for (int i = 0; i < 8; ++i) {
      const float h = ldsr<float>(lds0 + K4_H + (erow + 32 * i) * 4);
#pragma unroll
      for (int j = 0; j < 4; ++j) {
        ag[j] = i == 0 ? wf[0][j] * h : wf[i][j] * h + ag[j];
        au[j] = i == 0 ? wf[8][j] * h : wf[8 + i][j] * h + au[j];
      }
    }
    float v8[8], v4[4];
#pragma unroll
    for (int j = 0; j < 8; ++j) v8[j] = swap32_add(ag[j >> 1][j & 1], au[j >> 1][j & 1]);
#pragma unroll
    for (int j = 0; j < 4; ++j) v4[j] = dpp_add<0x128>(swap16_add(v8[j], v8[4 + j]));
    if ((lane & 8) == 0)
      ldsw(lds0 + K4_RED + wid * 512 + ((lane >> 5) * 64 + (lane & 7) * 8 + ((lane >> 4) & 1) * 4) * 4,
           (f32x4){v4[0], v4[1], v4[2], v4[3]});
  }
  LDS_BARRIER();
  {
    f32x2 wdf[7][4];
#pragma unroll
    for (int i = 0; i < 7; ++i)
#pragma unroll
      for (int j = 0; j < 4; ++j) { wdf[i][j] = up2(wdn[i][j]); asm volatile("" : "+v"(wdf[i][j])); }
    asm volatile("s_barrier" ::: "memory");
    f32x2 a[4];
#pragma unroll
    for (int i = 0; i < 7; ++i) {
      const float h = ldsr<float>(lds0 + K4_A + (erow + 32 * i) * 4);
#pragma unroll
      for (int j = 0; j < 4; ++j) a[j] = i == 0 ? wdf[0][j] * h : wdf[i][j] * h + a[j];
    }
    float v4[4], v2[2];
#pragma unroll
    for (int j = 0; j < 4; ++j) v4[j] = swap32_add(a[j >> 1][j & 1], a[2 + (j >> 1)][j & 1]);
#pragma unroll
    for (int j = 0; j < 2; ++j) v2[j] = dpp_add<0x128>(swap16_add(v4[j], v4[2 + j]));
    if ((lane & 8) == 0)
      ldsw(lds0 + K4_RED2 + wid * 256 + ((lane & 7) * 8 + (lane >> 5) * 4 + ((lane >> 4) & 1) * 2) * 4, (f32x2){v2[0], v2[1]});
  }
  LDS_BARRIER();
  TS_END(4, 0);
  PROF_END(3);
}

extern "C" void kernel_launch(void* const* d_in, const int* in_sizes, int n_in, void* d_out, int out_size, void* d_ws,
                              size_t ws_size, hipStream_t stream) {
  const u16* x = (const u16*)d_in[0];
  const u16* wqkv = (const u16*)d_in[1];
  const u16* wo = (const u16*)d_in[2];
  const u16* wr = (const u16*)d_in[4];
  const u16* wg = (const u16*)d_in[5];
  const u16* wu = (const u16*)d_in[6];
  const u16* wd = (const u16*)d_in[7];
  const u16* c = (const u16*)d_in[8];
  const u16* wmod = (const u16*)d_in[9];
  char* ws = (char*)d_ws;
  float* modp = (float*)(ws + WS_MODP);
  u16* nbuf = (u16*)(ws + WS_N);
  u16* kvf = (u16*)(ws + WS_K);
  u16* of = (u16*)(ws + WS_O);
  u16* out = (u16*)d_out;
  (void)hipFuncSetAttribute((const void*)k2_main, hipFuncAttributeMaxDynamicSharedMemorySize, K3_LDS);
  k1_mod_ln<<<256, 512, 65536, stream>>>(c, wmod, x, modp, nbuf, (u32x4*)(ws + WS_SYNC), wqkv);
  k2_main<<<256, 512, K3_LDS, stream>>>(nbuf, wqkv, modp, kvf, of, wo, x, wr, wg, wu, wd, out, (unsigned long long*)(ws + WS_SYNC));
}
```

```cpp
#include <hip/hip_runtime.h>
#include <hip/hip_bf16.h>
#include <stdint.h>

typedef unsigned short u16;
using bf16x8 = __attribute__((ext_vector_type(8))) short;
using s16x4 = __attribute__((ext_vector_type(4))) short;
using f32x16 = __attribute__((ext_vector_type(16))) float;
using f32x4 = __attribute__((ext_vector_type(4))) float;
using f32x2 = __attribute__((ext_vector_type(2))) float;
using u32x4 = __attribute__((ext_vector_type(4))) unsigned;
using u32x2 = __attribute__((ext_vector_type(2))) unsigned;
typedef __bf16 bf16x2_t __attribute__((ext_vector_type(2)));
#define LAS __attribute__((address_space(3)))
#define DI __device__ __forceinline__

constexpr int S = 2048, D = 1024, NH = 32, FF = 3584;
constexpr float QSCALE = 0.17677669529663687f * 1.4426950408889634f;

constexpr size_t WS_MODP = 0;
constexpr size_t WS_MODF = 98304;
constexpr size_t WS_SYNC = 131072;
constexpr int SY_ST = 0, SY_GC = 65536, SY_HC = SY_GC + 256, SY_GP = SY_HC + 8192, SY_GY = SY_GP + 4 * 3584,
              SY_OC = SY_GY + 8192, SY_KV = SY_OC + 64 * 16, SY_END = SY_KV + 128 * 16;
constexpr int SYNC_PER_BLOCK = (SY_END / 2 + 255) / 256;
static_assert(WS_SYNC + 256 * SYNC_PER_BLOCK * 16 <= (1u << 20), "sync area");
constexpr size_t WS_Q = 1u << 20;
constexpr size_t WS_K = WS_Q + (4u << 20);
constexpr size_t WS_V = WS_K + (4u << 20);
constexpr size_t WS_O = WS_V + (4u << 20);
constexpr size_t WS_X1 = WS_O + (4u << 20);
constexpr size_t WS_N = WS_X1 + (4u << 20);

#define PROF_BEGIN()
#define PROF_END(kid)
#define TS_DECL()
#define TS(i)
#define TS_END(kid, w)

#define TR(base, w, kt, i)
#define TR_FLUSH(base, w)
#define TR_INIT(base, w)

DI float bf2f(u16 v) { return __uint_as_float((unsigned)v << 16); }
DI float bflo(unsigned w) { return __uint_as_float(w << 16); }
DI float bfhi(unsigned w) { return __uint_as_float(w & 0xffff0000u); }
DI unsigned pack2(float lo, float hi) {
  f32x2 v = {lo, hi};
  bf16x2_t b = __builtin_convertvector(v, bf16x2_t);
  return __builtin_bit_cast(unsigned, b);
}
DI float rbf(float x) { return bflo(pack2(x, 0.f)); }
#define MFMA32(a, b, c) __builtin_amdgcn_mfma_f32_32x32x16_bf16((a), (b), (c), 0, 0, 0)
#define MFMA16(a, b, c) __builtin_amdgcn_mfma_f32_16x16x32_bf16((a), (b), (c), 0, 0, 0)
#define SBAR() __builtin_amdgcn_sched_barrier(0)
#define LDS_BARRIER() asm volatile("s_waitcnt lgkmcnt(0)\n\ts_barrier" ::: "memory")

DI float wave_sum(float v) {
#pragma unroll
  for (int o = 32; o > 0; o >>= 1) v += __shfl_xor(v, o);
  return v;
}
template <int CTRL>
DI float dpp_add(float v) {
  return v + __uint_as_float(__builtin_amdgcn_update_dpp(0u, __float_as_uint(v), CTRL, 0xf, 0xf, true));
}
DI float swap32_add(float a, float b) {
  auto r = __builtin_amdgcn_permlane32_swap(__float_as_uint(a), __float_as_uint(b), false, false);
  return __uint_as_float(r[0]) + __uint_as_float(r[1]);
}
DI float swap16_add(float a, float b) {
  auto r = __builtin_amdgcn_permlane16_swap(__float_as_uint(a), __float_as_uint(b), false, false);
  return __uint_as_float(r[0]) + __uint_as_float(r[1]);
}
DI float wsum(float v) {
  v = dpp_add<0xB1>(v);
  v = dpp_add<0x4E>(v);
  v = dpp_add<0x141>(v);
  v = dpp_add<0x140>(v);
  v = swap16_add(v, v);
  return swap32_add(v, v);
}
DI unsigned ln_pair(float lo, float hi) {
  const unsigned l = pack2(lo, 0.f), b = __float_as_uint(hi);
  const int t = max((int)(b & 0x7fffffffu) - (int)l + 0x8000, 0);
  return (b & 0x80000000u) | ((unsigned)t & 0x7fff0000u) | l;
}
DI void glds16(const void* sbase, unsigned voff, unsigned lds_dst) {
  unsigned keep;
  asm volatile("s_mov_b32 %0, m0\n\ts_mov_b32 m0, %3\n\ts_nop 0\n\tglobal_load_lds_dwordx4 %1, %2\n\ts_mov_b32 m0, %0"
               : "=&s"(keep)
               : "v"(voff), "s"(sbase), "s"(lds_dst)
               : "memory");
}
DI void st16(void* p, u32x4 v) {
  asm volatile("s_nop 0\n\tglobal_store_dwordx4 %0, %1, off sc1\n\ts_nop 1" ::"v"(p), "v"(v) : "memory");
}
DI bf16x8 lds_b128(unsigned addr) { return *(const LAS bf16x8*)(uintptr_t)addr; }
typedef short v4i16_t __attribute__((ext_vector_type(4)));
DI s16x4 lds_tr(unsigned addr) {
  return __builtin_bit_cast(s16x4, __builtin_amdgcn_ds_read_tr16_b64_v4i16((LAS v4i16_t*)(uintptr_t)addr));
}
DI bf16x8 cat8(s16x4 lo, s16x4 hi) { return __builtin_shufflevector(lo, hi, 0, 1, 2, 3, 4, 5, 6, 7); }

#define WPFPOL ""
__global__ __launch_bounds__(512) void k1_mod_ln(const u16* __restrict__ c, const u16* __restrict__ wmod,
                                                 const u16* __restrict__ x, float* __restrict__ modp,
                                                 u16* __restrict__ nbuf, u32x4* __restrict__ sync,
                                                 const u16* __restrict__ wqkv) {
  PROF_BEGIN();
  extern __shared__ __attribute__((aligned(16))) char smem[];
  float (*red)[64] = (float (*)[64])smem;
  const int t = threadIdx.x, lane = t & 63, wid = t >> 6, bid = blockIdx.x;
  if (t < SYNC_PER_BLOCK) st16(sync + bid * SYNC_PER_BLOCK + t, (u32x4){0u, 0u, 0u, 0u});
  if (bid < 192) {
    const int cg = bid % 96, kh = bid / 96;
    const int c8 = t & 7, rg = t >> 3;
    u32x4 w[8];
    float sc[8];
#pragma unroll
    for (int i = 0; i < 8; ++i) {
      const int k = kh * 512 + rg + 64 * i;
      w[i] = __builtin_nontemporal_load((const u32x4*)(wmod + (size_t)k * 6144 + cg * 64 + c8 * 8));
      const float cv = bf2f(c[k]);
      sc[i] = rbf(cv / (1.f + __expf(-cv)));
    }
    float acc[8] = {0.f, 0.f, 0.f, 0.f, 0.f, 0.f, 0.f, 0.f};
#pragma unroll
    for (int i = 0; i < 8; ++i)
#pragma unroll
      for (int j = 0; j < 4; ++j) {
        acc[2 * j] = fmaf(sc[i], bflo(w[i][j]), acc[2 * j]);
        acc[2 * j + 1] = fmaf(sc[i], bfhi(w[i][j]), acc[2 * j + 1]);
      }
#pragma unroll
    for (int j = 0; j < 8; ++j) {
      acc[j] += __shfl_xor(acc[j], 8);
      acc[j] += __shfl_xor(acc[j], 16);
      acc[j] += __shfl_xor(acc[j], 32);
    }
    if (lane < 8) {
#pragma unroll
      for (int j = 0; j < 8; ++j) red[wid][lane * 8 + j] = acc[j];
    }
    __syncthreads();
    if (t < 64) {
      float s = 0.f;
#pragma unroll
      for (int w8 = 0; w8 < 8; ++w8) s += red[w8][t];
      modp[kh * 6144 + cg * 64 + t] = s;
    }
  } else {
    const int r0 = (bid - 192) * 32 + wid * 4;
    u32x4 a[4], b[4];
#pragma unroll
    for (int i = 0; i < 4; ++i) {
      a[i] = *(const u32x4*)(x + (size_t)(r0 + i) * D + lane * 8);
      b[i] = *(const u32x4*)(x + (size_t)(r0 + i) * D + 512 + lane * 8);
    }
#pragma unroll
    for (int i = 0; i < 4; ++i) {
      float v[16];
#pragma unroll
      for (int j = 0; j < 4; ++j) {
        v[2 * j] = bflo(a[i][j]); v[2 * j + 1] = bfhi(a[i][j]);
        v[8 + 2 * j] = bflo(b[i][j]); v[8 + 2 * j + 1] = bfhi(b[i][j]);
      }
      float s = 0.f;
#pragma unroll
      for (int j = 0; j < 16; ++j) s += v[j];
      const float mean = wsum(s) * (1.f / D);
      float q = 0.f;
#pragma unroll
      for (int j = 0; j < 16; ++j) { v[j] -= mean; q = fmaf(v[j], v[j], q); }
      const float rstd = __builtin_amdgcn_rsqf(wsum(q) * (1.f / D) + 1e-5f);
      u32x4 o0, o1;
#pragma unroll
      for (int j = 0; j < 4; ++j) {
        o0[j] = ln_pair(v[2 * j] * rstd, v[2 * j + 1] * rstd);
        o1[j] = ln_pair(v[8 + 2 * j] * rstd, v[8 + 2 * j + 1] * rstd);
      }
      const int sw = ((wid * 4 + i) ^ (lane & 31)) * 16;
      *(u32x4*)(smem + lane * 512 + sw) = o0;
      *(u32x4*)(smem + (64 + lane) * 512 + sw) = o1;
    }
    __syncthreads();
#pragma unroll
    for (int i = 0; i < 8; ++i) {
      const int c = wid * 16 + i * 2 + (lane >> 5);
      const u32x4 v = *(const u32x4*)(smem + c * 512 + (((lane ^ c) & 31) * 16));
      st16(nbuf + (size_t)(bid - 192) * 32768 + (wid * 8 + i) * 512 + lane * 8, v);
    }
  }
  {
    const u16* wp = wqkv + (size_t)((bid >> 3) * 32 + wid * 4 + (lane >> 5)) * 3072 + ((bid & 7) >> 1) * 256 + (lane & 31) * 8;
    u32x4 pf[6];
#pragma unroll
    for (int i = 0; i < 6; ++i)
      asm volatile("global_load_dwordx4 %0, %1, off" WPFPOL : "=v"(pf[i]) : "v"(wp + (i & 1) * 2 * 3072 + (i >> 1) * 1024) : "memory");
    asm volatile("s_waitcnt vmcnt(0)" : "+v"(pf[0]), "+v"(pf[1]), "+v"(pf[2]), "+v"(pf[3]), "+v"(pf[4]), "+v"(pf[5])::"memory");
  }
  PROF_END(1);
}

constexpr int K2_NB = 6, K2_BSLOT = 24576, K2_DW = 3, K2_DN = 6;
constexpr int K2_LDS_GS = K2_NB * K2_BSLOT, K2_LDS_TR = K2_LDS_GS + 8192, K2_LDS = K2_LDS_TR + 2048;

#define GLOAD16(dst, voff, sbase) \
  asm volatile("global_load_dwordx4 %0, %1, %2" : "=v"(dst) : "v"(voff), "s"(sbase) : "memory")

#define GLOAD16NT(dst, voff, sbase) \
  asm volatile("global_load_dwordx4 %0, %1, %2 nt" : "=v"(dst) : "v"(voff), "s"(sbase) : "memory")

#define K2_TP0 16
#define K2_TN 2
#define K2_TS 2
constexpr unsigned POISON = 0xffffffffu;
constexpr int k2_touch(int p) { return p >= K2_TP0 && (p - K2_TP0) % K2_TS == 0 && (p - K2_TP0) / K2_TS < K2_TN ? (p - K2_TP0) / K2_TS : -1; }
struct K2Sched { int wn[33]; int ww[17]; };
constexpr int k2_younger(const int* seq, int n, int id) {
  int c = 0;
  for (int i = n - 1; i >= 0 && seq[i] != id; --i) ++c;
  return c;
}
constexpr K2Sched k2_make() {
  K2Sched s{};
  int seq[128] = {};
  int n = 0;
  seq[n++] = 200; seq[n++] = 201;
  for (int i = 0; i < 4; ++i) {
    if (i == 0) seq[n++] = 0;
    if (i < K2_DW) { seq[n++] = 100 + 3 * i; seq[n++] = 101 + 3 * i; seq[n++] = 102 + 3 * i; }
    if (2 * i + 1 < K2_DN) seq[n++] = 2 * i + 1;
    if (2 * i + 2 < K2_DN) seq[n++] = 2 * i + 2;
  }
  s.wn[32] = k2_younger(seq, n, 201);
  s.ww[16] = k2_younger(seq, n, 102);
  seq[n++] = K2_DN;
  for (int p = 0; p < 32; ++p) {
    const int t = p >> 1, j = p & 1;
    if (p + 1 <= 31) s.wn[p] = k2_younger(seq, n, p + 1);
    if (j == 1 && t + 1 <= 15) s.ww[t] = k2_younger(seq, n, 100 + 3 * (t + 1) + 2);
    if (p + 1 + K2_DN <= 31) seq[n++] = p + 1 + K2_DN;
    if (t + K2_DW <= 15) {
      if (j == 0) { seq[n++] = 100 + 3 * (t + K2_DW); seq[n++] = 101 + 3 * (t + K2_DW); }
      else seq[n++] = 102 + 3 * (t + K2_DW);
    }
    if (k2_touch(p) >= 0) seq[n++] = 300 + p;
  }
  return s;
}
constexpr K2Sched K2S = k2_make();

#define K2_WPOL ""
template <int KT, int PC>
DI void k2_issue_b(const u16* wq, unsigned wvoff, unsigned lds_dst) {
  if constexpr (KT <= 15) {
    const u16* sb = wq + (size_t)KT * 64 * 3072 + (PC == 2 ? 2048 - 512 : 0);
    asm volatile("s_nop 0\n\tglobal_load_lds_dwordx4 %0, %1 offset:%2" K2_WPOL
                 :
                 : "v"(wvoff), "s"(sb), "i"(PC == 0 ? 0 : PC == 1 ? 2048 : 1024), "{m0}"(lds_dst + (KT % K2_NB) * K2_BSLOT)
                 : "memory");
  }
}
template <int F>
DI void k2_issue_n(const u16* nb, unsigned nvoff, u32x4& r) {
  if constexpr (F <= 31)
    asm volatile("global_load_dwordx4 %0, %1, %2 offset:%3" : "=v"(r) : "v"(nvoff), "s"(nb + (F >> 1) * 2048), "i"((F & 1) * 1024) : "memory");
}
struct K2GS { f32x4 g0, g1, s0, s1; };
DI K2GS k2_gs(unsigned gsa) {
  K2GS r;
  r.g0 = *(const LAS f32x4*)(uintptr_t)(gsa); r.g1 = *(const LAS f32x4*)(uintptr_t)(gsa + 16);
  r.s0 = *(const LAS f32x4*)(uintptr_t)(gsa + 4096); r.s1 = *(const LAS f32x4*)(uintptr_t)(gsa + 4096 + 16);
  return r;
}
template <int WAIT>
DI u32x4 k2_xform(u32x4& x, const K2GS& q) {
  asm volatile("s_waitcnt vmcnt(%1)" : "+v"(x) : "i"(WAIT) : "memory");
  unsigned t0, t1, t2, t3, o0, o1, o2, o3;
  asm volatile(
      "v_lshlrev_b32 %4, 16, %24\n\tv_lshlrev_b32 %5, 16, %25\n\tv_lshlrev_b32 %6, 16, %26\n\tv_lshlrev_b32 %7, 16, %27\n\t"
      "v_fma_f32 %0, %24, %9, %17\n\tv_fma_f32 %1, %25, %11, %19\n\tv_fma_f32 %2, %26, %13, %21\n\tv_fma_f32 %3, %27, %15, %23\n\t"
      "v_fma_f32 %4, %4, %8, %16\n\tv_fma_f32 %5, %5, %10, %18\n\tv_fma_f32 %6, %6, %12, %20\n\tv_fma_f32 %7, %7, %14, %22\n\t"
      "v_cvt_pk_bf16_f32 %0, %4, %0\n\tv_cvt_pk_bf16_f32 %1, %5, %1\n\tv_cvt_pk_bf16_f32 %2, %6, %2\n\tv_cvt_pk_bf16_f32 %3, %7, %3\n\t"
      "s_nop 1"
      : "=&v"(o0), "=&v"(o1), "=&v"(o2), "=&v"(o3), "=&v"(t0), "=&v"(t1), "=&v"(t2), "=&v"(t3)
      : "v"(q.g0[0]), "v"(q.g0[1]), "v"(q.g0[2]), "v"(q.g0[3]), "v"(q.g1[0]), "v"(q.g1[1]), "v"(q.g1[2]), "v"(q.g1[3]),
        "v"(q.s0[0]), "v"(q.s0[1]), "v"(q.s0[2]), "v"(q.s0[3]), "v"(q.s1[0]), "v"(q.s1[1]), "v"(q.s1[2]), "v"(q.s1[3]),
        "v"(x[0]), "v"(x[1]), "v"(x[2]), "v"(x[3]));
  return (u32x4){o0, o1, o2, o3};
}

struct K2State {
  f32x16 acc[2][3];
  bf16x8 fb[2][6];
  u32x4 nr[K2_DN];
  u32x4 hf[2];
  K2GS gs;
  const u16* ob;
};
template <int P>
DI bf16x8 k2_ldb(const unsigned (&bb)[2][2], int hd, int sg) {
  constexpr int slot = (P >> 1) % K2_NB;
  const unsigned a = bb[hd][slot / 3] + (slot % 3) * K2_BSLOT + (P & 1) * 6144 + (sg == 0 ? 0 : sg == 1 ? 2048 : 1024);
  return cat8(lds_tr(a), lds_tr(a + 512));
}
template <int P>
DI void k2_phase(K2State& st, const unsigned (&bb)[2][2], unsigned gsa, const u16* wq, unsigned wvoff, unsigned wdst,
                 const u16* nb, unsigned nvoff, unsigned lds0) {
  constexpr int C = P & 1, N = C ^ 1, T = P >> 1, J = P & 1;
  constexpr bool NEXT = P < 31;
  const bf16x8 hf = __builtin_bit_cast(bf16x8, st.hf[C]);
#define LDB2(I0) do { if constexpr (NEXT) { st.fb[N][I0] = k2_ldb<P + 1>(bb, (I0) / 3, (I0) % 3); \
                                            st.fb[N][I0 + 1] = k2_ldb<P + 1>(bb, (I0 + 1) / 3, (I0 + 1) % 3); } } while (0)
#define MM(I) do { SBAR();                                                                                   \
    if ((I) % 3 == 2) st.acc[(I) / 3][2] = MFMA32(hf, st.fb[C][I], st.acc[(I) / 3][2]);                      \
    else st.acc[(I) / 3][(I) % 3] = MFMA32(st.fb[C][I], hf, st.acc[(I) / 3][(I) % 3]);                       \
    SBAR(); } while (0)
  if constexpr (J == 0) TR(lds0 + K2_LDS_TR, 0, T, 0);
  MM(0);
  if constexpr (NEXT) st.hf[N] = k2_xform<K2S.wn[P]>(st.nr[(P + 1) % K2_DN], st.gs);
  MM(1);
  if constexpr (J == 1 && NEXT) {
    TR(lds0 + K2_LDS_TR, 0, T, 1);
    asm volatile("s_waitcnt vmcnt(%0)" ::"i"(K2S.ww[T]) : "memory");
    TR(lds0 + K2_LDS_TR, 0, T, 2);
    asm volatile("s_barrier" ::: "memory");
    TR(lds0 + K2_LDS_TR, 0, T, 3);
  }
  LDB2(0);
  k2_issue_n<P + 1 + K2_DN>(nb, nvoff, st.nr[(P + 1) % K2_DN]);
  MM(2);
  LDB2(2);
  k2_issue_b<T + K2_DW, J == 0 ? 0 : 2>(wq, wvoff, wdst);
  MM(3);
  LDB2(4);
  if constexpr (J == 0) k2_issue_b<T + K2_DW, 1>(wq, wvoff, wdst);
  MM(4);
  if constexpr (k2_touch(P) >= 0)
    asm volatile("s_nop 0\n\tglobal_store_dwordx4 %0, %1, %2 offset:%3 sc1\n\ts_nop 1"
                 :
                 : "v"(nvoff), "v"((u32x4){POISON, POISON, POISON, POISON}), "s"(st.ob), "i"(k2_touch(P) * 1024)
                 : "memory");
  if constexpr (P < 30) st.gs = k2_gs(gsa + ((P + 2) >> 1) * 256 + (P & 1) * 64);
  MM(5);
#undef LDB2
#undef MM
}
template <int P>
DI void k2_phases(K2State& st, const unsigned (&bb)[2][2], unsigned gsa, const u16* wq, unsigned wvoff, unsigned wdst,
                  const u16* nb, unsigned nvoff, unsigned lds0) {
  k2_phase<P>(st, bb, gsa, wq, wvoff, wdst, nb, nvoff, lds0);
  if constexpr (P < 31) k2_phases<P + 1>(st, bb, gsa, wq, wvoff, wdst, nb, nvoff, lds0);
}

#define GAS __attribute__((address_space(1)))
typedef GAS unsigned long long gu64;
typedef GAS unsigned gu32;
#define RLX_AGENT __ATOMIC_RELAXED, __HIP_MEMORY_SCOPE_AGENT
constexpr unsigned SPIN_CAP = 1u << 18;
DI unsigned long long mk64(unsigned lo, unsigned hi) { return ((unsigned long long)hi << 32) | lo; }
template <int CTRL>
DI unsigned dpp_maxu(unsigned v) {
  const unsigned o = __builtin_amdgcn_update_dpp(0u, v, CTRL, 0xf, 0xf, true);
  return v > o ? v : o;
}
DI unsigned rowmaxu(unsigned v) {
  v = dpp_maxu<0xB1>(v); v = dpp_maxu<0x4E>(v); v = dpp_maxu<0x141>(v); return dpp_maxu<0x140>(v);
}
DI float sum8(float v) { return dpp_add<0x141>(dpp_add<0x4E>(dpp_add<0xB1>(v))); }
DI float sumrows(float v) { v = swap16_add(v, v); return swap32_add(v, v); }
DI f32x2 up2(unsigned w) { return (f32x2){bflo(w), bfhi(w)}; }
DI unsigned okey(float v) {
  const unsigned raw = __float_as_uint(v);
  return v == v ? (raw ^ ((unsigned)((int)raw >> 31) | 0x80000000u)) : 0u;
}
#define GLOADC(dst, voff, sbase, off) \
  asm volatile("global_load_dwordx4 %0, %1, %2 offset:%3 sc1" : "=v"(dst) : "v"(voff), "s"(sbase), "i"(off) : "memory")
template <class T> DI T ldsr(unsigned a) { return *(const LAS T*)(uintptr_t)a; }
template <class T> DI void ldsw(unsigned a, T v) { *(LAS T*)(uintptr_t)a = v; }
DI float ex2(float x) { return __builtin_amdgcn_exp2f(x); }
DI float xhalf_max(float v) {
  auto rr = __builtin_amdgcn_permlane32_swap(__float_as_uint(v), __float_as_uint(v), false, false);
  return fmaxf(__uint_as_float(rr[0]), __uint_as_float(rr[1]));
}
DI void lds_arrive(unsigned a) {
  asm volatile("s_waitcnt lgkmcnt(0)\n\ts_mov_b64 exec, 1\n\tds_add_u32 %0, %1\n\ts_mov_b64 exec, -1" ::"v"(a), "v"(1u) : "memory");
}
DI unsigned lds_peek(unsigned a) { return __hip_atomic_load((LAS unsigned*)(uintptr_t)a, __ATOMIC_RELAXED, __HIP_MEMORY_SCOPE_WORKGROUP); }
DI void lds_await(unsigned a, unsigned n) {
  if (__builtin_expect(lds_peek(a) < n, 0)) {
    do __builtin_amdgcn_s_sleep(2); while (lds_peek(a) < n);
  }
  asm volatile("" ::: "memory");
}

#define K4_R 8
#define HD0 30
#define HD0R 8
#define HD1 16
#define HD2 HD1
#define HD3 HD1
#define HD4 HD1
#define HT(i)
#define FT(i)
#define GT(i) TS(i)
#define KVF_AT 1
#define HDK 10
#define HDKR 8
#define SH_D 4
#define SH_W1 8
#define SH_L1 4
#define VECW 0
#define SH_END 24
#define HD0B 10
constexpr int K4_SM = 153600;
constexpr int K4_ROW = K4_SM;
constexpr int K4_VEC = K4_ROW + 2048;
constexpr int K4_BEST = K4_VEC + 1280;
constexpr int K4_FIN = K4_BEST + 64;
constexpr int K4_H = K4_FIN + 512;
constexpr int K4_A = K4_H + 1024;
constexpr int K4_RED = K4_A + 1024;
constexpr int K4_RED2 = K4_RED + 2048;
constexpr int K3_CNT = K4_RED2 + 1024;
constexpr int K4_IMG = 0;
constexpr int K3_QX = 49152;
constexpr int K3_LDS = 163840, K3_PART = 4096 + 256;
static_assert(K4_VEC >= K2_LDS_GS + 8192 && K3_CNT + 64 <= K3_LDS && 12 * K3_PART <= 65536 && 98304 + 12 * K3_PART <= K4_SM, "LDS map");
constexpr int K3_DUMP = K3_LDS - 1024;
static_assert(K3_CNT + 64 <= K3_DUMP, "LDS map");

__global__ __launch_bounds__(512) void k2_main(const u16* __restrict__ nf, const u16* __restrict__ wqkv,
                                               const float* __restrict__ modp, u16* kvf, u16* of,
                                               const u16* __restrict__ wo, const u16* __restrict__ x,
                                               const u16* __restrict__ wr, const u16* __restrict__ wg,
                                               const u16* __restrict__ wu, const u16* __restrict__ wd,
                                               u16* __restrict__ out, unsigned long long* sync) {
  PROF_BEGIN();
  extern __shared__ __attribute__((aligned(16))) char smem[];
  const unsigned lds0 = (unsigned)(uintptr_t)smem;
  const int tid = threadIdx.x, lane = tid & 63;
  const int wid = __builtin_amdgcn_readfirstlane(tid >> 6);
  const int b = blockIdx.x, xcd = b & 7, jj = b >> 3;
  const int mt = (xcd & 1) * 8 + (jj & 7), nt = (xcd >> 1) * 4 + (jj >> 3);
  const int qg = wid >> 2, kq = wid & 3;
  const int ot = (kq + 3) & 3;
  const int kr = lane >> 3, pos = lane & 7;
  const bool has_gu = b < 224;
  const int cg = b % 56, ks = has_gu ? b / 56 : 0;
  const int dcg = b & 15, dks = b >> 4;
  asm volatile("" : "+s"(kvf), "+s"(of), "+s"(wo), "+s"(x), "+s"(wr), "+s"(wg), "+s"(wu), "+s"(wd), "+s"(out), "+s"(sync));
  TS_DECL(); TS(0);
  u32x4 fq[2], fk[2], fv[2];
  {
    const int wm = kq, kh = qg;
    K2State st;
    if (tid < 16) ldsw(lds0 + K3_CNT + tid * 4, 0u);
    st.ob = of + ((size_t)(((mt * 4 + ot) * 2 + qg) * 16 + nt) * 2) * 512;
    f32x4 mp0, mp1;
    {
      const unsigned vo = (tid & 255) * 16;
      const float* mb = modp + (kh == 0 ? 1024 : 0);
      GLOAD16(mp0, vo, mb);
      GLOAD16(mp1, vo, mb + 6144);
    }
    const unsigned wvoff = (unsigned)(((wid * 8 + kr) * 3072 + (((pos >> 2) ^ ((kr >> 1) & 1)) * 32) + (pos & 3) * 8) * 2);
    const u16* wq = wqkv + nt * 64;
    const unsigned wdst = lds0 + wid * 3072;
    const u16* nb = nf + (size_t)(mt * 4 + wm) * 32768 + kh * 1024;
    const unsigned nvoff = lane * 16;
#define WT(KT) do { if constexpr (KT < K2_DW) { k2_issue_b<KT, 0>(wq, wvoff, wdst); k2_issue_b<KT, 1>(wq, wvoff, wdst); \
                                                  k2_issue_b<KT, 2>(wq, wvoff, wdst); } } while (0)
#define NT(F) do { if constexpr (F < K2_DN) k2_issue_n<F>(nb, nvoff, st.nr[(F) % K2_DN]); } while (0)
    NT(0); WT(0); NT(1); NT(2); WT(1); NT(3); NT(4); WT(2); NT(5); NT(6); WT(3); NT(7); NT(8);
#undef WT
#undef NT
    TR(lds0 + K2_LDS_TR, 0, 16, 1);
    asm volatile("s_waitcnt vmcnt(%2)" : "+v"(mp0), "+v"(mp1) : "i"(K2S.wn[32]) : "memory");
    {
      f32x4 v;
#pragma unroll
      for (int j = 0; j < 4; ++j) {
        v[j] = rbf(mp0[j] + mp1[j]);
        if (kh == 0) v[j] = rbf(1.f + v[j]);
      }
      *(LAS f32x4*)(uintptr_t)(lds0 + K2_LDS_GS + tid * 16) = v;
    }
    const int h = lane >> 5, q4 = (lane & 15) >> 2, p4 = lane & 3, ch = (lane >> 4) & 1;
    unsigned bb[2][2];
#pragma unroll
    for (int hd = 0; hd < 2; ++hd) {
      bb[hd][0] = lds0 + kh * 12288 + h * 3072 + q4 * 128 + (((hd ^ kh) ^ ((q4 >> 1) & 1)) * 64) + ch * 32 + p4 * 8;
      bb[hd][1] = bb[hd][0] + 3 * K2_BSLOT;
      asm volatile("" : "+v"(bb[hd][0]), "+v"(bb[hd][1]));
    }
    unsigned gsa = lds0 + K2_LDS_GS + (32 * kh + 8 * h) * 4;
    asm volatile("" : "+v"(gsa));
#pragma unroll
    for (int a = 0; a < 2; ++a)
#pragma unroll
      for (int b = 0; b < 3; ++b) st.acc[a][b] = (f32x16){};
    asm volatile("s_waitcnt vmcnt(%0) lgkmcnt(0)\n\ts_barrier" ::"i"(K2S.ww[16]) : "memory");
    TR(lds0 + K2_LDS_TR, 0, 16, 2);
    TS(1);
    st.gs = k2_gs(gsa);
#pragma unroll
    for (int i = 0; i < 6; ++i) st.fb[0][i] = k2_ldb<0>(bb, i / 3, i % 3);
    st.hf[0] = k2_xform<K2S.ww[16]>(st.nr[0], st.gs);
    k2_issue_n<K2_DN>(nb, nvoff, st.nr[0]);
    st.gs = k2_gs(gsa + 64);
    k2_phases<0>(st, bb, gsa, wq, wvoff, wdst, nb, nvoff, lds0);
    TS(2);
    const unsigned ibo = lds0 + (qg == 1 ? 0 : 98304) + kq * 12288 + lane * 16;
    const unsigned ibm = lds0 + (qg == 0 ? 0 : 98304) + kq * 12288 + lane * 16;
#pragma unroll
    for (int sg = 0; sg < 3; ++sg)
#pragma unroll
      for (int g = 0; g < 4; ++g)
        ldsw(ibo + sg * 4096 + g * 1024,
             (f32x4){st.acc[1][sg][4 * g], st.acc[1][sg][4 * g + 1], st.acc[1][sg][4 * g + 2], st.acc[1][sg][4 * g + 3]});
    FT(9);
    LDS_BARRIER();
    FT(10);
    f32x4 in[12];
#pragma unroll
    for (int i = 0; i < 12; ++i) in[i] = ldsr<f32x4>(ibm + (i < 8 ? 4096 + i * 1024 : (i - 8) * 1024));
#pragma unroll
    for (int i = 0; i < 3; ++i) {
      const int sg = i < 2 ? i + 1 : 0;
      const float sc = sg == 0 ? QSCALE : 1.f;
#pragma unroll
      for (int s = 0; s < 2; ++s) {
        const f32x4 a = in[i * 4 + s * 2], c = in[i * 4 + s * 2 + 1];
        u32x4 o;
#pragma unroll
        for (int j = 0; j < 4; ++j) {
          const float lo = st.acc[0][sg][8 * s + 2 * j] + (j < 2 ? a[2 * j] : c[2 * j - 4]);
          const float hi = st.acc[0][sg][8 * s + 2 * j + 1] + (j < 2 ? a[2 * j + 1] : c[2 * j - 3]);
          o[j] = pack2(lo * sc, hi * sc);
        }
        if (sg == 0) { fq[s] = o; ldsw(lds0 + K3_QX + wid * 2048 + s * 1024 + lane * 16, o); }
        else {
          if (sg == 1) fk[s] = o; else fv[s] = o;
          st16(kvf + (size_t)(sg - 1) * (2u << 20) + ((size_t)((nt * 2 + qg) * 64 + mt * 4 + kq) * 2 + s) * 512 + lane * 8, o);
        }
      }
    }
  }
  u16* const op = of + ((size_t)(((mt * 4 + ot) * 2 + qg) * 16 + nt) * 2) * 512 + lane * 8;
  const int erow = tid >> 3, ec = tid & 7;
  u32x4 wgu[16], wdn[7];
  u32x4 xr[2];
  u32x4 ofr[16];
  const u16* const ob = of + (size_t)(mt * 4 + kq) * 32768 + qg * 8192;
#define OLOAD(F0, BASE)                                                                              \
  _Pragma("unroll") for (int f = (F0); f < 16; ++f)                                                  \
    asm volatile("global_load_dwordx4 %0, %1, %2 offset:%3 sc1"                                      \
                 : "+v"(ofr[f]) : "v"(lane * 16), "s"((BASE) + (f >> 2) * 2048), "i"((f & 3) * 1024) : "memory")
#define OL4(A, B, C, D, S)                                                                           \
  "global_load_dwordx4 %" #A ", %16, %" #S " sc1\n\tglobal_load_dwordx4 %" #B ", %16, %" #S " offset:1024 sc1\n\t" \
  "global_load_dwordx4 %" #C ", %16, %" #S " offset:2048 sc1\n\tglobal_load_dwordx4 %" #D ", %16, %" #S " offset:3072 sc1\n\t"
#define OLOAD_G(G)                                                                                   \
  asm volatile("s_cmp_lg_u32 %21, " #G "\n\ts_cbranch_scc1 1f\n\t"                                   \
               OL4(0, 1, 2, 3, 17) OL4(4, 5, 6, 7, 18) OL4(8, 9, 10, 11, 19) OL4(12, 13, 14, 15, 20) "1:" \
               : "+v"(ofr[0]), "+v"(ofr[1]), "+v"(ofr[2]), "+v"(ofr[3]), "+v"(ofr[4]), "+v"(ofr[5]), "+v"(ofr[6]), "+v"(ofr[7]), \
                 "+v"(ofr[8]), "+v"(ofr[9]), "+v"(ofr[10]), "+v"(ofr[11]), "+v"(ofr[12]), "+v"(ofr[13]), "+v"(ofr[14]), "+v"(ofr[15]) \
               : "v"(lane * 16), "s"(ob), "s"(ob + 2048), "s"(ob + 4096), "s"(ob + 6144), "s"(qg)    \
               : "memory", "scc")
#define XLOAD_G(G)                                                                                   \
  asm volatile("s_cmp_lg_u32 %5, " #G "\n\ts_cbranch_scc1 1f\n\t"                                    \
               "global_load_dwordx4 %0, %2, %3\n\tglobal_load_dwordx4 %1, %2, %4\n\t1:"              \
               : "+v"(xr[0]), "+v"(xr[1])                                                            \
               : "v"((erow * D + ec * 8) * 2), "s"(x + (size_t)(mt * 128) * D + nt * 64),            \
                 "s"(x + (size_t)(mt * 128 + 64) * D + nt * 64), "s"(qg)                             \
               : "memory", "scc")
  const unsigned wvoff = (unsigned)(((kq * 8 + kr) * D + (((pos >> 2) ^ ((kr >> 1) & 1)) * 32) + (pos & 3) * 8) * 2);
  const u16* wb = wo + nt * 64;
#define WDMA(P, HF)                                                                                  \
  asm volatile("s_nop 0\n\tglobal_load_lds_dwordx4 %0, %1"                                           \
               :                                                                                     \
               : "v"(wvoff), "s"(wb + (size_t)((P) * 64 + (HF) * 32) * D),                           \
                 "{m0}"(lds0 + K4_IMG + (HF) * 65536 + (P) * 4096 + kq * 1024)                       \
               : "memory")
  const unsigned vgu = (unsigned)((erow * FF + pos * 8) * 2), vdn = (unsigned)((erow * D + pos * 8) * 2);
  const size_t gbase = (size_t)(ks * 256) * FF + cg * 64;
#define SDMA(U)                                                                                      \
  asm volatile("s_nop 0\n\tglobal_load_lds_dwordx4 %0, %1 nt"                                        \
               :                                                                                     \
               : "v"(sv), "s"(sp + ((U) == 3 ? s3 : (U) * ss)), "{m0}"(sl + (U) * 4096)              \
               : "memory")
  {
    const int head = nt * 2 + qg;
    const __amdgpu_buffer_rsrc_t rs = __builtin_amdgcn_make_buffer_rsrc((void*)kvf, 0, 8 << 20, 0x00020000);
    const int vk = lane * 16, vv = lane * 16 + (4 << 20);
    const int hb = head * 131072 + kq * 2048;
#define LDK(so, c) __builtin_bit_cast(bf16x8, __builtin_amdgcn_raw_buffer_load_b128(rs, vk + (c), (so), 16))
#define LDV(so, c) __builtin_bit_cast(bf16x8, __builtin_amdgcn_raw_buffer_load_b128(rs, vv + (c), (so), 16))
#define TOFF(i) (hb + ((mt + (i)) & 15) * 8192)
    FT(11);
    LDS_BARRIER();
    bf16x8 q[4][2];
    q[0][0] = __builtin_bit_cast(bf16x8, fq[0]); q[0][1] = __builtin_bit_cast(bf16x8, fq[1]);
#pragma unroll
    for (int i = 1; i < 4; ++i) {
      const unsigned qa = lds0 + K3_QX + (qg * 4 + ((i + kq) & 3)) * 2048 + lane * 16;
      q[i][0] = lds_b128(qa); q[i][1] = lds_b128(qa + 1024);
    }
    bf16x8 ka0 = __builtin_bit_cast(bf16x8, fk[0]), ka1 = __builtin_bit_cast(bf16x8, fk[1]);
    bf16x8 va0 = __builtin_bit_cast(bf16x8, fv[0]), va1 = __builtin_bit_cast(bf16x8, fv[1]);
    bf16x8 kb0, kb1, vb0, vb1;
    bf16x8 ones;
    {
      const short o = ((((lane & 15) >> 2) & 1) == ((lane >> 4) & 1)) ? (short)0x3F80 : (short)0;
#pragma unroll
      for (int j = 0; j < 8; ++j) ones[j] = o;
    }
    const f32x16 zero = (f32x16){};
    f32x16 o[4] = {zero, zero, zero, zero};
    f32x4 l[4] = {(f32x4){}, (f32x4){}, (f32x4){}, (f32x4){}};
    f32x16 sA = MFMA32(ka0, q[0][0], zero), sB;
    sA = MFMA32(ka1, q[0][1], sA);
    asm volatile("s_waitcnt lgkmcnt(0)\n\ts_barrier"
                 : "+v"(q[1][0]), "+v"(q[1][1]), "+v"(q[2][0]), "+v"(q[2][1]), "+v"(q[3][0]), "+v"(q[3][1])::"memory");
    TS(3);
    u32x4 p0, p1 = (u32x4){0u, 0u, 0u, 0u};
#define PK(S_, i) pack2(S_[i], S_[i + 1])
#define EX(S_, i) S_[i] = ex2(S_[i])
#define PIN(x_) asm volatile("" : "+v"(x_))
#define UNIT(SC, SN, KN0, KN1, N, C, V0C, P, V1P, LD_A, LD_B)                               \
  do {                                                                                     \
    SN = MFMA32(KN0, q[N][0], zero); SBAR();                                               \
    EX(SC, 0); EX(SC, 1); EX(SC, 2); p0[0] = PK(SC, 0); PIN(p0); SBAR();                   \
    o[P] = MFMA32(V1P, __builtin_bit_cast(bf16x8, p1), o[P]); SBAR();                      \
    LD_A;                                                                                  \
    EX(SC, 3); EX(SC, 4); EX(SC, 5); p0[1] = PK(SC, 2); PIN(p0); SBAR();                   \
    l[P] = MFMA16(ones, __builtin_bit_cast(bf16x8, p1), l[P]); SBAR();                     \
    EX(SC, 6); EX(SC, 7); p0[2] = PK(SC, 4); p0[3] = PK(SC, 6); PIN(p0); SBAR();           \
    SN = MFMA32(KN1, q[N][1], SN); SBAR();                                                 \
    LD_B;                                                                                  \
    EX(SC, 8); EX(SC, 9); EX(SC, 10); p1[0] = PK(SC, 8); PIN(p1); SBAR();                  \
    o[C] = MFMA32(V0C, __builtin_bit_cast(bf16x8, p0), o[C]); SBAR();                      \
    EX(SC, 11); EX(SC, 12); EX(SC, 13); p1[1] = PK(SC, 10); PIN(p1); SBAR();               \
    l[C] = MFMA16(ones, __builtin_bit_cast(bf16x8, p0), l[C]); SBAR();                     \
    EX(SC, 14); EX(SC, 15); p1[2] = PK(SC, 12); p1[3] = PK(SC, 14); PIN(p1); SBAR();       \
  } while (0)
    const unsigned fl = (unsigned)((SY_KV + (head * 4 + kq) * 16) * 8);
    const unsigned fo = (unsigned)((SY_OC + (mt * 4 + ot) * 16) * 8);
#define KVFLAG()                                                                                       \
  do {                                                                                                 \
    asm volatile("s_waitcnt vmcnt(0)" ::: "memory");                                                   \
    if (lane < 2) __hip_atomic_store((gu32*)((char*)sync + (lane ? fo + head * 4 : fl + mt * 4)), 1u, RLX_AGENT); \
    FT(13);                                                                                            \
    SBAR();                                                                                            \
  } while (0)
    UNIT(sA, sB, ka0, ka1, 1, 0, va0, 3, va1  , (void)0, (void)0);
    KVFLAG();
    UNIT(sB, sA, ka0, ka1, 2, 1, va0, 0, va1, (void)0, (void)0);
    UNIT(sA, sB, ka0, ka1, 3, 2, va0, 1, va1, (void)0, (void)0);
    {
      const unsigned vo = fl + (lane & 15) * 4;
      if (HDK) __builtin_amdgcn_s_sleep(HDK);
      _Pragma("nounroll") for (unsigned spin = 0; spin < SPIN_CAP; ++spin) {
        unsigned c;
        asm volatile("global_load_dword %0, %1, %2 sc1\n\ts_waitcnt vmcnt(0)" : "=v"(c) : "v"(vo), "s"(sync) : "memory");
        if (__all(c != 0u)) break;
        __builtin_amdgcn_s_sleep(HDKR);
      }
    }
    TS(4);
    kb0 = LDK(TOFF(1), 0); kb1 = LDK(TOFF(1), 1024); SBAR();
    vb0 = LDV(TOFF(1), 0); vb1 = LDV(TOFF(1), 1024); SBAR();
    ka0 = LDK(TOFF(2), 0); ka1 = LDK(TOFF(2), 1024); SBAR();
#define WARM(I)                                                                                              \
  asm volatile("s_nop 0\n\tglobal_load_lds_dwordx4 %0, %1"                                                   \
               :                                                                                             \
               : "v"(wvoff), "s"(wb + (size_t)((mt & 7) * 128 + (I) * 32) * D), "{m0}"(lds0 + K3_DUMP)       \
               : "memory")
#define WARM_G 1
#define WARM_AT 0
#define WARM4(AT) do { if (WARM_AT == (AT)) { if (qg == WARM_G) { WARM(0); WARM(1); WARM(2); WARM(3); } SBAR(); } } while (0)
    WARM4(0);
    UNIT(sB, sA, kb0, kb1, 0, 3, va0, 2, va1, (void)0, (void)0);
    UNIT(sA, sB, kb0, kb1, 1, 0, vb0, 3, va1, va0 = LDV(TOFF(2), 0), va1 = LDV(TOFF(2), 1024));
    UNIT(sB, sA, kb0, kb1, 2, 1, vb0, 0, vb1, (void)0, (void)0);
    UNIT(sA, sB, kb0, kb1, 3, 2, vb0, 1, vb1, (void)0, (void)0);
    UNIT(sB, sA, ka0, ka1, 0, 3, vb0, 2, vb1, kb0 = LDK(TOFF(3), 0), kb1 = LDK(TOFF(3), 1024));
    const unsigned rb = lds0 + 98304;
#define SEND(I)                                                                                                          \
  do {                                                                                                                   \
    const unsigned wa = rb + ((((I) + kq + 1) & 3) * 3 + (I)) * K3_PART;                                                 \
    _Pragma("unroll") for (int g = 0; g < 4; ++g)                                                                        \
      ldsw(wa + g * 1024 + lane * 16, (f32x4){o[I][4 * g], o[I][4 * g + 1], o[I][4 * g + 2], o[I][4 * g + 3]});          \
    ldsw(wa + 4096 + lane * 4, l[I][0]);                                                                                 \
  } while (0)
#define PASS(X)                                                                                      \
  do {                                                                                               \
    const int t1 = TOFF(j + 1), t2 = TOFF(j + 2), t3 = TOFF(j + 3);                                  \
    UNIT(sA, sB, ka0, ka1, 1, 0, va0, 3, vb1, vb0 = LDV(t1, 0), vb1 = LDV(t1, 1024));                \
    UNIT(sB, sA, ka0, ka1, 2, 1, va0, 0, va1, X(0), (void)0);                                        \
    UNIT(sA, sB, ka0, ka1, 3, 2, va0, 1, va1, X(1), (void)0);                                        \
    UNIT(sB, sA, kb0, kb1, 0, 3, va0, 2, va1, ka0 = LDK(t2, 0), ka1 = LDK(t2, 1024));                \
    UNIT(sA, sB, kb0, kb1, 1, 0, vb0, 3, va1, va0 = LDV(t2, 0), va1 = LDV(t2, 1024));                \
    UNIT(sB, sA, kb0, kb1, 2, 1, vb0, 0, vb1, X(2), (void)0);                                        \
    UNIT(sA, sB, kb0, kb1, 3, 2, vb0, 1, vb1, X(3), (void)0);                                        \
    UNIT(sB, sA, ka0, ka1, 0, 3, vb0, 2, vb1, kb0 = LDK(t3, 0), kb1 = LDK(t3, 1024));                \
  } while (0)
#define NOP1(U) (void)0
    WARM4(1);
    int j = 2, e0 = qg == 0 ? 14 : 2;
    asm volatile("" : "+s"(e0));
#pragma unroll 1
    for (; j < e0; j += 2) {
      const unsigned sv = j < 10 ? vgu : vdn, sl = lds0 + (j - 2) * 8192 + kq * 1024;
      const int ss = j < 10 ? 32 * FF : 32 * D, s3 = j == 12 ? 2 * ss : 3 * ss;
      const u16* sp = (j < 6 ? wg + gbase : j < 10 ? wu + gbase : wd + (size_t)(dks * 224) * D + dcg * 64) + ((j - 2) & 2) * 2 * ss;
      PASS(SDMA);
    }
#pragma unroll 1
    for (; j < 14; j += 2) PASS(NOP1);
    UNIT(sA, sB, ka0, ka1, 1, 0, va0, 3, vb1, vb0 = LDV(TOFF(15), 0), vb1 = LDV(TOFF(15), 1024));
    WARM4(2);
    UNIT(sB, sA, ka0, ka1, 2, 1, va0, 0, va1, (void)0, (void)0);
    UNIT(sA, sB, ka0, ka1, 3, 2, va0, 1, va1, (void)0, (void)0);
    UNIT(sB, sA, kb0, kb1, 0, 3, va0, 2, va1, (void)0, (void)0);
    UNIT(sA, sB, kb0, kb1, 1, 0, vb0, 3, va1, (void)0, lds_await(lds0 + K3_CNT + 4, qg * 4u));
    UNIT(sB, sA, kb0, kb1, 2, 1, vb0, 0, vb1, (void)0, SEND(0));
    UNIT(sA, sB, kb0, kb1, 3, 2, vb0, 1, vb1, (void)0, SEND(1));
    UNIT(sB, sA, kb0, kb1, 0, 3, vb0, 2, vb1, (void)0, SEND(2));
    o[3] = MFMA32(vb1, __builtin_bit_cast(bf16x8, p1), o[3]);
    l[3] = MFMA16(ones, __builtin_bit_cast(bf16x8, p1), l[3]);
#undef UNIT
    TS(5);
    if (qg == 1) ldsw(lds0 + K3_CNT + 32 + kq * 4, 1u);
    lds_arrive(lds0 + K3_CNT);
    HT(9);
    const unsigned fr = (unsigned)((SY_OC + (mt * 4 + kq) * 16) * 8 + (lane & 31) * 4);
    unsigned fc;
    asm volatile("" : "=v"(fc), "=v"(xr[0]), "=v"(xr[1]));
#pragma unroll
    for (int f = 0; f < 16; ++f) asm volatile("" : "=v"(ofr[f]));
    asm volatile("s_cmp_lg_u32 %3, 0\n\ts_cbranch_scc1 1f\n\tglobal_load_dword %0, %1, %2 sc1\n\t1:"
                 : "+v"(fc) : "v"(fr), "s"(sync), "s"(qg) : "memory", "scc");
    XLOAD_G(1);
    OLOAD_G(1);
    HT(10);
    lds_await(lds0 + K3_CNT, qg == 0 ? 4u : 8u);
    HT(11);
    f32x16 oo = o[3];
    float lsum = l[3][0];
#pragma unroll
    for (int i = 0; i < 3; ++i) {
      const unsigned ra = rb + (kq * 3 + i) * K3_PART;
#pragma unroll
      for (int g = 0; g < 4; ++g) {
        const f32x4 v = ldsr<f32x4>(ra + g * 1024 + lane * 16);
        oo[4 * g] += v[0]; oo[4 * g + 1] += v[1]; oo[4 * g + 2] += v[2]; oo[4 * g + 3] += v[3];
      }
      lsum += ldsr<float>(ra + 4096 + lane * 4);
    }
    PIN(oo); PIN(lsum);
    HT(12);
    if (qg == 0) {
      asm volatile("s_waitcnt vmcnt(0)" : "+v"(fc)::"memory");
      _Pragma("nounroll") for (unsigned spin = 0; spin < SPIN_CAP && !__all(fc != 0u); ++spin) {
        __builtin_amdgcn_s_sleep(8);
        asm volatile("global_load_dword %0, %1, %2 sc1\n\ts_waitcnt vmcnt(0)" : "=v"(fc) : "v"(fr), "s"(sync) : "memory");
      }
    }
    lds_arrive(lds0 + K3_CNT + (qg == 0 ? 4 : 16));
    HT(13);
    float rl = 1.f / lsum;
    bool bad = !(lsum < 1.0e30f && lsum > 1.0e-30f);
#pragma unroll
    for (int r = 0; r < 16; ++r) {
      oo[r] *= rl;
      bad |= !(fabsf(oo[r]) < 3.0e38f);
    }
    if (__builtin_expect(__any(bad), 0)) {
      asm volatile("s_waitcnt vmcnt(0)" ::: "memory");
      const bf16x8 q0 = q[3][0], q1 = q[3][1];
      float m = -INFINITY, ls = 0.f;
      oo = zero;
      for (int t = 0; t < 64; ++t) {
        const int so = head * 131072 + t * 2048;
        const bf16x8 a0 = LDK(so, 0), a1 = LDK(so, 1024), b0 = LDV(so, 0), b1 = LDV(so, 1024);
        f32x16 s = MFMA32(a0, q0, zero);
        s = MFMA32(a1, q1, s);
        float tm = s[0];
#pragma unroll
        for (int r = 1; r < 16; ++r) tm = fmaxf(tm, s[r]);
        tm = xhalf_max(tm);
        const float mn = fmaxf(m, tm);
        const float alpha = ex2(m - mn);
        m = mn;
        u32x4 pa, pb;
        float ps = 0.f;
#pragma unroll
        for (int j = 0; j < 4; ++j) {
          pa[j] = pack2(ex2(s[2 * j] - mn), ex2(s[2 * j + 1] - mn));
          pb[j] = pack2(ex2(s[8 + 2 * j] - mn), ex2(s[8 + 2 * j + 1] - mn));
          ps += bflo(pa[j]) + bfhi(pa[j]) + bflo(pb[j]) + bfhi(pb[j]);
        }
        ps += __shfl_xor(ps, 32);
        ls = ls * alpha + ps;
#pragma unroll
        for (int r = 0; r < 16; ++r) oo[r] *= alpha;
        oo = MFMA32(b0, __builtin_bit_cast(bf16x8, pa), oo);
        oo = MFMA32(b1, __builtin_bit_cast(bf16x8, pb), oo);
      }
      rl = 1.f / ls;
#pragma unroll
      for (int r = 0; r < 16; ++r) oo[r] *= rl;
    }
#pragma unroll
    for (int s = 0; s < 2; ++s) {
      u32x4 w;
#pragma unroll
      for (int j = 0; j < 4; ++j) w[j] = min(pack2(oo[8 * s + 2 * j], oo[8 * s + 2 * j + 1]), 0xfffeffffu);
      st16(op + s * 512, w);
    }
#undef PK
#undef EX
#undef LDK
#undef LDV
#undef TOFF
  }
  if (qg == 0) {
#define PACE() asm volatile("s_waitcnt vmcnt(%0)" ::"i"(SH_D - 1) : "memory")
    TS(6);
#pragma unroll
    for (int i = 0; i < 16; ++i) wgu[i] = ldsr<u32x4>(lds0 + i * 4096 + kq * 1024 + lane * 16);
#pragma unroll
    for (int i = 0; i < 7; ++i) wdn[i] = ldsr<u32x4>(lds0 + (16 + i) * 4096 + kq * 1024 + lane * 16);
    asm volatile("" : "+v"(wgu[0]), "+v"(wgu[1]), "+v"(wgu[2]), "+v"(wgu[3]), "+v"(wgu[4]), "+v"(wgu[5]), "+v"(wgu[6]), "+v"(wgu[7]),
                      "+v"(wgu[8]), "+v"(wgu[9]), "+v"(wgu[10]), "+v"(wgu[11]), "+v"(wgu[12]), "+v"(wgu[13]), "+v"(wgu[14]), "+v"(wgu[15]));
    asm volatile("" : "+v"(wdn[0]), "+v"(wdn[1]), "+v"(wdn[2]), "+v"(wdn[3]), "+v"(wdn[4]), "+v"(wdn[5]), "+v"(wdn[6])::"memory");
#pragma unroll
    for (int p = 0; p < SH_L1; ++p) WDMA(p & 15, p >> 4);
    float mv[8];
    unsigned wrw;
    {
      const float* mp = modp + 2048 + nt * 64;
#pragma unroll
      for (int i = 0; i < 8; ++i)
        asm volatile("global_load_dword %0, %1, %2" : "=v"(mv[i]) : "v"(lane * 4), "s"(mp + (i >> 2) * 6144 + (i & 3) * 1024) : "memory");
      asm volatile("global_load_ushort %0, %1, %2" : "=v"(wrw) : "v"(lane * 2), "s"(wr + nt * 64) : "memory");
    }
#pragma unroll
    for (int p = SH_L1; p < 24; ++p) { PACE(); WDMA(p & 15, p >> 4); }
    FT(15);
    asm volatile("s_waitcnt vmcnt(0)" : "+v"(mv[0]), "+v"(mv[1]), "+v"(mv[2]), "+v"(mv[3]), "+v"(mv[4]), "+v"(mv[5]), "+v"(mv[6]), "+v"(mv[7]), "+v"(wrw)::"memory");
    if (wid == 0) {
      float v[4];
#pragma unroll
      for (int i = 0; i < 4; ++i) v[i] = rbf(mv[i] + mv[4 + i]);
      const float w = bflo(wrw);
      const float g2 = rbf(1.f + v[2]), gw = g2 * w;
      const unsigned va = lds0 + K4_VEC + lane * 4;
      ldsw(va, v[0]); ldsw(va + 256, g2); ldsw(va + 512, v[1]); ldsw(va + 768, v[3]); ldsw(va + 1024, gw);
      const float gs = wsum(gw), cs = wsum(v[1] * w);
      if (lane == 0) __hip_atomic_store((gu64*)sync + SY_GC + mt * 16 + nt, mk64(__float_as_uint(gs) | 1u, __float_as_uint(cs)), RLX_AGENT);
    }
    lds_arrive(lds0 + K3_CNT + 8);
  } else {
    TS(6);
  }
  OLOAD_G(0);
  XLOAD_G(0);
  lds_await(lds0 + K3_CNT + 8, 4u);
  if (qg == 0) lds_await(lds0 + K3_CNT + 32 + kq * 4, 1u);

  FT(12);
  float xv[2][8];
  {
    const int kh = qg, wt = kq, h = lane >> 5;
    unsigned bb[2];
    {
      const int q4 = (lane & 15) >> 2, p4 = lane & 3, ch = (lane >> 4) & 1;
#pragma unroll
      for (int c = 0; c < 2; ++c)
        bb[c] = lds0 + K4_IMG + qg * 32768 + (4 * h + q4) * 128 + ((c ^ ((q4 >> 1) & 1)) * 64) + ch * 32 + p4 * 8;
    }
#define WFRAG(C, F) cat8(lds_tr(bb[C] + (F) * 2048), lds_tr(bb[C] + (F) * 2048 + 1024))
    f32x16 acc0 = (f32x16){}, acc1 = (f32x16){};
#define OBAD(C)                                                                                                            \
  __any(max(max(max(max(ofr[4 * (C)][0], ofr[4 * (C)][1]), max(ofr[4 * (C)][2], ofr[4 * (C)][3])),                         \
                max(max(ofr[4 * (C) + 1][0], ofr[4 * (C) + 1][1]), max(ofr[4 * (C) + 1][2], ofr[4 * (C) + 1][3]))),        \
            max(max(max(ofr[4 * (C) + 2][0], ofr[4 * (C) + 2][1]), max(ofr[4 * (C) + 2][2], ofr[4 * (C) + 2][3])),         \
                max(max(ofr[4 * (C) + 3][0], ofr[4 * (C) + 3][1]), max(ofr[4 * (C) + 3][2], ofr[4 * (C) + 3][3])))) == POISON)
#define OWAIT(C, N)                                                                                                        \
  asm volatile("s_waitcnt vmcnt(%4)"                                                                                       \
               : "+v"(ofr[4 * (C)]), "+v"(ofr[4 * (C) + 1]), "+v"(ofr[4 * (C) + 2]), "+v"(ofr[4 * (C) + 3]) : "i"(N) : "memory")
#define OPHASE(BASE, Y)                                                                                                    \
  _Pragma("unroll") for (int c = 0; c < 4; ++c) {                                                                          \
    bf16x8 w[4][2];                                                                                                        \
    _Pragma("unroll") for (int i = 0; i < 4; ++i) { w[i][0] = WFRAG(0, 4 * c + i); w[i][1] = WFRAG(1, 4 * c + i); }        \
    OWAIT(c, 12 - 4 * c + (Y));                                                                                            \
    _Pragma("nounroll") for (unsigned spin = 0; spin < SPIN_CAP; ++spin) {                                                 \
      if (__builtin_expect(!OBAD(c), 1)) break;                                                                            \
      __builtin_amdgcn_s_sleep(HD0R);                                                                                      \
      _Pragma("unroll") for (int d = c; d < 4; ++d) OWAIT(d, 0);                                                           \
      _Pragma("unroll") for (int d = c; d < 4; ++d) {                                                                      \
        const int go = d == c ? 1 : __builtin_amdgcn_readfirstlane((int)OBAD(d));                                          \
        asm volatile("s_cmp_lg_u32 %6, 1\n\ts_cbranch_scc1 1f\n\t"                                                         \
                     "global_load_dwordx4 %0, %4, %5 sc1\n\tglobal_load_dwordx4 %1, %4, %5 offset:1024 sc1\n\t"            \
                     "global_load_dwordx4 %2, %4, %5 offset:2048 sc1\n\tglobal_load_dwordx4 %3, %4, %5 offset:3072 sc1\n\t1:" \
                     : "+v"(ofr[4 * d]), "+v"(ofr[4 * d + 1]), "+v"(ofr[4 * d + 2]), "+v"(ofr[4 * d + 3])                  \
                     : "v"(lane * 16), "s"((BASE) + d * 2048), "s"(go) : "memory", "scc");                                 \
      }                                                                                                                    \
      _Pragma("unroll") for (int d = c; d < 4; ++d) OWAIT(d, 0);                                                           \
    }                                                                                                                      \
    _Pragma("unroll") for (int i = 0; i < 4; ++i) {                                                                        \
      const bf16x8 a = __builtin_bit_cast(bf16x8, ofr[4 * c + i]);                                                         \
      acc0 = MFMA32(w[i][0], a, acc0);                                                                                     \
      acc1 = MFMA32(w[i][1], a, acc1);                                                                                     \
    }                                                                                                                      \
    SBAR();                                                                                                                \
  }
    OPHASE(ob, 2);
    TS(7);
    if (qg == 0) {
      lds_await(lds0 + K3_CNT + 16, 4u);
#pragma unroll
      for (int p = 8; p < 16; ++p) WDMA(p, 1);
      __builtin_amdgcn_s_sleep(HD0);
    } else if (HD0B) {
      __builtin_amdgcn_s_sleep(HD0B);
    }
#undef WDMA
    bb[0] += 65536; bb[1] += 65536;
    OLOAD(0, ob + 16384);
    if (qg == 0) {
      asm volatile("s_waitcnt vmcnt(16)" ::: "memory");
      lds_arrive(lds0 + K3_CNT + 20);
    } else {
      lds_await(lds0 + K3_CNT + 20, 4u);
    }
    OPHASE(ob + 16384, 0);
#undef OPHASE
#undef WFRAG
    LDS_BARRIER();
    TS(8);
    {
      const unsigned wa = lds0 + K4_IMG + kh * 34816 + (wt * 32 + (lane & 31)) * 272 + h * 16;
#pragma unroll
      for (int g = 0; g < 4; ++g) {
        ldsw(wa + g * 32, (f32x4){acc0[4 * g], acc0[4 * g + 1], acc0[4 * g + 2], acc0[4 * g + 3]});
        ldsw(wa + 128 + g * 32, (f32x4){acc1[4 * g], acc1[4 * g + 1], acc1[4 * g + 2], acc1[4 * g + 3]});
      }
    }
    asm volatile("s_waitcnt vmcnt(0) lgkmcnt(0)\n\ts_barrier" : "+v"(xr[0]), "+v"(xr[1])::"memory");
    const unsigned va = lds0 + K4_VEC + ec * 32;
    const f32x4 ga0 = ldsr<f32x4>(va), ga1 = ldsr<f32x4>(va + 16);
    const f32x4 gw0 = ldsr<f32x4>(va + 1024), gw1 = ldsr<f32x4>(va + 1024 + 16);
    float sm[2], sq[2], sd[2];
#pragma unroll
    for (int i = 0; i < 2; ++i) {
      const unsigned ra = lds0 + K4_IMG + (erow + 64 * i) * 272 + ec * 32;
      const f32x4 a0 = ldsr<f32x4>(ra), a1 = ldsr<f32x4>(ra + 16);
      const f32x4 b0 = ldsr<f32x4>(ra + 34816), b1 = ldsr<f32x4>(ra + 34816 + 16);
#pragma unroll
      for (int j = 0; j < 4; ++j) {
        const float lo = (j < 2 ? a0[2 * j] + b0[2 * j] : a1[2 * j - 4] + b1[2 * j - 4]);
        const float hi = (j < 2 ? a0[2 * j + 1] + b0[2 * j + 1] : a1[2 * j - 3] + b1[2 * j - 3]);
        const float gl = j < 2 ? ga0[2 * j] : ga1[2 * j - 4], gh = j < 2 ? ga0[2 * j + 1] : ga1[2 * j - 3];
        const unsigned w = pack2(fmaf(gl, lo, bflo(xr[i][j])), fmaf(gh, hi, bfhi(xr[i][j])));
        xv[i][2 * j] = bflo(w); xv[i][2 * j + 1] = bfhi(w);
      }
      sm[i] = sum8(((xv[i][0] + xv[i][1]) + (xv[i][2] + xv[i][3])) + ((xv[i][4] + xv[i][5]) + (xv[i][6] + xv[i][7]))) * (1.f / 64);
      float q = 0.f, d = 0.f;
#pragma unroll
      for (int j = 0; j < 8; ++j) {
        const float e = xv[i][j] - sm[i];
        q = fmaf(e, e, q);
        d = fmaf(e, j < 4 ? gw0[j] : gw1[j - 4], d);
      }
      sq[i] = sum8(q); sd[i] = sum8(d);
    }
    if (ec < 2) {
      const u32x4 rec = {__float_as_uint(ec ? sm[1] : sm[0]) | 1u, __float_as_uint(ec ? sq[1] : sq[0]),
                         __float_as_uint(ec ? sd[1] : sd[0]) | 1u, 0u};
      st16(sync + SY_ST + ((size_t)(mt * 16 + nt) * 128 + erow + 64 * ec) * 2, rec);
    }
  }
  GT(9);
  {
    const int r16 = lane & 15, sg = lane >> 4;
    const unsigned vo = (unsigned)(sg * 8192 + (wid * 16 + r16) * 16), vg = (unsigned)((SY_GC + mt * 16) * 8 + sg * 32);
    const unsigned long long* sb = sync + SY_ST + (size_t)mt * 4096;
    u32x4 c[4], g[2];
    __builtin_amdgcn_s_sleep(HD1);
    _Pragma("nounroll") for (unsigned spin = 0; spin < SPIN_CAP; ++spin) {
      GLOADC(c[0], vo, sb, 0); GLOADC(c[1], vo, sb, 2048); GLOADC(c[2], vo, sb + 512, 0); GLOADC(c[3], vo, sb + 512, 2048);
      GLOADC(g[0], vg, sync, 0); GLOADC(g[1], vg, sync, 16);
      asm volatile("s_waitcnt vmcnt(0)" : "+v"(c[0]), "+v"(c[1]), "+v"(c[2]), "+v"(c[3]), "+v"(g[0]), "+v"(g[1])::"memory");
      const unsigned m = (c[0][0] & c[0][2]) & (c[1][0] & c[1][2]) & (c[2][0] & c[2][2]) & (c[3][0] & c[3][2]) &
                         (g[0][0] & g[0][2]) & (g[1][0] & g[1][2]);
      if (__all(m & 1u)) break;
      __builtin_amdgcn_s_sleep(4);
    }
    GT(10);
#define F(x_) __uint_as_float(x_)
    const float mean = sumrows((F(c[0][0]) + F(c[1][0])) + (F(c[2][0]) + F(c[3][0]))) * (1.f / 16);
    float q = 0.f, d = 0.f;
#pragma unroll
    for (int i = 0; i < 4; ++i) {
      const float e = F(c[i][0]) - mean;
      q += fmaf(64.f * e, e, F(c[i][1]));
      d += fmaf(e, F(g[i >> 1][(i & 1) * 2]), F(c[i][2]));
    }
    const float cs = sumrows((F(g[0][1]) + F(g[0][3])) + (F(g[1][1]) + F(g[1][3])));
#undef F
    const float rstd = __builtin_amdgcn_rsqf(sumrows(q) * (1.f / D) + 1e-5f);
    const float rr = fmaf(rstd, sumrows(d), cs);
    const unsigned key = okey(rr), kmax = rowmaxu(key);
    const unsigned rbest = ~rowmaxu(key == kmax ? ~(unsigned)(wid * 16 + r16) : 0u);
    if (lane < 16) ldsw(lds0 + K4_ROW + (wid * 16 + r16) * 16, (f32x4){mean, rstd, rr, 0.f});
    if (lane == 0) ldsw(lds0 + K4_BEST + wid * 8, (u32x2){kmax, rbest});
  }
  LDS_BARRIER();
  GT(11);
  unsigned bkey, brow;
  {
    const u32x4 e0 = ldsr<u32x4>(lds0 + K4_BEST), e1 = ldsr<u32x4>(lds0 + K4_BEST + 16);
    const u32x4 e2 = ldsr<u32x4>(lds0 + K4_BEST + 32), e3 = ldsr<u32x4>(lds0 + K4_BEST + 48);
    const unsigned kk[8] = {e0[0], e0[2], e1[0], e1[2], e2[0], e2[2], e3[0], e3[2]};
    const unsigned rs[8] = {e0[1], e0[3], e1[1], e1[3], e2[1], e2[3], e3[1], e3[3]};
    bkey = kk[0]; brow = rs[0];
#pragma unroll
    for (int w = 1; w < 8; ++w) { const bool tk = kk[w] > bkey; bkey = tk ? kk[w] : bkey; brow = tk ? rs[w] : brow; }
  }
  {
    const unsigned va = lds0 + K4_VEC + ec * 32;
    const f32x4 g0 = ldsr<f32x4>(va + 256), g1 = ldsr<f32x4>(va + 256 + 16);
    const f32x4 s0 = ldsr<f32x4>(va + 512), s1 = ldsr<f32x4>(va + 512 + 16);
    const f32x4 b0 = ldsr<f32x4>(va + 768), b1 = ldsr<f32x4>(va + 768 + 16);
    u32x4 hw[2];
#pragma unroll
    for (int i = 0; i < 2; ++i) {
      const f32x2 ms = ldsr<f32x2>(lds0 + K4_ROW + (erow + 64 * i) * 16);
#pragma unroll
      for (int j = 0; j < 4; ++j) {
        const float n0 = (xv[i][2 * j] - ms[0]) * ms[1], n1 = (xv[i][2 * j + 1] - ms[0]) * ms[1];
        hw[i][j] = pack2(fmaf(n0, j < 2 ? g0[2 * j] : g1[2 * j - 4], j < 2 ? s0[2 * j] : s1[2 * j - 4]),
                         fmaf(n1, j < 2 ? g0[2 * j + 1] : g1[2 * j - 3], j < 2 ? s0[2 * j + 1] : s1[2 * j - 3]));
      }
    }
    if (erow == (int)(brow & 63u)) {
      const bool hi = brow >= 64u;
      const u32x4 hc = hi ? hw[1] : hw[0];
      unsigned long long* hp = sync + SY_HC + mt * 512 + nt * 32 + ec * 4;
      st16(hp, (u32x4){hc[0], bkey | 1u, hc[1], bkey | 1u});
      st16(hp + 2, (u32x4){hc[2], bkey | 1u, hc[3], bkey | 1u});
      float xc[8];
#pragma unroll
      for (int j = 0; j < 8; ++j) xc[j] = hi ? xv[1][j] : xv[0][j];
      ldsw(lds0 + K4_FIN + ec * 32, (f32x4){xc[0], xc[1], xc[2], xc[3]});
      ldsw(lds0 + K4_FIN + ec * 32 + 16, (f32x4){xc[4], xc[5], xc[6], xc[7]});
      ldsw(lds0 + K4_FIN + 256 + ec * 32, (f32x4){bflo(hc[0]), bfhi(hc[0]), bflo(hc[1]), bfhi(hc[1])});
      ldsw(lds0 + K4_FIN + 256 + ec * 32 + 16, (f32x4){bflo(hc[2]), bfhi(hc[2]), bflo(hc[3]), bfhi(hc[3])});
    }
#pragma unroll
    for (int i = 0; i < 2; ++i) {
      const int row = erow + 64 * i;
      u32x4 o;
#pragma unroll
      for (int j = 0; j < 4; ++j)
        o[j] = pack2(fmaf(j < 2 ? b0[2 * j] : b1[2 * j - 4], bflo(hw[i][j]), xv[i][2 * j]),
                     fmaf(j < 2 ? b0[2 * j + 1] : b1[2 * j - 3], bfhi(hw[i][j]), xv[i][2 * j + 1]));
      if (row != (int)brow) st16(out + (size_t)(mt * 128 + row) * D + nt * 64 + ec * 8, o);
    }
  }

  if (wid > 4) {
    asm volatile("s_waitcnt lgkmcnt(0)" ::: "memory");
    PROF_END(3);
    return;
  }
  gu64* gp = (gu64*)sync + SY_GP;
  gu64* gy = (gu64*)sync + SY_GY;
  if (wid == 4) {
    int win;
    {
      const unsigned vo = (unsigned)(ks * 1024 + lane * 16);
      const unsigned long long* hb = sync + SY_HC;
      u32x4 c[16];
      __builtin_amdgcn_s_sleep(HD2);
      _Pragma("nounroll") for (unsigned spin = 0; spin < SPIN_CAP; ++spin) {
#pragma unroll
        for (int i = 0; i < 16; ++i) GLOADC(c[i], vo, hb + i * 512, 0);
        asm volatile("s_waitcnt vmcnt(0)"
                     : "+v"(c[0]), "+v"(c[1]), "+v"(c[2]), "+v"(c[3]), "+v"(c[4]), "+v"(c[5]), "+v"(c[6]), "+v"(c[7]),
                       "+v"(c[8]), "+v"(c[9]), "+v"(c[10]), "+v"(c[11]), "+v"(c[12]), "+v"(c[13]), "+v"(c[14]), "+v"(c[15])::"memory");
        unsigned m = 1u;
#pragma unroll
        for (int i = 0; i < 16; ++i) m &= c[i][1] & c[i][3];
        if (__all(m & 1u)) break;
        __builtin_amdgcn_s_sleep(4);
      }
      GT(12);
      unsigned k0 = c[0][1], h0 = c[0][0], h1 = c[0][2];
      win = 0;
#pragma unroll
      for (int i = 1; i < 16; ++i) {
        const bool tk = c[i][1] > k0;
        k0 = tk ? c[i][1] : k0; h0 = tk ? c[i][0] : h0; h1 = tk ? c[i][2] : h1; win = tk ? i : win;
      }
      ldsw(lds0 + K4_H + lane * 16, (f32x4){bflo(h0), bfhi(h0), bflo(h1), bfhi(h1)});
      win = __builtin_amdgcn_readfirstlane(win);
    }
    LDS_BARRIER();
    const f32x2 fx = ldsr<f32x2>(lds0 + K4_FIN + (lane & 31) * 8), fg = ldsr<f32x2>(lds0 + K4_VEC + 768 + (lane & 31) * 8);
    const f32x2 fh = ldsr<f32x2>(lds0 + K4_FIN + 256 + (lane & 31) * 8);
    const float fsig = rbf(1.f / (1.f + __expf(-rbf(ldsr<float>(lds0 + K4_ROW + brow * 16 + 8)))));
    unsigned* orow = (unsigned*)(out + (size_t)(mt * 128 + brow) * D + nt * 64 + (lane & 31) * 2);
    if (win != mt && lane < 32) *orow = pack2(fmaf(fg[0], fh[0], fx[0]), fmaf(fg[1], fh[1], fx[1]));
    LDS_BARRIER();
    if (has_gu) {
      float sg = 0.f, su = 0.f;
#pragma unroll
      for (int w = 0; w < 4; ++w) {
        sg += ldsr<float>(lds0 + K4_RED + w * 512 + lane * 4);
        su += ldsr<float>(lds0 + K4_RED + w * 512 + 256 + lane * 4);
      }
      __hip_atomic_store(gp + ks * FF + cg * 64 + lane, mk64(__float_as_uint(sg) | 1u, __float_as_uint(su)), RLX_AGENT);
    }

    {
      const unsigned v0 = (unsigned)(dks * 1792 + lane * 16), v1 = (unsigned)(dks * 1792 + (lane < 48 ? lane + 64 : 111) * 16);
      const unsigned long long* gb = sync + SY_GP;
      u32x4 c[8];
      __builtin_amdgcn_s_sleep(HD3);
      _Pragma("nounroll") for (unsigned spin = 0; spin < SPIN_CAP; ++spin) {
#pragma unroll
        for (int i = 0; i < 4; ++i) { GLOADC(c[i], v0, gb + i * FF, 0); GLOADC(c[4 + i], v1, gb + i * FF, 0); }
        asm volatile("s_waitcnt vmcnt(0)"
                     : "+v"(c[0]), "+v"(c[1]), "+v"(c[2]), "+v"(c[3]), "+v"(c[4]), "+v"(c[5]), "+v"(c[6]), "+v"(c[7])::"memory");
        unsigned m = 1u;
#pragma unroll
        for (int i = 0; i < 8; ++i) m &= c[i][0] & c[i][2];
        if (__all(m & 1u)) break;
        __builtin_amdgcn_s_sleep(4);
      }
      GT(13);
#pragma unroll
      for (int r = 0; r < 2; ++r) {
        float a[2];
#pragma unroll
        for (int e = 0; e < 2; ++e) {
          float sg = 0.f, su = 0.f;
#pragma unroll
          for (int i = 0; i < 4; ++i) { sg += __uint_as_float(c[r * 4 + i][2 * e]); su += __uint_as_float(c[r * 4 + i][2 * e + 1]); }
          sg = rbf(sg); su = rbf(su);
          a[e] = rbf(rbf(sg / (1.f + __expf(-sg))) * su);
        }
        ldsw(lds0 + K4_A + (r * 64 + lane) * 8, (f32x2){a[0], a[1]});
      }
    }
    LDS_BARRIER();
    asm volatile("s_barrier" ::: "memory");
    if (lane < 32) {
      f32x2 y = (f32x2){0.f, 0.f};
#pragma unroll
      for (int w = 0; w < 4; ++w) y += ldsr<f32x2>(lds0 + K4_RED2 + w * 256 + lane * 8);
      __hip_atomic_store(gy + dks * 512 + dcg * 32 + lane, mk64(__float_as_uint(y[0]) | 1u, __float_as_uint(y[1])), RLX_AGENT);
    }
    TS(14);
    if (win == mt) {
      unsigned long long c[8];
      __builtin_amdgcn_s_sleep(HD4);
      _Pragma("nounroll") for (unsigned spin = 0; spin < SPIN_CAP; ++spin) {
        bool ok = true;
#pragma unroll
        for (int i = 0; i < 8; ++i) {
          c[i] = __hip_atomic_load(gy + ((lane >> 5) * 8 + i) * 512 + nt * 32 + (lane & 31), RLX_AGENT);
          ok &= (c[i] & 1ull) != 0;
        }
        if (__all(ok)) break;
        __builtin_amdgcn_s_sleep(4);
      }
      float y0 = 0.f, y1 = 0.f;
#pragma unroll
      for (int i = 0; i < 8; ++i) { y0 += __uint_as_float((unsigned)c[i]); y1 += __uint_as_float((unsigned)(c[i] >> 32)); }
      y0 = swap32_add(y0, y0); y1 = swap32_add(y1, y1);
      y0 = rbf(rbf(y0) * fsig); y1 = rbf(rbf(y1) * fsig);
      if (lane < 32) *orow = pack2(fmaf(fg[0], rbf(fh[0] + y0), fx[0]), fmaf(fg[1], rbf(fh[1] + y1), fx[1]));
    }
    TS(15);
    TS_END(3, 4);
    PROF_END(3);
    return;
  }
  {
    f32x2 wf[16][4];
#pragma unroll
    for (int i = 0; i < 16; ++i)
#pragma unroll
      for (int j = 0; j < 4; ++j) { wf[i][j] = up2(wgu[i][j]); asm volatile("" : "+v"(wf[i][j])); }
    LDS_BARRIER();
    f32x2 ag[4], au[4];
#pragma unroll
    for (int i = 0; i < 8; ++i) {
      const float h = ldsr<float>(lds0 + K4_H + (erow + 32 * i) * 4);
#pragma unroll
      for (int j = 0; j < 4; ++j) {
        ag[j] = i == 0 ? wf[0][j] * h : wf[i][j] * h + ag[j];
        au[j] = i == 0 ? wf[8][j] * h : wf[8 + i][j] * h + au[j];
      }
    }
    float v8[8], v4[4];
#pragma unroll
    for (int j = 0; j < 8; ++j) v8[j] = swap32_add(ag[j >> 1][j & 1], au[j >> 1][j & 1]);
#pragma unroll
    for (int j = 0; j < 4; ++j) v4[j] = dpp_add<0x128>(swap16_add(v8[j], v8[4 + j]));
    if ((lane & 8) == 0)
      ldsw(lds0 + K4_RED + wid * 512 + ((lane >> 5) * 64 + (lane & 7) * 8 + ((lane >> 4) & 1) * 4) * 4,
           (f32x4){v4[0], v4[1], v4[2], v4[3]});
  }
  LDS_BARRIER();
  {
    f32x2 wdf[7][4];
#pragma unroll
    for (int i = 0; i < 7; ++i)
#pragma unroll
      for (int j = 0; j < 4; ++j) { wdf[i][j] = up2(wdn[i][j]); asm volatile("" : "+v"(wdf[i][j])); }
    asm volatile("s_barrier" ::: "memory");
    f32x2 a[4];
#pragma unroll
    for (int i = 0; i < 7; ++i) {
      const float h = ldsr<float>(lds0 + K4_A + (erow + 32 * i) * 4);
#pragma unroll
      for (int j = 0; j < 4; ++j) a[j] = i == 0 ? wdf[0][j] * h : wdf[i][j] * h + a[j];
    }
    float v4[4], v2[2];
#pragma unroll
    for (int j = 0; j < 4; ++j) v4[j] = swap32_add(a[j >> 1][j & 1], a[2 + (j >> 1)][j & 1]);
#pragma unroll
    for (int j = 0; j < 2; ++j) v2[j] = dpp_add<0x128>(swap16_add(v4[j], v4[2 + j]));
    if ((lane & 8) == 0)
      ldsw(lds0 + K4_RED2 + wid * 256 + ((lane & 7) * 8 + (lane >> 5) * 4 + ((lane >> 4) & 1) * 2) * 4, (f32x2){v2[0], v2[1]});
  }
  LDS_BARRIER();
  TS_END(4, 0);
  PROF_END(3);
}

extern "C" void kernel_launch(void* const* d_in, const int* in_sizes, int n_in, void* d_out, int out_size, void* d_ws,
                              size_t ws_size, hipStream_t stream) {
  const u16* x = (const u16*)d_in[0];
  const u16* wqkv = (const u16*)d_in[1];
  const u16* wo = (const u16*)d_in[2];
  const u16* wr = (const u16*)d_in[4];
  const u16* wg = (const u16*)d_in[5];
  const u16* wu = (const u16*)d_in[6];
  const u16* wd = (const u16*)d_in[7];
  const u16* c = (const u16*)d_in[8];
  const u16* wmod = (const u16*)d_in[9];
  char* ws = (char*)d_ws;
  float* modp = (float*)(ws + WS_MODP);
  u16* nbuf = (u16*)(ws + WS_N);
  u16* kvf = (u16*)(ws + WS_K);
  u16* of = (u16*)(ws + WS_O);
  u16* out = (u16*)d_out;
  (void)hipFuncSetAttribute((const void*)k2_main, hipFuncAttributeMaxDynamicSharedMemorySize, K3_LDS);
  k1_mod_ln<<<256, 512, 65536, stream>>>(c, wmod, x, modp, nbuf, (u32x4*)(ws + WS_SYNC), wqkv);
  k2_main<<<256, 512, K3_LDS, stream>>>(nbuf, wqkv, modp, kvf, of, wo, x, wr, wg, wu, wd, out, (unsigned long long*)(ws + WS_SYNC));
}
```

```cpp
#include <hip/hip_runtime.h>
#include <hip/hip_bf16.h>
#include <stdint.h>

typedef unsigned short u16;
using bf16x8 = __attribute__((ext_vector_type(8))) short;
using s16x4 = __attribute__((ext_vector_type(4))) short;
using f32x16 = __attribute__((ext_vector_type(16))) float;
using f32x4 = __attribute__((ext_vector_type(4))) float;
using f32x2 = __attribute__((ext_vector_type(2))) float;
using u32x4 = __attribute__((ext_vector_type(4))) unsigned;
using u32x2 = __attribute__((ext_vector_type(2))) unsigned;
typedef __bf16 bf16x2_t __attribute__((ext_vector_type(2)));
#define LAS __attribute__((address_space(3)))
#define DI __device__ __forceinline__

constexpr int S = 2048, D = 1024, NH = 32, FF = 3584;
constexpr float QSCALE = 0.17677669529663687f * 1.4426950408889634f;

constexpr size_t WS_MODP = 0;
constexpr size_t WS_SYNC = 131072;
constexpr int SY_OC = 0, SY_KV = SY_OC + 64 * 16;
constexpr int SY_KEY = SY_KV + 128 * 16;
constexpr int SY_HC = SY_KEY + 256, HCP = 272;
constexpr int SY_XC = SY_HC + 16 * HCP;
constexpr int SY_GP = SY_XC + 16 * HCP, GPP = 3648;
constexpr int SY_GY = SY_GP + 4 * GPP, GYP = 528;
constexpr int SY_ST = SY_GY + 16 * GYP, STP = 272;
constexpr int SY_END = SY_ST + 256 * STP;
static_assert(WS_SYNC + SY_END * 8 <= (1u << 20), "sync area");
constexpr size_t WS_Q = 1u << 20;
constexpr size_t WS_K = WS_Q + (4u << 20);
constexpr size_t WS_V = WS_K + (4u << 20);
constexpr size_t WS_O = WS_V + (4u << 20);
constexpr size_t WS_N = WS_O + (8u << 20);

#define PROF_BEGIN()
#define PROF_END(kid)
#define PROF_END2(kid)
#define DRAIN()
#define TS_DECL()
#define TS(i)
#define TS_END(kid, w)

#define TR(base, w, kt, i)
#define TR_FLUSH(base, w)
#define TR_INIT(base, w)

DI float bf2f(u16 v) { return __uint_as_float((unsigned)v << 16); }
DI float bflo(unsigned w) { return __uint_as_float(w << 16); }
DI float bfhi(unsigned w) { return __uint_as_float(w & 0xffff0000u); }
DI unsigned pack2(float lo, float hi) {
  f32x2 v = {lo, hi};
  bf16x2_t b = __builtin_convertvector(v, bf16x2_t);
  return __builtin_bit_cast(unsigned, b);
}
DI float rbf(float x) { return bflo(pack2(x, 0.f)); }
#define MFMA32(a, b, c) __builtin_amdgcn_mfma_f32_32x32x16_bf16((a), (b), (c), 0, 0, 0)
#define MFMA16(a, b, c) __builtin_amdgcn_mfma_f32_16x16x32_bf16((a), (b), (c), 0, 0, 0)
#define SBAR() __builtin_amdgcn_sched_barrier(0)
#define LDS_BARRIER() asm volatile("s_waitcnt lgkmcnt(0)\n\ts_barrier" ::: "memory")

DI float wave_sum(float v) {
#pragma unroll
  for (int o = 32; o > 0; o >>= 1) v += __shfl_xor(v, o);
  return v;
}
template <int CTRL>
DI float dpp_add(float v) {
  return v + __uint_as_float(__builtin_amdgcn_update_dpp(0u, __float_as_uint(v), CTRL, 0xf, 0xf, true));
}
DI float swap32_add(float a, float b) {
  auto r = __builtin_amdgcn_permlane32_swap(__float_as_uint(a), __float_as_uint(b), false, false);
  return __uint_as_float(r[0]) + __uint_as_float(r[1]);
}
DI float swap16_add(float a, float b) {
  auto r = __builtin_amdgcn_permlane16_swap(__float_as_uint(a), __float_as_uint(b), false, false);
  return __uint_as_float(r[0]) + __uint_as_float(r[1]);
}
DI float wsum(float v) {
  v = dpp_add<0xB1>(v);
  v = dpp_add<0x4E>(v);
  v = dpp_add<0x141>(v);
  v = dpp_add<0x140>(v);
  v = swap16_add(v, v);
  return swap32_add(v, v);
}
DI unsigned ln_pair(float lo, float hi) {
  const unsigned l = pack2(lo, 0.f), b = __float_as_uint(hi);
  const int t = max((int)(b & 0x7fffffffu) - (int)l + 0x8000, 0);
  return (b & 0x80000000u) | ((unsigned)t & 0x7fff0000u) | l;
}
DI void glds16(const void* sbase, unsigned voff, unsigned lds_dst) {
  unsigned keep;
  asm volatile("s_mov_b32 %0, m0\n\ts_mov_b32 m0, %3\n\ts_nop 0\n\tglobal_load_lds_dwordx4 %1, %2\n\ts_mov_b32 m0, %0"
               : "=&s"(keep)
               : "v"(voff), "s"(sbase), "s"(lds_dst)
               : "memory");
}
DI void st16(void* p, u32x4 v) {
  asm volatile("s_nop 0\n\tglobal_store_dwordx4 %0, %1, off sc1\n\ts_nop 1" ::"v"(p), "v"(v) : "memory");
}
DI bf16x8 lds_b128(unsigned addr) { return *(const LAS bf16x8*)(uintptr_t)addr; }
typedef short v4i16_t __attribute__((ext_vector_type(4)));
DI s16x4 lds_tr(unsigned addr) {
  return __builtin_bit_cast(s16x4, __builtin_amdgcn_ds_read_tr16_b64_v4i16((LAS v4i16_t*)(uintptr_t)addr));
}
DI bf16x8 cat8(s16x4 lo, s16x4 hi) { return __builtin_shufflevector(lo, hi, 0, 1, 2, 3, 4, 5, 6, 7); }

#define GLD(dst, ptr, pol) asm volatile("global_load_dwordx4 %0, %1, off" pol : "=v"(dst) : "v"(ptr) : "memory")
DI float red8(float v) {
  v = dpp_add<0x128>(v);
  v = swap16_add(v, v);
  return swap32_add(v, v);
}
DI float silu_bf(float v) { return rbf(v / (1.f + __expf(-v))); }
#define PF_A 12
#define XPOL " nt"
#define PFPOL ""
#define XRPOL ""
#define XWPOL ""
#define XW_G 1
#define XW_AT 1
#define SDPOL " nt"
#define SWPOL " nt"
#define WOPOL ""
#define PF_B 4
__global__ __launch_bounds__(512) void k1_mod_ln(const u16* __restrict__ c, const u16* __restrict__ wmod,
                                                 const u16* __restrict__ x, float* __restrict__ modp,
                                                 u16* __restrict__ nbuf, u32x4* __restrict__ sync,
                                                 const u16* __restrict__ wqkv) {
  PROF_BEGIN();
  __shared__ __attribute__((aligned(16))) float rs[8][8][2];
  __shared__ __attribute__((aligned(16))) float red[8][64];
  __shared__ __attribute__((aligned(16))) char dump[1024];
  const int t = threadIdx.x, lane = t & 63, bid = blockIdx.x, jj = bid >> 3;
  const int wid = __builtin_amdgcn_readfirstlane(t >> 6);
  TS_DECL(); TS(0);
  const bool gv = bid < 192;
  const int r = lane & 7, ch = wid * 16 + (lane >> 3);
  const int cg = bid % 96, kh = bid / 96;
  u32x4 xa, xb, cw, w[8];
  const u16* xp = x + (size_t)(bid * 8 + r) * D + ch * 8;
  GLD(xa, xp, XPOL); GLD(xb, xp + 64, XPOL);
  if (gv) {
    const int k0 = kh * 512 + (t >> 3) * 8;
    GLD(cw, c + k0, "");
    asm volatile("s_barrier" ::: "memory");
    const u16* p0 = wmod + (size_t)k0 * 6144 + cg * 64 + (t & 7) * 8;
#pragma unroll
    for (int i = 0; i < 8; ++i) GLD(w[i], p0 + i * 6144, " nt");
    TS(1);
    asm volatile("s_waitcnt vmcnt(9)" ::: "memory");
  } else {
    asm volatile("s_waitcnt vmcnt(0)" ::: "memory");
  }
  asm volatile("" : "+v"(xa), "+v"(xb)::"memory");
  TS(2);
  float v[16];
#pragma unroll
  for (int j = 0; j < 4; ++j) {
    v[2 * j] = bflo(xa[j]); v[2 * j + 1] = bfhi(xa[j]);
    v[8 + 2 * j] = bflo(xb[j]); v[8 + 2 * j + 1] = bfhi(xb[j]);
  }
  {
    float a = 0.f;
#pragma unroll
    for (int j = 0; j < 16; ++j) a += v[j];
    const float mw = red8(a) * (1.f / 128);
    float q = 0.f;
#pragma unroll
    for (int j = 0; j < 16; ++j) q = fmaf(v[j] - mw, v[j] - mw, q);
    q = red8(q);
    if (lane < 8) *(f32x2*)rs[r][wid] = (f32x2){mw, q};
  }
  LDS_BARRIER();
  {
    f32x4 e[4];
#pragma unroll
    for (int i = 0; i < 4; ++i) e[i] = *(const f32x4*)rs[r][2 * i];
    const float mean = (((e[0][0] + e[0][2]) + (e[1][0] + e[1][2])) + ((e[2][0] + e[2][2]) + (e[3][0] + e[3][2]))) * 0.125f;
    float q = 0.f;
#pragma unroll
    for (int i = 0; i < 4; ++i) {
      const float d0 = e[i][0] - mean, d1 = e[i][2] - mean;
      q += fmaf(128.f * d0, d0, e[i][1]) + fmaf(128.f * d1, d1, e[i][3]);
    }
    const float rstd = __builtin_amdgcn_rsqf(q * (1.f / D) + 1e-5f);
    u32x4 o0, o1;
#pragma unroll
    for (int j = 0; j < 4; ++j) {
      o0[j] = ln_pair((v[2 * j] - mean) * rstd, (v[2 * j + 1] - mean) * rstd);
      o1[j] = ln_pair((v[8 + 2 * j] - mean) * rstd, (v[8 + 2 * j + 1] - mean) * rstd);
    }
    u16* np = nbuf + (size_t)(bid >> 2) * 32768 + ch * 256 + ((bid & 3) * 8 + r) * 8;
    st16(np, o0);
    st16(np + 2048, o1);
  }
  TS(3);
  if (gv) {
    asm volatile("s_waitcnt vmcnt(2)"
                 : "+v"(cw), "+v"(w[0]), "+v"(w[1]), "+v"(w[2]), "+v"(w[3]), "+v"(w[4]), "+v"(w[5]), "+v"(w[6]), "+v"(w[7])::"memory");
    TS(4);
    float acc[8];
#pragma unroll
    for (int i = 0; i < 8; ++i) {
      const float sc = silu_bf((i & 1) ? bfhi(cw[i >> 1]) : bflo(cw[i >> 1]));
#pragma unroll
      for (int j = 0; j < 4; ++j) {
        acc[2 * j] = i ? fmaf(sc, bflo(w[i][j]), acc[2 * j]) : sc * bflo(w[0][j]);
        acc[2 * j + 1] = i ? fmaf(sc, bfhi(w[i][j]), acc[2 * j + 1]) : sc * bfhi(w[0][j]);
      }
    }
    float v4[4], v2[2];
#pragma unroll
    for (int j = 0; j < 4; ++j) v4[j] = swap32_add(acc[j], acc[4 + j]);
#pragma unroll
    for (int j = 0; j < 2; ++j) v2[j] = dpp_add<0x128>(swap16_add(v4[j], v4[2 + j]));
    if ((lane & 8) == 0) *(f32x2*)&red[wid][(lane & 7) * 8 + (lane >> 5) * 4 + ((lane >> 4) & 1) * 2] = (f32x2){v2[0], v2[1]};
    LDS_BARRIER();
    if (t < 16) {
      f32x4 a = *(const f32x4*)&red[0][t * 4];
#pragma unroll
      for (int i = 1; i < 8; ++i) a += *(const f32x4*)&red[i][t * 4];
      st16(modp + kh * 6144 + cg * 64 + t * 4, __builtin_bit_cast(u32x4, a));
    }
    if (PF_B) asm volatile("s_barrier" ::: "memory");
  } else {
    const int z = wid * 64 + bid - 192;
    const unsigned v = z < SY_HC / 128 ? 0u : 0xffffffffu;
    if (z < SY_GP / 128) st16(sync + z * 64 + lane, (u32x4){v, v, v, v});
  }
  TS(5);
  {
    const unsigned vo = (unsigned)(((lane >> 5) * 3072 + ((bid & 7) >> 1) * 256 + (lane & 31) * 8) * 2);
    const int u0 = gv ? 64 * PF_A + jj * 8 + wid : (jj - 24) * 8 + wid, us = gv ? 192 : 64, n = gv ? PF_B : PF_A;
    for (int i = 0, u = u0; i < n; ++i, u += us)
      asm volatile("s_nop 0\n\tglobal_load_lds_dwordx4 %0, %1" PFPOL
                   :
                   : "v"(vo), "s"(wqkv + (size_t)(u / 3) * 6144 + (u % 3) * 1024), "{m0}"((unsigned)(uintptr_t)dump)
                   : "memory");
  }
  TS(6);
  asm volatile("s_waitcnt vmcnt(0)" ::: "memory");
  TS(7);
  TS_END(1, 1);
  PROF_END(1);
}

constexpr int K2_NB = 6, K2_BSLOT = 24576, K2_DW = 3, K2_DN = 6;
constexpr int K2_LDS_GS = K2_NB * K2_BSLOT, K2_LDS_TR = K2_LDS_GS + 8192, K2_LDS = K2_LDS_TR + 2048;

#define GLOAD16(dst, voff, sbase) \
  asm volatile("global_load_dwordx4 %0, %1, %2" : "=v"(dst) : "v"(voff), "s"(sbase) : "memory")

#define GLOAD16NT(dst, voff, sbase) \
  asm volatile("global_load_dwordx4 %0, %1, %2 nt" : "=v"(dst) : "v"(voff), "s"(sbase) : "memory")

#define K2_TP0 16
#define K2_TN 2
#define K2_TS 2
constexpr unsigned POISON = 0xffffffffu;
constexpr int k2_touch(int p) { return p >= K2_TP0 && (p - K2_TP0) % K2_TS == 0 && (p - K2_TP0) / K2_TS < K2_TN ? (p - K2_TP0) / K2_TS : -1; }
struct K2Sched { int wn[33]; int ww[17]; };
constexpr int k2_younger(const int* seq, int n, int id) {
  int c = 0;
  for (int i = n - 1; i >= 0 && seq[i] != id; --i) ++c;
  return c;
}
constexpr K2Sched k2_make() {
  K2Sched s{};
  int seq[128] = {};
  int n = 0;
  seq[n++] = 200; seq[n++] = 201;
  for (int i = 0; i < 4; ++i) {
    if (i == 0) seq[n++] = 0;
    if (i < K2_DW) { seq[n++] = 100 + 3 * i; seq[n++] = 101 + 3 * i; seq[n++] = 102 + 3 * i; }
    if (2 * i + 1 < K2_DN) seq[n++] = 2 * i + 1;
    if (2 * i + 2 < K2_DN) seq[n++] = 2 * i + 2;
  }
  s.wn[32] = k2_younger(seq, n, 201);
  s.ww[16] = k2_younger(seq, n, 102);
  seq[n++] = K2_DN;
  for (int p = 0; p < 32; ++p) {
    const int t = p >> 1, j = p & 1;
    if (p + 1 <= 31) s.wn[p] = k2_younger(seq, n, p + 1);
    if (j == 1 && t + 1 <= 15) s.ww[t] = k2_younger(seq, n, 100 + 3 * (t + 1) + 2);
    if (p + 1 + K2_DN <= 31) seq[n++] = p + 1 + K2_DN;
    if (t + K2_DW <= 15) {
      if (j == 0) { seq[n++] = 100 + 3 * (t + K2_DW); seq[n++] = 101 + 3 * (t + K2_DW); }
      else seq[n++] = 102 + 3 * (t + K2_DW);
    }
    if (k2_touch(p) >= 0) seq[n++] = 300 + p;
  }
  return s;
}
constexpr K2Sched K2S = k2_make();

#define K2_WPOL ""
template <int KT, int PC>
DI void k2_issue_b(const u16* wq, unsigned wvoff, unsigned lds_dst) {
  if constexpr (KT <= 15) {
    const u16* sb = wq + (size_t)KT * 64 * 3072 + (PC == 2 ? 2048 - 512 : 0);
    asm volatile("s_nop 0\n\tglobal_load_lds_dwordx4 %0, %1 offset:%2" K2_WPOL
                 :
                 : "v"(wvoff), "s"(sb), "i"(PC == 0 ? 0 : PC == 1 ? 2048 : 1024), "{m0}"(lds_dst + (KT % K2_NB) * K2_BSLOT)
                 : "memory");
  }
}
template <int F>
DI void k2_issue_n(const u16* nb, unsigned nvoff, u32x4& r) {
  if constexpr (F <= 31)
    asm volatile("global_load_dwordx4 %0, %1, %2 offset:%3" : "=v"(r) : "v"(nvoff), "s"(nb + (F >> 1) * 2048), "i"((F & 1) * 1024) : "memory");
}
struct K2GS { f32x4 g0, g1, s0, s1; };
DI K2GS k2_gs(unsigned gsa) {
  K2GS r;
  r.g0 = *(const LAS f32x4*)(uintptr_t)(gsa); r.g1 = *(const LAS f32x4*)(uintptr_t)(gsa + 16);
  r.s0 = *(const LAS f32x4*)(uintptr_t)(gsa + 4096); r.s1 = *(const LAS f32x4*)(uintptr_t)(gsa + 4096 + 16);
  return r;
}
template <int WAIT>
DI u32x4 k2_xform(u32x4& x, const K2GS& q) {
  asm volatile("s_waitcnt vmcnt(%1)" : "+v"(x) : "i"(WAIT) : "memory");
  unsigned t0, t1, t2, t3, o0, o1, o2, o3;
  asm volatile(
      "v_lshlrev_b32 %4, 16, %24\n\tv_lshlrev_b32 %5, 16, %25\n\tv_lshlrev_b32 %6, 16, %26\n\tv_lshlrev_b32 %7, 16, %27\n\t"
      "v_fma_f32 %0, %24, %9, %17\n\tv_fma_f32 %1, %25, %11, %19\n\tv_fma_f32 %2, %26, %13, %21\n\tv_fma_f32 %3, %27, %15, %23\n\t"
      "v_fma_f32 %4, %4, %8, %16\n\tv_fma_f32 %5, %5, %10, %18\n\tv_fma_f32 %6, %6, %12, %20\n\tv_fma_f32 %7, %7, %14, %22\n\t"
      "v_cvt_pk_bf16_f32 %0, %4, %0\n\tv_cvt_pk_bf16_f32 %1, %5, %1\n\tv_cvt_pk_bf16_f32 %2, %6, %2\n\tv_cvt_pk_bf16_f32 %3, %7, %3\n\t"
      "s_nop 1"
      : "=&v"(o0), "=&v"(o1), "=&v"(o2), "=&v"(o3), "=&v"(t0), "=&v"(t1), "=&v"(t2), "=&v"(t3)
      : "v"(q.g0[0]), "v"(q.g0[1]), "v"(q.g0[2]), "v"(q.g0[3]), "v"(q.g1[0]), "v"(q.g1[1]), "v"(q.g1[2]), "v"(q.g1[3]),
        "v"(q.s0[0]), "v"(q.s0[1]), "v"(q.s0[2]), "v"(q.s0[3]), "v"(q.s1[0]), "v"(q.s1[1]), "v"(q.s1[2]), "v"(q.s1[3]),
        "v"(x[0]), "v"(x[1]), "v"(x[2]), "v"(x[3]));
  return (u32x4){o0, o1, o2, o3};
}

struct K2State {
  f32x16 acc[2][3];
  bf16x8 fb[2][6];
  u32x4 nr[K2_DN];
  u32x4 hf[2];
  K2GS gs;
  const u16* ob;
};
template <int P>
DI bf16x8 k2_ldb(const unsigned (&bb)[2][2], int hd, int sg) {
  constexpr int slot = (P >> 1) % K2_NB;
  const unsigned a = bb[hd][slot / 3] + (slot % 3) * K2_BSLOT + (P & 1) * 6144 + (sg == 0 ? 0 : sg == 1 ? 2048 : 1024);
  return cat8(lds_tr(a), lds_tr(a + 512));
}
template <int P>
DI void k2_phase(K2State& st, const unsigned (&bb)[2][2], unsigned gsa, const u16* wq, unsigned wvoff, unsigned wdst,
                 const u16* nb, unsigned nvoff, unsigned lds0) {
  constexpr int C = P & 1, N = C ^ 1, T = P >> 1, J = P & 1;
  constexpr bool NEXT = P < 31;
  const bf16x8 hf = __builtin_bit_cast(bf16x8, st.hf[C]);
#define LDB2(I0) do { if constexpr (NEXT) { st.fb[N][I0] = k2_ldb<P + 1>(bb, (I0) / 3, (I0) % 3); \
                                            st.fb[N][I0 + 1] = k2_ldb<P + 1>(bb, (I0 + 1) / 3, (I0 + 1) % 3); } } while (0)
#define MM(I) do { SBAR();                                                                                   \
    if ((I) % 3 == 2) st.acc[(I) / 3][2] = MFMA32(hf, st.fb[C][I], st.acc[(I) / 3][2]);                      \
    else st.acc[(I) / 3][(I) % 3] = MFMA32(st.fb[C][I], hf, st.acc[(I) / 3][(I) % 3]);                       \
    SBAR(); } while (0)
  if constexpr (J == 0) TR(lds0 + K2_LDS_TR, 0, T, 0);
  MM(0);
  if constexpr (NEXT) st.hf[N] = k2_xform<K2S.wn[P]>(st.nr[(P + 1) % K2_DN], st.gs);
  MM(1);
  if constexpr (J == 1 && NEXT) {
    TR(lds0 + K2_LDS_TR, 0, T, 1);
    asm volatile("s_waitcnt vmcnt(%0)" ::"i"(K2S.ww[T]) : "memory");
    TR(lds0 + K2_LDS_TR, 0, T, 2);
    asm volatile("s_barrier" ::: "memory");
    TR(lds0 + K2_LDS_TR, 0, T, 3);
  }
  LDB2(0);
  k2_issue_n<P + 1 + K2_DN>(nb, nvoff, st.nr[(P + 1) % K2_DN]);
  MM(2);
  LDB2(2);
  k2_issue_b<T + K2_DW, J == 0 ? 0 : 2>(wq, wvoff, wdst);
  MM(3);
  LDB2(4);
  if constexpr (J == 0) k2_issue_b<T + K2_DW, 1>(wq, wvoff, wdst);
  MM(4);
  if constexpr (k2_touch(P) >= 0)
    asm volatile("s_nop 0\n\tglobal_store_dwordx4 %0, %1, %2 offset:%3 sc1\n\ts_nop 1"
                 :
                 : "v"(nvoff), "v"((u32x4){POISON, POISON, POISON, POISON}), "s"(st.ob), "i"(k2_touch(P) * 1024)
                 : "memory");
  if constexpr (P < 30) st.gs = k2_gs(gsa + ((P + 2) >> 1) * 256 + (P & 1) * 64);
  MM(5);
#undef LDB2
#undef MM
}
template <int P>
DI void k2_phases(K2State& st, const unsigned (&bb)[2][2], unsigned gsa, const u16* wq, unsigned wvoff, unsigned wdst,
                  const u16* nb, unsigned nvoff, unsigned lds0) {
  k2_phase<P>(st, bb, gsa, wq, wvoff, wdst, nb, nvoff, lds0);
  if constexpr (P < 31) k2_phases<P + 1>(st, bb, gsa, wq, wvoff, wdst, nb, nvoff, lds0);
}

#define GAS __attribute__((address_space(1)))
typedef GAS unsigned long long gu64;
typedef GAS unsigned gu32;
#define RLX_AGENT __ATOMIC_RELAXED, __HIP_MEMORY_SCOPE_AGENT
constexpr unsigned SPIN_CAP = 1u << 18;
DI unsigned long long mk64(unsigned lo, unsigned hi) { return ((unsigned long long)hi << 32) | lo; }
template <int CTRL>
DI unsigned dpp_maxu(unsigned v) {
  const unsigned o = __builtin_amdgcn_update_dpp(0u, v, CTRL, 0xf, 0xf, true);
  return v > o ? v : o;
}
DI unsigned rowmaxu(unsigned v) {
  v = dpp_maxu<0xB1>(v); v = dpp_maxu<0x4E>(v); v = dpp_maxu<0x141>(v); return dpp_maxu<0x140>(v);
}
DI float sum8(float v) { return dpp_add<0x141>(dpp_add<0x4E>(dpp_add<0xB1>(v))); }
DI float sumrows(float v) { v = swap16_add(v, v); return swap32_add(v, v); }
DI f32x2 up2(unsigned w) { return (f32x2){bflo(w), bfhi(w)}; }
DI unsigned okey(float v) {
  const unsigned raw = __float_as_uint(v);
  return v == v ? (raw ^ ((unsigned)((int)raw >> 31) | 0x80000000u)) : 0u;
}
#define GLOADC(dst, voff, sbase, off) \
  asm volatile("global_load_dwordx4 %0, %1, %2 offset:%3 sc1" : "=v"(dst) : "v"(voff), "s"(sbase), "i"(off) : "memory")
template <class T> DI T ldsr(unsigned a) { return *(const LAS T*)(uintptr_t)a; }
template <class T> DI void ldsw(unsigned a, T v) { *(LAS T*)(uintptr_t)a = v; }
DI float ex2(float x) { return __builtin_amdgcn_exp2f(x); }
DI float xhalf_max(float v) {
  auto rr = __builtin_amdgcn_permlane32_swap(__float_as_uint(v), __float_as_uint(v), false, false);
  return fmaxf(__uint_as_float(rr[0]), __uint_as_float(rr[1]));
}
DI void lds_arrive(unsigned a) {
  asm volatile("s_waitcnt lgkmcnt(0)\n\ts_mov_b64 exec, 1\n\tds_add_u32 %0, %1\n\ts_mov_b64 exec, -1" ::"v"(a), "v"(1u) : "memory");
}
DI unsigned lds_peek(unsigned a) { return __hip_atomic_load((LAS unsigned*)(uintptr_t)a, __ATOMIC_RELAXED, __HIP_MEMORY_SCOPE_WORKGROUP); }
DI void lds_await(unsigned a, unsigned n) {
  if (__builtin_expect(lds_peek(a) < n, 0)) {
    do __builtin_amdgcn_s_sleep(2); while (lds_peek(a) < n);
  }
  asm volatile("" ::: "memory");
}

#define K4_R 8
#define HD0 30
#define HD0R 8
#define HD1 16
#define HD2 32
#define HD3 16
#define HD4 16
#define PG1 4
#define PG2 PG1
#define PG3 PG1
#define PG4 PG1
#define HT(i)
#define FT(i)
#define GT(i) TS(i)
#define KVF_AT 1
#define HDK 10
#define HDKR 8
#define SH_D 4
#define SH_W1 8
#define SH_L1 4
#define VECW 0
#define SH_END 24
#define HD0B 10
constexpr int K4_SM = 153600;
constexpr int K4_VEC = K4_SM + 2048;
constexpr int K4_FIN = K4_VEC + 1536;
constexpr int K4_RED = K4_FIN + 512;
constexpr int K3_CNT = K4_RED + 1024;
constexpr int K4_IMG = 0;
constexpr int K3_QX = 49152;
constexpr int K3_LDS = 163840, K3_PART = 4096 + 256;
static_assert(K4_VEC >= K2_LDS_GS + 8192 && K3_CNT + 64 <= K3_LDS && 12 * K3_PART <= 65536 && 98304 + 12 * K3_PART <= K4_SM, "LDS map");
constexpr int K3_DUMP = K3_LDS - 1024;
static_assert(K3_CNT + 64 <= K3_DUMP, "LDS map");

__global__ __launch_bounds__(512) void k2_main(const u16* __restrict__ nf, const u16* __restrict__ wqkv,
                                               const float* __restrict__ modp, u16* kvf, u16* of,
                                               const u16* __restrict__ wo, const u16* __restrict__ x,
                                               const u16* __restrict__ wr, const u16* __restrict__ wg,
                                               const u16* __restrict__ wu, const u16* __restrict__ wd,
                                               u16* __restrict__ out, unsigned long long* sync) {
  PROF_BEGIN();
  extern __shared__ __attribute__((aligned(16))) char smem[];
  const unsigned lds0 = (unsigned)(uintptr_t)smem;
  const int tid = threadIdx.x, lane = tid & 63;
  const int wid = __builtin_amdgcn_readfirstlane(tid >> 6);
  const int b = blockIdx.x, xcd = b & 7, jj = b >> 3;
  const int mt = (xcd & 1) * 8 + (jj & 7), nt = (xcd >> 1) * 4 + (jj >> 3);
  const int qg = wid >> 2, kq = wid & 3;
  const int ot = (kq + 3) & 3;
  const int kr = lane >> 3, pos = lane & 7;
  const bool has_gu = b < 224;
  const int cg = b % 56, ks = has_gu ? b / 56 : 0;
  const int dcg = b & 15, dks = b >> 4;
  asm volatile("" : "+s"(kvf), "+s"(of), "+s"(wo), "+s"(x), "+s"(wr), "+s"(wg), "+s"(wu), "+s"(wd), "+s"(out), "+s"(sync));
  TS_DECL(); TS(0);
  u32x4 fq[2], fk[2], fv[2];
  {
    const int wm = kq, kh = qg;
    K2State st;
    if (tid < 16) ldsw(lds0 + K3_CNT + tid * 4, 0u);
    st.ob = of + ((size_t)(((mt * 4 + ot) * 2 + qg) * 16 + nt) * 2) * 512;
    f32x4 mp0, mp1;
    {
      const unsigned vo = (tid & 255) * 16;
      const float* mb = modp + (kh == 0 ? 1024 : 0);
      GLOAD16(mp0, vo, mb);
      GLOAD16(mp1, vo, mb + 6144);
    }
    const unsigned wvoff = (unsigned)(((wid * 8 + kr) * 3072 + (((pos >> 2) ^ ((kr >> 1) & 1)) * 32) + (pos & 3) * 8) * 2);
    const u16* wq = wqkv + nt * 64;
    const unsigned wdst = lds0 + wid * 3072;
    const u16* nb = nf + (size_t)(mt * 4 + wm) * 32768 + kh * 1024;
    const unsigned nvoff = lane * 16;
#define WT(KT) do { if constexpr (KT < K2_DW) { k2_issue_b<KT, 0>(wq, wvoff, wdst); k2_issue_b<KT, 1>(wq, wvoff, wdst); \
                                                  k2_issue_b<KT, 2>(wq, wvoff, wdst); } } while (0)
#define NT(F) do { if constexpr (F < K2_DN) k2_issue_n<F>(nb, nvoff, st.nr[(F) % K2_DN]); } while (0)
    NT(0); WT(0); NT(1); NT(2); WT(1); NT(3); NT(4); WT(2); NT(5); NT(6); WT(3); NT(7); NT(8);
#undef WT
#undef NT
    TR(lds0 + K2_LDS_TR, 0, 16, 1);
    asm volatile("s_waitcnt vmcnt(%2)" : "+v"(mp0), "+v"(mp1) : "i"(K2S.wn[32]) : "memory");
    {
      f32x4 v;
#pragma unroll
      for (int j = 0; j < 4; ++j) {
        v[j] = rbf(mp0[j] + mp1[j]);
        if (kh == 0) v[j] = rbf(1.f + v[j]);
      }
      *(LAS f32x4*)(uintptr_t)(lds0 + K2_LDS_GS + tid * 16) = v;
    }
    const int h = lane >> 5, q4 = (lane & 15) >> 2, p4 = lane & 3, ch = (lane >> 4) & 1;
    unsigned bb[2][2];
#pragma unroll
    for (int hd = 0; hd < 2; ++hd) {
      bb[hd][0] = lds0 + kh * 12288 + h * 3072 + q4 * 128 + (((hd ^ kh) ^ ((q4 >> 1) & 1)) * 64) + ch * 32 + p4 * 8;
      bb[hd][1] = bb[hd][0] + 3 * K2_BSLOT;
      asm volatile("" : "+v"(bb[hd][0]), "+v"(bb[hd][1]));
    }
    unsigned gsa = lds0 + K2_LDS_GS + (32 * kh + 8 * h) * 4;
    asm volatile("" : "+v"(gsa));
#pragma unroll
    for (int a = 0; a < 2; ++a)
#pragma unroll
      for (int b = 0; b < 3; ++b) st.acc[a][b] = (f32x16){};
    asm volatile("s_waitcnt vmcnt(%0) lgkmcnt(0)\n\ts_barrier" ::"i"(K2S.ww[16]) : "memory");
    TR(lds0 + K2_LDS_TR, 0, 16, 2);
    TS(1);
    st.gs = k2_gs(gsa);
#pragma unroll
    for (int i = 0; i < 6; ++i) st.fb[0][i] = k2_ldb<0>(bb, i / 3, i % 3);
    st.hf[0] = k2_xform<K2S.ww[16]>(st.nr[0], st.gs);
    k2_issue_n<K2_DN>(nb, nvoff, st.nr[0]);
    st.gs = k2_gs(gsa + 64);
    k2_phases<0>(st, bb, gsa, wq, wvoff, wdst, nb, nvoff, lds0);
    TS(2);
    const unsigned ibo = lds0 + (qg == 1 ? 0 : 98304) + kq * 12288 + lane * 16;
    const unsigned ibm = lds0 + (qg == 0 ? 0 : 98304) + kq * 12288 + lane * 16;
#pragma unroll
    for (int sg = 0; sg < 3; ++sg)
#pragma unroll
      for (int g = 0; g < 4; ++g)
        ldsw(ibo + sg * 4096 + g * 1024,
             (f32x4){st.acc[1][sg][4 * g], st.acc[1][sg][4 * g + 1], st.acc[1][sg][4 * g + 2], st.acc[1][sg][4 * g + 3]});
    FT(9);
    LDS_BARRIER();
    FT(10);
    f32x4 in[12];
#pragma unroll
    for (int i = 0; i < 12; ++i) in[i] = ldsr<f32x4>(ibm + (i < 8 ? 4096 + i * 1024 : (i - 8) * 1024));
#pragma unroll
    for (int i = 0; i < 3; ++i) {
      const int sg = i < 2 ? i + 1 : 0;
      const float sc = sg == 0 ? QSCALE : 1.f;
#pragma unroll
      for (int s = 0; s < 2; ++s) {
        const f32x4 a = in[i * 4 + s * 2], c = in[i * 4 + s * 2 + 1];
        u32x4 o;
#pragma unroll
        for (int j = 0; j < 4; ++j) {
          const float lo = st.acc[0][sg][8 * s + 2 * j] + (j < 2 ? a[2 * j] : c[2 * j - 4]);
          const float hi = st.acc[0][sg][8 * s + 2 * j + 1] + (j < 2 ? a[2 * j + 1] : c[2 * j - 3]);
          o[j] = pack2(lo * sc, hi * sc);
        }
        if (sg == 0) { fq[s] = o; ldsw(lds0 + K3_QX + wid * 2048 + s * 1024 + lane * 16, o); }
        else {
          if (sg == 1) fk[s] = o; else fv[s] = o;
          st16(kvf + (size_t)(sg - 1) * (2u << 20) + ((size_t)((nt * 2 + qg) * 64 + mt * 4 + kq) * 2 + s) * 512 + lane * 8, o);
        }
      }
    }
  }
  u16* const op = of + ((size_t)(((mt * 4 + ot) * 2 + qg) * 16 + nt) * 2) * 512 + lane * 8;
  const int erow = wid * 16 + kr;
  bf16x8 wgu[16], wdn[7];
  u32x4 xr[2];
  u32x4 ofr[16];
  const u16* const ob = of + (size_t)(mt * 4 + kq) * 32768 + qg * 8192;
#define OLOAD(F0, BASE)                                                                              \
  _Pragma("unroll") for (int f = (F0); f < 16; ++f)                                                  \
    asm volatile("global_load_dwordx4 %0, %1, %2 offset:%3 sc1"                                      \
                 : "+v"(ofr[f]) : "v"(lane * 16), "s"((BASE) + (f >> 2) * 2048), "i"((f & 3) * 1024) : "memory")
#define OL4(A, B, C, D, S)                                                                           \
  "global_load_dwordx4 %" #A ", %16, %" #S " sc1\n\tglobal_load_dwordx4 %" #B ", %16, %" #S " offset:1024 sc1\n\t" \
  "global_load_dwordx4 %" #C ", %16, %" #S " offset:2048 sc1\n\tglobal_load_dwordx4 %" #D ", %16, %" #S " offset:3072 sc1\n\t"
#define OLOAD_G(G)                                                                                   \
  asm volatile("s_cmp_lg_u32 %21, " #G "\n\ts_cbranch_scc1 1f\n\t"                                   \
               OL4(0, 1, 2, 3, 17) OL4(4, 5, 6, 7, 18) OL4(8, 9, 10, 11, 19) OL4(12, 13, 14, 15, 20) "1:" \
               : "+v"(ofr[0]), "+v"(ofr[1]), "+v"(ofr[2]), "+v"(ofr[3]), "+v"(ofr[4]), "+v"(ofr[5]), "+v"(ofr[6]), "+v"(ofr[7]), \
                 "+v"(ofr[8]), "+v"(ofr[9]), "+v"(ofr[10]), "+v"(ofr[11]), "+v"(ofr[12]), "+v"(ofr[13]), "+v"(ofr[14]), "+v"(ofr[15]) \
               : "v"(lane * 16), "s"(ob), "s"(ob + 2048), "s"(ob + 4096), "s"(ob + 6144), "s"(qg)    \
               : "memory", "scc")
#define XLOAD_G(G)                                                                                   \
  asm volatile("s_cmp_lg_u32 %5, " #G "\n\ts_cbranch_scc1 1f\n\t"                                    \
               "global_load_dwordx4 %0, %2, %3" XRPOL "\n\tglobal_load_dwordx4 %1, %2, %4" XRPOL "\n\t1:" \
               : "+v"(xr[0]), "+v"(xr[1])                                                            \
               : "v"((erow * D + pos * 8) * 2), "s"(x + (size_t)(mt * 128) * D + nt * 64),           \
                 "s"(x + (size_t)(mt * 128 + 8) * D + nt * 64), "s"(qg)                              \
               : "memory", "scc")
  const unsigned wvoff = (unsigned)(((kq * 8 + kr) * D + (((pos >> 2) ^ ((kr >> 1) & 1)) * 32) + (pos & 3) * 8) * 2);
  const u16* wb = wo + nt * 64;
#define WDMA(P, HF)                                                                                  \
  asm volatile("s_nop 0\n\tglobal_load_lds_dwordx4 %0, %1"                                           \
               :                                                                                     \
               : "v"(wvoff), "s"(wb + (size_t)((P) * 64 + (HF) * 32) * D),                           \
                 "{m0}"(lds0 + K4_IMG + (HF) * 65536 + (P) * 4096 + kq * 1024)                       \
               : "memory")
  const int sc = pos ^ ((kr & 2) | ((kq & 1) << 2));
  const unsigned vgu = (unsigned)(((kq * 8 + kr) * FF + sc * 8) * 2), vdn = (unsigned)(((kq * 8 + kr) * D + sc * 8) * 2);
  const size_t gbase = (size_t)(ks * 256) * FF + cg * 64;
#define SDMA(U)                                                                                      \
  asm volatile("s_nop 0\n\tglobal_load_lds_dwordx4 %0, %1" SDPOL                                     \
               :                                                                                     \
               : "v"(sv), "s"(sp + ((U) == 3 ? s3 : (U) * ss)), "{m0}"(sl + (U) * 4096)              \
               : "memory")
  {
    const int head = nt * 2 + qg;
    const __amdgpu_buffer_rsrc_t rs = __builtin_amdgcn_make_buffer_rsrc((void*)kvf, 0, 8 << 20, 0x00020000);
    const int vk = lane * 16, vv = lane * 16 + (4 << 20);
    const int hb = head * 131072 + kq * 2048;
#define LDK(so, c) __builtin_bit_cast(bf16x8, __builtin_amdgcn_raw_buffer_load_b128(rs, vk + (c), (so), 16))
#define LDV(so, c) __builtin_bit_cast(bf16x8, __builtin_amdgcn_raw_buffer_load_b128(rs, vv + (c), (so), 16))
#define TOFF(i) (hb + ((mt + (i)) & 15) * 8192)
    FT(11);
    LDS_BARRIER();
    bf16x8 q[4][2];
    q[0][0] = __builtin_bit_cast(bf16x8, fq[0]); q[0][1] = __builtin_bit_cast(bf16x8, fq[1]);
#pragma unroll
    for (int i = 1; i < 4; ++i) {
      const unsigned qa = lds0 + K3_QX + (qg * 4 + ((i + kq) & 3)) * 2048 + lane * 16;
      q[i][0] = lds_b128(qa); q[i][1] = lds_b128(qa + 1024);
    }
    bf16x8 ka0 = __builtin_bit_cast(bf16x8, fk[0]), ka1 = __builtin_bit_cast(bf16x8, fk[1]);
    bf16x8 va0 = __builtin_bit_cast(bf16x8, fv[0]), va1 = __builtin_bit_cast(bf16x8, fv[1]);
    bf16x8 kb0, kb1, vb0, vb1;
    bf16x8 ones;
    {
      const short o = ((((lane & 15) >> 2) & 1) == ((lane >> 4) & 1)) ? (short)0x3F80 : (short)0;
#pragma unroll
      for (int j = 0; j < 8; ++j) ones[j] = o;
    }
    const f32x16 zero = (f32x16){};
    f32x16 o[4] = {zero, zero, zero, zero};
    f32x4 l[4] = {(f32x4){}, (f32x4){}, (f32x4){}, (f32x4){}};
    f32x16 sA = MFMA32(ka0, q[0][0], zero), sB;
    sA = MFMA32(ka1, q[0][1], sA);
    asm volatile("s_waitcnt lgkmcnt(0)\n\ts_barrier"
                 : "+v"(q[1][0]), "+v"(q[1][1]), "+v"(q[2][0]), "+v"(q[2][1]), "+v"(q[3][0]), "+v"(q[3][1])::"memory");
    TS(3);
    u32x4 p0, p1 = (u32x4){0u, 0u, 0u, 0u};
#define PK(S_, i) pack2(S_[i], S_[i + 1])
#define EX(S_, i) S_[i] = ex2(S_[i])
#define PIN(x_) asm volatile("" : "+v"(x_))
#define UNIT(SC, SN, KN0, KN1, N, C, V0C, P, V1P, LD_A, LD_B)                               \
  do {                                                                                     \
    SN = MFMA32(KN0, q[N][0], zero); SBAR();                                               \
    EX(SC, 0); EX(SC, 1); EX(SC, 2); p0[0] = PK(SC, 0); PIN(p0); SBAR();                   \
    o[P] = MFMA32(V1P, __builtin_bit_cast(bf16x8, p1), o[P]); SBAR();                      \
    LD_A;                                                                                  \
    EX(SC, 3); EX(SC, 4); EX(SC, 5); p0[1] = PK(SC, 2); PIN(p0); SBAR();                   \
    l[P] = MFMA16(ones, __builtin_bit_cast(bf16x8, p1), l[P]); SBAR();                     \
    EX(SC, 6); EX(SC, 7); p0[2] = PK(SC, 4); p0[3] = PK(SC, 6); PIN(p0); SBAR();           \
    SN = MFMA32(KN1, q[N][1], SN); SBAR();                                                 \
    LD_B;                                                                                  \
    EX(SC, 8); EX(SC, 9); EX(SC, 10); p1[0] = PK(SC, 8); PIN(p1); SBAR();                  \
    o[C] = MFMA32(V0C, __builtin_bit_cast(bf16x8, p0), o[C]); SBAR();                      \
    EX(SC, 11); EX(SC, 12); EX(SC, 13); p1[1] = PK(SC, 10); PIN(p1); SBAR();               \
    l[C] = MFMA16(ones, __builtin_bit_cast(bf16x8, p0), l[C]); SBAR();                     \
    EX(SC, 14); EX(SC, 15); p1[2] = PK(SC, 12); p1[3] = PK(SC, 14); PIN(p1); SBAR();       \
  } while (0)
    const unsigned fl = (unsigned)((SY_KV + (head * 4 + kq) * 16) * 8);
    const unsigned fo = (unsigned)((SY_OC + (mt * 4 + ot) * 16) * 8);
#define KVFLAG()                                                                                       \
  do {                                                                                                 \
    asm volatile("s_waitcnt vmcnt(0)" ::: "memory");                                                   \
    if (lane < 2) __hip_atomic_store((gu32*)((char*)sync + (lane ? fo + head * 4 : fl + mt * 4)), 1u, RLX_AGENT); \
    FT(13);                                                                                            \
    SBAR();                                                                                            \
  } while (0)
    UNIT(sA, sB, ka0, ka1, 1, 0, va0, 3, va1  , (void)0, (void)0);
    KVFLAG();
    UNIT(sB, sA, ka0, ka1, 2, 1, va0, 0, va1, (void)0, (void)0);
    UNIT(sA, sB, ka0, ka1, 3, 2, va0, 1, va1, (void)0, (void)0);
    {
      const unsigned vo = fl + (lane & 15) * 4;
      if (HDK) __builtin_amdgcn_s_sleep(HDK);
      _Pragma("nounroll") for (unsigned spin = 0; spin < SPIN_CAP; ++spin) {
        unsigned c;
        asm volatile("global_load_dword %0, %1, %2 sc1\n\ts_waitcnt vmcnt(0)" : "=v"(c) : "v"(vo), "s"(sync) : "memory");
        if (__all(c != 0u)) break;
        __builtin_amdgcn_s_sleep(HDKR);
      }
    }
    TS(4);
    kb0 = LDK(TOFF(1), 0); kb1 = LDK(TOFF(1), 1024); SBAR();
    vb0 = LDV(TOFF(1), 0); vb1 = LDV(TOFF(1), 1024); SBAR();
    ka0 = LDK(TOFF(2), 0); ka1 = LDK(TOFF(2), 1024); SBAR();
#define WARM(I)                                                                                              \
  asm volatile("s_nop 0\n\tglobal_load_lds_dwordx4 %0, %1" WOPOL                                             \
               :                                                                                             \
               : "v"(wvoff), "s"(wb + (size_t)((mt & 7) * 128 + (I) * 32) * D), "{m0}"(lds0 + K3_DUMP)       \
               : "memory")
#define WARM_G 1
#define WARM_AT 0
#define XWARM(I)                                                                                             \
  asm volatile("s_nop 0\n\tglobal_load_lds_dwordx4 %0, %1" XWPOL                                             \
               :                                                                                             \
               : "v"(wvoff), "s"(x + (size_t)(mt * 128 + (I) * 32) * D + nt * 64), "{m0}"(lds0 + K3_DUMP)    \
               : "memory")
#define WARM4(AT) do { if (WARM_AT == (AT)) { if (qg == WARM_G) { WARM(0); WARM(1); WARM(2); WARM(3); } SBAR(); }          \
                       if (XW_AT == (AT)) { if (qg == XW_G) { XWARM(0); XWARM(1); XWARM(2); XWARM(3); } SBAR(); } } while (0)
    WARM4(0);
    UNIT(sB, sA, kb0, kb1, 0, 3, va0, 2, va1, (void)0, (void)0);
    UNIT(sA, sB, kb0, kb1, 1, 0, vb0, 3, va1, va0 = LDV(TOFF(2), 0), va1 = LDV(TOFF(2), 1024));
    UNIT(sB, sA, kb0, kb1, 2, 1, vb0, 0, vb1, (void)0, (void)0);
    UNIT(sA, sB, kb0, kb1, 3, 2, vb0, 1, vb1, (void)0, (void)0);
    UNIT(sB, sA, ka0, ka1, 0, 3, vb0, 2, vb1, kb0 = LDK(TOFF(3), 0), kb1 = LDK(TOFF(3), 1024));
    const unsigned rb = lds0 + 98304;
#define SEND(I)                                                                                                          \
  do {                                                                                                                   \
    const unsigned wa = rb + ((((I) + kq + 1) & 3) * 3 + (I)) * K3_PART;                                                 \
    _Pragma("unroll") for (int g = 0; g < 4; ++g)                                                                        \
      ldsw(wa + g * 1024 + lane * 16, (f32x4){o[I][4 * g], o[I][4 * g + 1], o[I][4 * g + 2], o[I][4 * g + 3]});          \
    ldsw(wa + 4096 + lane * 4, l[I][0]);                                                                                 \
  } while (0)
#define PASS(X)                                                                                      \
  do {                                                                                               \
    const int t1 = TOFF(j + 1), t2 = TOFF(j + 2), t3 = TOFF(j + 3);                                  \
    UNIT(sA, sB, ka0, ka1, 1, 0, va0, 3, vb1, vb0 = LDV(t1, 0), vb1 = LDV(t1, 1024));                \
    UNIT(sB, sA, ka0, ka1, 2, 1, va0, 0, va1, X(0), (void)0);                                        \
    UNIT(sA, sB, ka0, ka1, 3, 2, va0, 1, va1, X(1), (void)0);                                        \
    UNIT(sB, sA, kb0, kb1, 0, 3, va0, 2, va1, ka0 = LDK(t2, 0), ka1 = LDK(t2, 1024));                \
    UNIT(sA, sB, kb0, kb1, 1, 0, vb0, 3, va1, va0 = LDV(t2, 0), va1 = LDV(t2, 1024));                \
    UNIT(sB, sA, kb0, kb1, 2, 1, vb0, 0, vb1, X(2), (void)0);                                        \
    UNIT(sA, sB, kb0, kb1, 3, 2, vb0, 1, vb1, X(3), (void)0);                                        \
    UNIT(sB, sA, ka0, ka1, 0, 3, vb0, 2, vb1, kb0 = LDK(t3, 0), kb1 = LDK(t3, 1024));                \
  } while (0)
#define NOP1(U) (void)0
    WARM4(1);
    if (qg == 0) {
      const unsigned zo = 8 * (lane < 32 ? SY_ST + (mt * 16 + nt) * STP + kq * 64 + lane * 2
                               : lane < 40 ? SY_GP + ks * GPP + cg * 64 + kq * 16 + (lane - 32) * 2
                                           : SY_GY + dks * GYP + dcg * 32 + kq * 8 + (lane & 3) * 2);
      asm volatile("s_mov_b64 exec, %3\n\tglobal_store_dwordx4 %0, %1, %2 sc1\n\ts_mov_b64 exec, -1\n\ts_nop 0"
                   :
                   : "v"(zo), "v"((u32x4){0u, 0u, 0u, 0u}), "s"(sync), "s"(0xfffffffffffull)
                   : "memory");
    }
    SBAR();
    int j = 2, e0 = qg == 0 ? 14 : 2;
    asm volatile("" : "+s"(e0));
#pragma unroll 1
    for (; j < e0; j += 2) {
      const unsigned sv = j < 10 ? vgu : vdn, sl = lds0 + (j - 2) * 8192 + kq * 1024;
      const int ss = j < 10 ? 32 * FF : 32 * D, s3 = j == 12 ? 2 * ss : 3 * ss;
      const u16* sp = (j < 6 ? wg + gbase : j < 10 ? wu + gbase : wd + (size_t)(dks * 224) * D + dcg * 64) + ((j - 2) & 2) * 2 * ss;
      PASS(SDMA);
    }
#pragma unroll 1
    for (; j < 14; j += 2) PASS(NOP1);
    UNIT(sA, sB, ka0, ka1, 1, 0, va0, 3, vb1, vb0 = LDV(TOFF(15), 0), vb1 = LDV(TOFF(15), 1024));
    WARM4(2);
    UNIT(sB, sA, ka0, ka1, 2, 1, va0, 0, va1, (void)0, (void)0);
    UNIT(sA, sB, ka0, ka1, 3, 2, va0, 1, va1, (void)0, (void)0);
    UNIT(sB, sA, kb0, kb1, 0, 3, va0, 2, va1, (void)0, (void)0);
    UNIT(sA, sB, kb0, kb1, 1, 0, vb0, 3, va1, (void)0, lds_await(lds0 + K3_CNT + 4, qg * 4u));
    UNIT(sB, sA, kb0, kb1, 2, 1, vb0, 0, vb1, (void)0, SEND(0));
    UNIT(sA, sB, kb0, kb1, 3, 2, vb0, 1, vb1, (void)0, SEND(1));
    UNIT(sB, sA, kb0, kb1, 0, 3, vb0, 2, vb1, (void)0, SEND(2));
    o[3] = MFMA32(vb1, __builtin_bit_cast(bf16x8, p1), o[3]);
    l[3] = MFMA16(ones, __builtin_bit_cast(bf16x8, p1), l[3]);
#undef UNIT
    TS(5);
    if (qg == 1) ldsw(lds0 + K3_CNT + 32 + kq * 4, 1u);
    lds_arrive(lds0 + K3_CNT);
    HT(9);
    const unsigned fr = (unsigned)((SY_OC + (mt * 4 + kq) * 16) * 8 + (lane & 31) * 4);
    unsigned fc;
    asm volatile("" : "=v"(fc), "=v"(xr[0]), "=v"(xr[1]));
#pragma unroll
    for (int f = 0; f < 16; ++f) asm volatile("" : "=v"(ofr[f]));
    asm volatile("s_cmp_lg_u32 %3, 0\n\ts_cbranch_scc1 1f\n\tglobal_load_dword %0, %1, %2 sc1\n\t1:"
                 : "+v"(fc) : "v"(fr), "s"(sync), "s"(qg) : "memory", "scc");
    XLOAD_G(1);
    OLOAD_G(1);
    HT(10);
    lds_await(lds0 + K3_CNT, qg == 0 ? 4u : 8u);
    HT(11);
    f32x16 oo = o[3];
    float lsum = l[3][0];
#pragma unroll
    for (int i = 0; i < 3; ++i) {
      const unsigned ra = rb + (kq * 3 + i) * K3_PART;
#pragma unroll
      for (int g = 0; g < 4; ++g) {
        const f32x4 v = ldsr<f32x4>(ra + g * 1024 + lane * 16);
        oo[4 * g] += v[0]; oo[4 * g + 1] += v[1]; oo[4 * g + 2] += v[2]; oo[4 * g + 3] += v[3];
      }
      lsum += ldsr<float>(ra + 4096 + lane * 4);
    }
    PIN(oo); PIN(lsum);
    HT(12);
    if (qg == 0) {
      asm volatile("s_waitcnt vmcnt(0)" : "+v"(fc)::"memory");
      _Pragma("nounroll") for (unsigned spin = 0; spin < SPIN_CAP && !__all(fc != 0u); ++spin) {
        __builtin_amdgcn_s_sleep(8);
        asm volatile("global_load_dword %0, %1, %2 sc1\n\ts_waitcnt vmcnt(0)" : "=v"(fc) : "v"(fr), "s"(sync) : "memory");
      }
    }
    lds_arrive(lds0 + K3_CNT + (qg == 0 ? 4 : 16));
    HT(13);
    float rl = 1.f / lsum;
    bool bad = !(lsum < 1.0e30f && lsum > 1.0e-30f);
#pragma unroll
    for (int r = 0; r < 16; ++r) {
      oo[r] *= rl;
      bad |= !(fabsf(oo[r]) < 3.0e38f);
    }
    if (__builtin_expect(__any(bad), 0)) {
      asm volatile("s_waitcnt vmcnt(0)" ::: "memory");
      const bf16x8 q0 = q[3][0], q1 = q[3][1];
      float m = -INFINITY, ls = 0.f;
      oo = zero;
      for (int t = 0; t < 64; ++t) {
        const int so = head * 131072 + t * 2048;
        const bf16x8 a0 = LDK(so, 0), a1 = LDK(so, 1024), b0 = LDV(so, 0), b1 = LDV(so, 1024);
        f32x16 s = MFMA32(a0, q0, zero);
        s = MFMA32(a1, q1, s);
        float tm = s[0];
#pragma unroll
        for (int r = 1; r < 16; ++r) tm = fmaxf(tm, s[r]);
        tm = xhalf_max(tm);
        const float mn = fmaxf(m, tm);
        const float alpha = ex2(m - mn);
        m = mn;
        u32x4 pa, pb;
        float ps = 0.f;
#pragma unroll
        for (int j = 0; j < 4; ++j) {
          pa[j] = pack2(ex2(s[2 * j] - mn), ex2(s[2 * j + 1] - mn));
          pb[j] = pack2(ex2(s[8 + 2 * j] - mn), ex2(s[8 + 2 * j + 1] - mn));
          ps += bflo(pa[j]) + bfhi(pa[j]) + bflo(pb[j]) + bfhi(pb[j]);
        }
        ps += __shfl_xor(ps, 32);
        ls = ls * alpha + ps;
#pragma unroll
        for (int r = 0; r < 16; ++r) oo[r] *= alpha;
        oo = MFMA32(b0, __builtin_bit_cast(bf16x8, pa), oo);
        oo = MFMA32(b1, __builtin_bit_cast(bf16x8, pb), oo);
      }
      rl = 1.f / ls;
#pragma unroll
      for (int r = 0; r < 16; ++r) oo[r] *= rl;
    }
#pragma unroll
    for (int s = 0; s < 2; ++s) {
      u32x4 w;
#pragma unroll
      for (int j = 0; j < 4; ++j) w[j] = min(pack2(oo[8 * s + 2 * j], oo[8 * s + 2 * j + 1]), 0xfffeffffu);
      st16(op + s * 512, w);
    }
#undef PK
#define PK 1
#undef EX
#undef LDK
#undef LDV
#undef TOFF
  }
  if (qg == 0) {
#define PACE() asm volatile("s_waitcnt vmcnt(%0)" ::"i"(SH_D - 1) : "memory")
    TS(6);
    {
      const unsigned ta = lds0 + (lane >> 4) * 1024 + ((lane >> 2) & 3) * 128 + (kq ^ ((lane >> 3) & 3)) * 32 + (lane & 3) * 8;
#pragma unroll
      for (int i = 0; i < 16; ++i) wgu[i] = cat8(lds_tr(ta + i * 4096), lds_tr(ta + i * 4096 + 512));
#pragma unroll
      for (int i = 0; i < 7; ++i) wdn[i] = cat8(lds_tr(ta + 65536 + i * 4096), lds_tr(ta + 65536 + i * 4096 + 512));
    }
    asm volatile("" : "+v"(wgu[0]), "+v"(wgu[1]), "+v"(wgu[2]), "+v"(wgu[3]), "+v"(wgu[4]), "+v"(wgu[5]), "+v"(wgu[6]), "+v"(wgu[7]),
                      "+v"(wgu[8]), "+v"(wgu[9]), "+v"(wgu[10]), "+v"(wgu[11]), "+v"(wgu[12]), "+v"(wgu[13]), "+v"(wgu[14]), "+v"(wgu[15]));
    asm volatile("" : "+v"(wdn[0]), "+v"(wdn[1]), "+v"(wdn[2]), "+v"(wdn[3]), "+v"(wdn[4]), "+v"(wdn[5]), "+v"(wdn[6])::"memory");
    lds_arrive(lds0 + K3_CNT + 12);
    lds_await(lds0 + K3_CNT + 12, 4u);
#pragma unroll
    for (int p = 0; p < SH_L1; ++p) WDMA(p & 15, p >> 4);
    float mv[8];
    unsigned wrw;
    {
      const float* mp = modp + 2048 + nt * 64;
#pragma unroll
      for (int i = 0; i < 8; ++i)
        asm volatile("global_load_dword %0, %1, %2" : "=v"(mv[i]) : "v"(lane * 4), "s"(mp + (i >> 2) * 6144 + (i & 3) * 1024) : "memory");
      asm volatile("global_load_ushort %0, %1, %2" : "=v"(wrw) : "v"(lane * 2), "s"(wr + nt * 64) : "memory");
    }
#pragma unroll
    for (int p = SH_L1; p < 24; ++p) { PACE(); WDMA(p & 15, p >> 4); }
    FT(15);
    asm volatile("s_waitcnt vmcnt(0)" : "+v"(mv[0]), "+v"(mv[1]), "+v"(mv[2]), "+v"(mv[3]), "+v"(mv[4]), "+v"(mv[5]), "+v"(mv[6]), "+v"(mv[7]), "+v"(wrw)::"memory");
    if (wid == 0) {
      float v[4];
#pragma unroll
      for (int i = 0; i < 4; ++i) v[i] = rbf(mv[i] + mv[4 + i]);
      const float w = bflo(wrw);
      const float g2 = rbf(1.f + v[2]), gw = g2 * w;
      const unsigned va = lds0 + K4_VEC + lane * 4;
      ldsw(va, v[0]); ldsw(va + 256, g2); ldsw(va + 512, v[1]); ldsw(va + 768, v[3]); ldsw(va + 1024, gw);
      ldsw(va + 1280, __float_as_uint(wsum(gw)) | 1u);
    }
    if (!has_gu) {
      const f32x4 s0 = *(const f32x4*)(modp + 3072 + tid * 4), s1 = *(const f32x4*)(modp + 6144 + 3072 + tid * 4);
      const u32x2 w = *(const u32x2*)(wr + tid * 4);
      const float* gp = modp + 5120 + (b - 224) * 32 + (lane & 31);
      ldsw(lds0 + K4_FIN + (lane & 31) * 4, rbf(gp[0] + gp[6144]));
      const float cs = wsum((rbf(s0[0] + s1[0]) * bflo(w[0]) + rbf(s0[1] + s1[1]) * bfhi(w[0])) +
                            (rbf(s0[2] + s1[2]) * bflo(w[1]) + rbf(s0[3] + s1[3]) * bfhi(w[1])));
      ldsw(lds0 + K4_FIN + 128 + kq * 4, cs);
    }
    lds_arrive(lds0 + K3_CNT + 8);
  } else {
    TS(6);
  }
  OLOAD_G(0);
  XLOAD_G(0);
  lds_await(lds0 + K3_CNT + 8, 4u);
  if (qg == 0) lds_await(lds0 + K3_CNT + 32 + kq * 4, 1u);

  FT(12);
  f32x2 xv[2][4];
  u32x4 xw[2];
  {
    const int h = lane >> 5;
    unsigned bb[2];
    {
      const int q4 = (lane & 15) >> 2, p4 = lane & 3, ch = (lane >> 4) & 1;
#pragma unroll
      for (int c = 0; c < 2; ++c)
        bb[c] = lds0 + K4_IMG + qg * 32768 + (4 * h + q4) * 128 + ((c ^ ((q4 >> 1) & 1)) * 64) + ch * 32 + p4 * 8;
    }
#define WFRAG(C, F) cat8(lds_tr(bb[C] + (F) * 2048), lds_tr(bb[C] + (F) * 2048 + 1024))
    f32x16 acc0 = (f32x16){}, acc1 = (f32x16){};
#define OBAD(C)                                                                                                            \
  __any(max(max(max(max(ofr[4 * (C)][0], ofr[4 * (C)][1]), max(ofr[4 * (C)][2], ofr[4 * (C)][3])),                         \
                max(max(ofr[4 * (C) + 1][0], ofr[4 * (C) + 1][1]), max(ofr[4 * (C) + 1][2], ofr[4 * (C) + 1][3]))),        \
            max(max(max(ofr[4 * (C) + 2][0], ofr[4 * (C) + 2][1]), max(ofr[4 * (C) + 2][2], ofr[4 * (C) + 2][3])),         \
                max(max(ofr[4 * (C) + 3][0], ofr[4 * (C) + 3][1]), max(ofr[4 * (C) + 3][2], ofr[4 * (C) + 3][3])))) == POISON)
#define OWAIT(C, N)                                                                                                        \
  asm volatile("s_waitcnt vmcnt(%4)"                                                                                       \
               : "+v"(ofr[4 * (C)]), "+v"(ofr[4 * (C) + 1]), "+v"(ofr[4 * (C) + 2]), "+v"(ofr[4 * (C) + 3]) : "i"(N) : "memory")
#define OPHASE(BASE, Y, PRE3)                                                                                              \
  _Pragma("unroll") for (int c = 0; c < 4; ++c) {                                                                          \
    bf16x8 w[4][2];                                                                                                        \
    _Pragma("unroll") for (int i = 0; i < 4; ++i) { w[i][0] = WFRAG(0, 4 * c + i); w[i][1] = WFRAG(1, 4 * c + i); }        \
    if (c == 3) { PRE3; }                                                                                                  \
    OWAIT(c, 12 - 4 * c + (Y));                                                                                            \
    _Pragma("nounroll") for (unsigned spin = 0; spin < SPIN_CAP; ++spin) {                                                 \
      if (__builtin_expect(!OBAD(c), 1)) break;                                                                            \
      __builtin_amdgcn_s_sleep(HD0R);                                                                                      \
      _Pragma("unroll") for (int d = c; d < 4; ++d) OWAIT(d, 0);                                                           \
      _Pragma("unroll") for (int d = c; d < 4; ++d) {                                                                      \
        const int go = d == c ? 1 : __builtin_amdgcn_readfirstlane((int)OBAD(d));                                          \
        asm volatile("s_cmp_lg_u32 %6, 1\n\ts_cbranch_scc1 1f\n\t"                                                         \
                     "global_load_dwordx4 %0, %4, %5 sc1\n\tglobal_load_dwordx4 %1, %4, %5 offset:1024 sc1\n\t"            \
                     "global_load_dwordx4 %2, %4, %5 offset:2048 sc1\n\tglobal_load_dwordx4 %3, %4, %5 offset:3072 sc1\n\t1:" \
                     : "+v"(ofr[4 * d]), "+v"(ofr[4 * d + 1]), "+v"(ofr[4 * d + 2]), "+v"(ofr[4 * d + 3])                  \
                     : "v"(lane * 16), "s"((BASE) + d * 2048), "s"(go) : "memory", "scc");                                 \
      }                                                                                                                    \
      _Pragma("unroll") for (int d = c; d < 4; ++d) OWAIT(d, 0);                                                           \
    }                                                                                                                      \
    _Pragma("unroll") for (int i = 0; i < 4; ++i) {                                                                        \
      const bf16x8 a = __builtin_bit_cast(bf16x8, ofr[4 * c + i]);                                                         \
      acc0 = MFMA32(w[i][0], a, acc0);                                                                                     \
      acc1 = MFMA32(w[i][1], a, acc1);                                                                                     \
    }                                                                                                                      \
    SBAR();                                                                                                                \
  }
    OPHASE(ob, 2, (void)0);
    TS(7);
    if (qg == 0) {
      lds_await(lds0 + K3_CNT + 16, 4u);
#pragma unroll
      for (int p = 8; p < 16; ++p) WDMA(p, 1);
      __builtin_amdgcn_s_sleep(HD0);
    } else if (HD0B) {
      __builtin_amdgcn_s_sleep(HD0B);
    }
#undef WDMA
    bb[0] += 65536; bb[1] += 65536;
    OLOAD(0, ob + 16384);
    if (qg == 0) {
      asm volatile("s_waitcnt vmcnt(16)" ::: "memory");
      lds_arrive(lds0 + K3_CNT + 20);
    } else {
      lds_arrive(lds0 + K3_CNT + 28);
      lds_await(lds0 + K3_CNT + 20, 4u);
    }
    f32x2 ga[4], gw[4], xs[2][4];
    float gs;
    unsigned wa, ra0, ra1, vrec;
#define PRE3                                                                                                     \
    do {                                                                                                         \
      asm volatile("" : "+v"(xr[0]), "+v"(xr[1]));                                                               \
      lds_await(lds0 + K3_CNT + 20, 4u); lds_await(lds0 + K3_CNT + 28, 4u);     \
      const unsigned va = lds0 + K4_VEC + pos * 32;                                                              \
      const f32x4 a0 = ldsr<f32x4>(va), a1 = ldsr<f32x4>(va + 16);                                               \
      const f32x4 b0 = ldsr<f32x4>(va + 1024), b1 = ldsr<f32x4>(va + 1024 + 16);                                 \
      gs = ldsr<float>(lds0 + K4_VEC + 1280);                                                                    \
      ga[0] = a0.lo; ga[1] = a0.hi; ga[2] = a1.lo; ga[3] = a1.hi;                                                \
      gw[0] = b0.lo; gw[1] = b0.hi; gw[2] = b1.lo; gw[3] = b1.hi;                                                \
      _Pragma("unroll") for (int i = 0; i < 8; ++i) { xs[i >> 2][i & 3] = up2(xr[i >> 2][i & 3]); PIN(xs[i >> 2][i & 3]); } \
      wa = lds0 + (wid < 7 ? wid * 8704 : 131072) + (lane & 31) * 272 + h * 16;                                  \
      ra0 = lds0 + (wid >> 1) * 8704 + ((wid & 1) * 16 + kr) * 272 + pos * 32;                                   \
      ra1 = ra0 + (wid < 6 ? 4 * 8704 : 131072 - 3 * 8704);                                                      \
      vrec = (unsigned)((erow + (pos & 1) * 8) * 16);                                                            \
      PIN(wa); PIN(ra0); PIN(ra1); PIN(vrec); PIN(gs);                                                           \
      _Pragma("unroll") for (int i = 0; i < 4; ++i) { PIN(ga[i]); PIN(gw[i]); }                                  \
    } while (0)
    OPHASE(ob + 16384, 0, PRE3);
#undef OPHASE
#undef WFRAG
    TS(8);
#pragma unroll
    for (int g = 0; g < 4; ++g) {
      ldsw(wa + g * 32, (f32x4){acc0[4 * g], acc0[4 * g + 1], acc0[4 * g + 2], acc0[4 * g + 3]});
      ldsw(wa + 128 + g * 32, (f32x4){acc1[4 * g], acc1[4 * g + 1], acc1[4 * g + 2], acc1[4 * g + 3]});
    }
    LDS_BARRIER();
    f32x4 pa[2][4];
#pragma unroll
    for (int i = 0; i < 2; ++i) {
      pa[i][0] = ldsr<f32x4>(ra0 + i * 2176); pa[i][1] = ldsr<f32x4>(ra1 + i * 2176);
      pa[i][2] = ldsr<f32x4>(ra0 + i * 2176 + 16); pa[i][3] = ldsr<f32x4>(ra1 + i * 2176 + 16);
    }
    float sm[2], sq[2], sd[2];
#pragma unroll
    for (int i = 0; i < 2; ++i) {
#pragma unroll
      for (int j = 0; j < 4; ++j) {
        const f32x4 a = pa[i][j & 2], c = pa[i][(j & 2) + 1];
        const f32x2 v = ga[j] * ((j & 1) ? a.hi + c.hi : a.lo + c.lo) + xs[i][j];
        xw[i][j] = pack2(v[0], v[1]);
        xv[i][j] = up2(xw[i][j]);
      }
      const f32x2 t = (xv[i][0] + xv[i][1]) + (xv[i][2] + xv[i][3]);
      sm[i] = sum8(t[0] + t[1]) * (1.f / 64);
    }
#pragma unroll
    for (int i = 0; i < 2; ++i) {
      f32x2 q, d;
#pragma unroll
      for (int j = 0; j < 4; ++j) {
        const f32x2 e = xv[i][j] - sm[i];
        q = j ? e * e + q : e * e;
        d = j ? e * gw[j] + d : e * gw[0];
      }
      sq[i] = sum8(q[0] + q[1]); sd[i] = sum8(d[0] + d[1]);
    }
    const bool r1 = pos & 1;
    const u32x4 rec = {__float_as_uint(r1 ? sm[1] : sm[0]) | 1u, __float_as_uint(r1 ? sq[1] : sq[0]) | 1u,
                       __float_as_uint(r1 ? sd[1] : sd[0]) | 1u, __float_as_uint(gs)};
    asm volatile("s_mov_b64 exec, %3\n\tglobal_store_dwordx4 %0, %1, %2 sc1\n\ts_mov_b64 exec, -1\n\ts_nop 0"
                 :
                 : "v"(vrec), "v"(rec), "s"(sync + SY_ST + (mt * 16 + nt) * STP), "s"(0x0303030303030303ull)
                 : "memory");
  }
  GT(9);
#define POLL(ISSUE, N, TIE, BAD, DELAY, GAP)                                                  \
  do {                                                                                        \
    unsigned long long todo = ~0ull;                                                          \
    __builtin_amdgcn_s_sleep(DELAY);                                                          \
    _Pragma("unroll") for (int k_ = 1; k_ < PK; ++k_) { ISSUE; __builtin_amdgcn_s_sleep(GAP); } \
    _Pragma("nounroll") for (unsigned spin = 0; spin < SPIN_CAP; ++spin) {                    \
      ISSUE;                                                                                  \
      asm volatile("s_waitcnt vmcnt(%0)" ::"i"((PK - 1) * (N)) : "memory");                   \
      TIE;                                                                                    \
      todo = __ballot(BAD);                                                                   \
      if (__builtin_expect(todo == 0ull, 1)) break;                                           \
      if (PK == 1) __builtin_amdgcn_s_sleep(GAP);                                             \
    }                                                                                         \
  } while (0)
#define AND4(v_) ((v_[0] & v_[1]) & (v_[2] & v_[3]))
#define MAX4(v_) max(max(v_[0], v_[1]), max(v_[2], v_[3]))
  u32x4 c[4] = {(u32x4){}, (u32x4){}, (u32x4){}, (u32x4){}};
  u32x4 o[2];
  unsigned long long om[2];
  unsigned vout = (unsigned)((erow * D + pos * 8) * 2);
#define OUTST()                                                                                                    \
  asm volatile("s_mov_b64 exec, %5\n\tglobal_store_dwordx4 %0, %1, %3 sc1\n\ts_mov_b64 exec, %6\n\t"               \
               "global_store_dwordx4 %0, %2, %4 sc1\n\ts_mov_b64 exec, -1\n\ts_nop 0"                              \
               :                                                                                                   \
               : "v"(vout), "v"(o[0]), "v"(o[1]), "s"(out + (size_t)(mt * 128) * D + nt * 64),                     \
                 "s"(out + (size_t)(mt * 128 + 8) * D + nt * 64), "s"(om[0]), "s"(om[1])                           \
               : "memory")
#define TIE_C() asm volatile("" : "+v"(c[0]), "+v"(c[1]), "+v"(c[2]), "+v"(c[3]))
  {
    const unsigned va = lds0 + K4_VEC + pos * 32;
    f32x4 t[6];
#pragma unroll
    for (int i = 0; i < 6; ++i) t[i] = ldsr<f32x4>(va + 256 + (i >> 1) * 256 + (i & 1) * 16);
    asm volatile("" : "+v"(t[0]), "+v"(t[1]), "+v"(t[2]), "+v"(t[3]), "+v"(t[4]), "+v"(t[5]));
    unsigned vo = (unsigned)(pos * (STP * 16) + erow * 16), vc = (unsigned)(mt * (HCP * 8) + nt * 128 + pos * 16);
    unsigned ba = lds0 + K3_CNT + 48;
    PIN(vo); PIN(vc); PIN(vout); PIN(ba);
#define ISSUE1                                                                                                     \
    asm volatile("s_mov_b64 exec, %6\n\tglobal_load_dwordx4 %0, %4, %5 sc1\n\tglobal_load_dwordx4 %1, %4, %5 offset:%7 sc1\n\t" \
                 "global_load_dwordx4 %2, %4, %5 offset:128 sc1\n\tglobal_load_dwordx4 %3, %4, %5 offset:%8 sc1\n\t" \
                 "s_mov_b64 exec, -1"                                                                              \
                 : "+v"(c[0]), "+v"(c[1]), "+v"(c[2]), "+v"(c[3])                                                  \
                 : "v"(vo), "s"(sync + SY_ST + mt * 16 * STP), "s"(todo), "i"(STP * 8), "i"(STP * 8 + 128)         \
                 : "memory")
    POLL(ISSUE1, 4, TIE_C(), !((AND4(c[0]) & AND4(c[1])) & (AND4(c[2]) & AND4(c[3])) & 1u), HD1, PG1);
    GT(10);
#define F(x_) __uint_as_float(x_)
    float mean[2], rstd[2];
    unsigned key[2];
#pragma unroll
    for (int i = 0; i < 2; ++i) mean[i] = sum8(F(c[2 * i][0]) + F(c[2 * i + 1][0])) * (1.f / 16);
#pragma unroll
    for (int i = 0; i < 2; ++i) {
      const float e0 = F(c[2 * i][0]) - mean[i], e1 = F(c[2 * i + 1][0]) - mean[i];
      const float q = fmaf(64.f, fmaf(e1, e1, e0 * e0), F(c[2 * i][1]) + F(c[2 * i + 1][1]));
      const float d = fmaf(e1, F(c[2 * i + 1][3]), fmaf(e0, F(c[2 * i][3]), F(c[2 * i][2]) + F(c[2 * i + 1][2])));
      rstd[i] = __builtin_amdgcn_rsqf(sum8(q) * (1.f / D) + 1e-5f);
      key[i] = okey(rstd[i] * sum8(d));
    }
    const bool hi = key[1] > key[0];
    const u32x2 mine = {~(unsigned)(erow + (hi ? 8 : 0)), hi ? key[1] : key[0]};
    asm volatile("s_mov_b64 exec, %3\n\tds_max_u64 %0, %1\n\ts_mov_b64 exec, 1\n\tds_add_u32 %0, %2 offset:8\n\ts_mov_b64 exec, -1"
                 :
                 : "v"(ba), "v"(mine), "v"(1u), "s"(0x0101010101010101ull)
                 : "memory");
    u32x4 hw[2], hc, xc;
#pragma unroll
    for (int i = 0; i < 2; ++i)
#pragma unroll
      for (int j = 0; j < 4; ++j) {
        const f32x2 g = (j & 1) ? t[j >> 1].hi : t[j >> 1].lo, sh = (j & 1) ? t[2 + (j >> 1)].hi : t[2 + (j >> 1)].lo;
        const f32x2 v = ((xv[i][j] - mean[i]) * rstd[i]) * g + sh;
        hw[i][j] = pack2(v[0], v[1]);
      }
#pragma unroll
    for (int j = 0; j < 4; ++j) {
      hc[j] = min(hi ? hw[1][j] : hw[0][j], 0xfffeffffu);
      xc[j] = min(hi ? xw[1][j] : xw[0][j], 0xfffeffffu);
    }
    PIN(hc); PIN(xc);
    u32x4 best = ldsr<u32x4>(ba);
    while (__builtin_expect(__builtin_amdgcn_readfirstlane(best[2]) < 8u, 0)) {
      __builtin_amdgcn_s_sleep(1);
      asm volatile("" ::: "memory");
      best = ldsr<u32x4>(ba);
    }
    GT(11);
    if (best[0] == mine[0]) {
      asm volatile("s_nop 0\n\tglobal_store_dwordx4 %0, %1, %3 sc1\n\tglobal_store_dwordx4 %0, %2, %4 sc1\n\ts_nop 1"
                   :
                   : "v"(vc), "v"(hc), "v"(xc), "s"(sync + SY_HC), "s"(sync + SY_XC)
                   : "memory");
      if (pos == 0)
        __hip_atomic_store((gu64*)sync + SY_KEY + nt * 16 + mt, mk64(mine[1] | 1u, (unsigned)(mt * 128) + ~mine[0]), RLX_AGENT);
    }
#pragma unroll
    for (int i = 0; i < 2; ++i) {
#pragma unroll
      for (int j = 0; j < 4; ++j) {
        const f32x2 v = ((j & 1) ? t[4 + (j >> 1)].hi : t[4 + (j >> 1)].lo) * up2(hw[i][j]) + xv[i][j];
        o[i][j] = pack2(v[0], v[1]);
      }
      om[i] = __ballot(best[0] != ~(unsigned)(erow + 8 * i));
    }
    OUTST();
  }
  if (qg == 0) {
    const int n = lane & 15, kg = lane >> 4;
    u32x4 hb[2] = {(u32x4){POISON, POISON, POISON, POISON}, (u32x4){POISON, POISON, POISON, POISON}};
    unsigned key = 0u;
#define TIE_H() asm volatile("" : "+v"(hb[0]), "+v"(hb[1]), "+v"(key))
    {
      unsigned vh = (unsigned)(kr * (HCP * 8) + pos * 16), vk = (unsigned)(n * 8), vg = kg * 16;
      unsigned wa = lds0 + K4_RED + 512 + kq * 128 + pos * 16, ra = lds0 + K4_RED + 512 + kg * 16;
      PIN(vh); PIN(vk); PIN(vg); PIN(wa); PIN(ra);
      const unsigned long long* hp = sync + SY_HC + (ks * 4 + kq) * 16;
#define ISSUE2                                                                                                     \
      asm volatile("global_load_dword %2, %4, %7 sc1\n\ts_mov_b64 exec, %8\n\t"                                    \
                   "global_load_dwordx4 %0, %3, %5 sc1\n\tglobal_load_dwordx4 %1, %3, %6 sc1\n\ts_mov_b64 exec, -1" \
                   : "+v"(hb[0]), "+v"(hb[1]), "+v"(key)                                                           \
                   : "v"(vh), "v"(vk), "s"(hp), "s"(hp + 8 * HCP), "s"(sync + SY_KEY + (ks * 4 + kq) * 16), "s"(todo) \
                   : "memory")
      POLL(ISSUE2, 3, TIE_H(), max(MAX4(hb[0]), MAX4(hb[1])) == POISON || !(key & 1u), HD2, PG2);
      TIE_C();
      GT(12);
      const int win = __builtin_ctzll(__ballot(key == rowmaxu(key)));
      asm volatile("s_mov_b64 exec, %3\n\tds_write_b128 %0, %1\n\ts_mov_b64 exec, 1\n\tds_add_u32 %2, %4\n\ts_mov_b64 exec, -1"
                   :
                   : "v"(wa), "v"(win < 8 ? hb[0] : hb[1]), "v"(lds0 + K3_CNT + 24), "s"(0xffull << ((win & 7) * 8)), "v"(1u)
                   : "memory");
      while (lds_peek(lds0 + K3_CNT + 24) < 4u) {}
      asm volatile("" ::: "memory");
      f32x4 ag = (f32x4){}, au = (f32x4){};
#pragma unroll
      for (int i = 0; i < 8; ++i) {
        const bf16x8 h = lds_b128(ra + i * 64);
        ag = MFMA16(wgu[i], h, ag);
        au = MFMA16(wgu[8 + i], h, au);
      }
      const u32x4 g = __builtin_bit_cast(u32x4, ag) | 1u, u = __builtin_bit_cast(u32x4, au) | 1u;
      asm volatile("s_mov_b64 exec, %4\n\tglobal_store_dwordx4 %0, %1, %3 sc1\n\tglobal_store_dwordx4 %0, %2, %3 offset:64 sc1\n\t"
                   "s_mov_b64 exec, -1\n\ts_nop 0"
                   :
                   : "v"(vg), "v"(g), "v"(u), "s"(sync + SY_GP + ks * GPP + cg * 64 + kq * 16),
                     "s"(has_gu ? 0x0001000100010001ull : 0ull)
                   : "memory");
    }
    {
      const int q = kq * 14 + min(lane >> 2, 13);
      unsigned vo = (unsigned)((lane & 3) * (GPP * 8) + (q >> 2) * 128 + (q & 3) * 16);
      unsigned wa = lds0 + K4_RED + (q * 4 + (lane & 3)) * 2, ra = lds0 + K4_RED + kg * 16, vy = kg * 16;
      PIN(vo); PIN(wa); PIN(ra); PIN(vy);
      c[0] = (u32x4){}; c[1] = (u32x4){};
#define ISSUE3                                                                                                     \
      asm volatile("s_mov_b64 exec, %4\n\tglobal_load_dwordx4 %0, %2, %3 sc1\n\tglobal_load_dwordx4 %1, %2, %3 offset:64 sc1\n\t" \
                   "s_mov_b64 exec, -1"                                                                            \
                   : "+v"(c[0]), "+v"(c[1])                                                                        \
                   : "v"(vo), "s"(sync + SY_GP + dks * 224), "s"(todo)                                             \
                   : "memory")
      POLL(ISSUE3, 2, asm volatile("" : "+v"(c[0]), "+v"(c[1])), !(AND4(c[0]) & AND4(c[1]) & 1u), HD3, PG3);
      TIE_H();
      GT(13);
      float sg[4], su[4];
#pragma unroll
      for (int j = 0; j < 4; ++j) {
        sg[j] = dpp_add<0x4E>(dpp_add<0xB1>(F(c[0][j])));
        su[j] = dpp_add<0x4E>(dpp_add<0xB1>(F(c[1][j])));
      }
      const float g = (lane & 2) ? ((lane & 1) ? sg[3] : sg[2]) : ((lane & 1) ? sg[1] : sg[0]);
      const float u = (lane & 2) ? ((lane & 1) ? su[3] : su[2]) : ((lane & 1) ? su[1] : su[0]);
      ldsw(wa, (u16)pack2(silu_bf(rbf(g)) * rbf(u), 0.f));
      asm volatile("s_mov_b64 exec, 1\n\tds_add_u32 %0, %1\n\ts_mov_b64 exec, -1" ::"v"(lds0 + K3_CNT + 24), "v"(1u) : "memory");
      while (lds_peek(lds0 + K3_CNT + 24) < 8u) {}
      asm volatile("" ::: "memory");
      f32x4 y = (f32x4){};
#pragma unroll
      for (int i = 0; i < 7; ++i) y = MFMA16(wdn[i], lds_b128(ra + i * 64), y);
      asm volatile("s_mov_b64 exec, %3\n\tglobal_store_dwordx4 %0, %1, %2 sc1\n\ts_mov_b64 exec, -1\n\ts_nop 0"
                   :
                   : "v"(vy), "v"(__builtin_bit_cast(u32x4, y) | 1u), "s"(sync + SY_GY + dks * GYP + dcg * 32 + kq * 8),
                     "s"(0x0001000100010001ull)
                   : "memory");
      TS(14);
      DRAIN();
      asm volatile("" : "+v"(c[0]), "+v"(c[1]));
    }
    TS_END(4, 0);
  } else if (!has_gu && wid == 7) {
    const int fc0 = (b - 224) * 32;
    const unsigned vh = (unsigned)((lane >> 2) * (HCP * 8) + fc0 * 2 + (lane & 3) * 16), vk = (unsigned)((SY_KEY + (fc0 >> 6) * 16 + (lane >> 2)) * 8);
    const f32x4 g0 = ldsr<f32x4>(lds0 + K4_FIN + (lane & 3) * 32), g1 = ldsr<f32x4>(lds0 + K4_FIN + (lane & 3) * 32 + 16);
    const f32x4 cs4 = ldsr<f32x4>(lds0 + K4_FIN + 128);
    u32x4 hc, xc;
    u32x2 kr2;
    __builtin_amdgcn_s_sleep(HD2);
    _Pragma("nounroll") for (unsigned spin = 0; spin < SPIN_CAP; ++spin) {
      GLOADC(hc, vh, sync + SY_HC, 0); GLOADC(xc, vh, sync + SY_XC, 0);
      asm volatile("global_load_dwordx2 %0, %1, %2 sc1" : "=v"(kr2) : "v"(vk), "s"(sync) : "memory");
      asm volatile("s_waitcnt vmcnt(0)" : "+v"(hc), "+v"(xc), "+v"(kr2)::"memory");
      if (__all(max(MAX4(hc), MAX4(xc)) != POISON && (kr2[0] & 1u))) break;
      __builtin_amdgcn_s_sleep(8);
    }
    TIE_C();
    TS(12);
    unsigned km = rowmaxu(kr2[0]);
    km = max(max(__builtin_amdgcn_readlane(km, 0), __builtin_amdgcn_readlane(km, 16)),
             max(__builtin_amdgcn_readlane(km, 32), __builtin_amdgcn_readlane(km, 48)));
    const int wl = __builtin_ctzll(__ballot(kr2[0] == km));
    const unsigned wrow = __builtin_amdgcn_readlane(kr2[1], wl);
    if ((lane >> 2) == (wl >> 2)) {
      ldsw(lds0 + K4_FIN + 256 + (lane & 3) * 16, xc);
      ldsw(lds0 + K4_FIN + 320 + (lane & 3) * 16, hc);
    } else {
      u32x4 o;
#pragma unroll
      for (int j = 0; j < 4; ++j) {
        const f32x2 v = (j == 0 ? g0.lo : j == 1 ? g0.hi : j == 2 ? g1.lo : g1.hi) * up2(hc[j]) + up2(xc[j]);
        o[j] = pack2(v[0], v[1]);
      }
      st16(out + (size_t)kr2[1] * D + fc0 + (lane & 3) * 8, o);
    }
    const int col = pos * 4 + (lane >> 5) + ((lane >> 4) & 1) * 2;
    float fx = bf2f(ldsr<u16>(lds0 + K4_FIN + 256 + col * 2)), fh = bf2f(ldsr<u16>(lds0 + K4_FIN + 320 + col * 2));
    float fg = ldsr<float>(lds0 + K4_FIN + col * 4);
    const float rr = __uint_as_float(km ^ ((km >> 31) ? 0x80000000u : 0xffffffffu)) + ((cs4[0] + cs4[1]) + (cs4[2] + cs4[3]));
    float fsig = rbf(1.f / (1.f + __expf(-rbf(rr))));
    unsigned vo = (unsigned)(kr * (GYP * 8) + fc0 * 4 + pos * 16), vs = (unsigned)(col * 2);
    PIN(fsig); PIN(fx); PIN(fh); PIN(fg); PIN(vo); PIN(vs);
    c[0] = (u32x4){}; c[1] = (u32x4){};
#define ISSUE4                                                                                                     \
    asm volatile("s_mov_b64 exec, %5\n\tglobal_load_dwordx4 %0, %2, %3 sc1\n\tglobal_load_dwordx4 %1, %2, %4 sc1\n\t" \
                 "s_mov_b64 exec, -1"                                                                              \
                 : "+v"(c[0]), "+v"(c[1])                                                                          \
                 : "v"(vo), "s"(sync + SY_GY), "s"(sync + SY_GY + 8 * GYP), "s"(todo)                              \
                 : "memory")
    POLL(ISSUE4, 2, asm volatile("" : "+v"(c[0]), "+v"(c[1])), !(AND4(c[0]) & AND4(c[1]) & 1u), HD4, PG4);
    TS(13);
    const f32x4 v = __builtin_bit_cast(f32x4, c[0]) + __builtin_bit_cast(f32x4, c[1]);
    const float y = dpp_add<0x128>(swap16_add(swap32_add(v[0], v[1]), swap32_add(v[2], v[3])));
    const unsigned ov = pack2(fmaf(fg, rbf(fh + rbf(rbf(y) * fsig)), fx), 0.f);
    asm volatile("s_mov_b64 exec, %3\n\tglobal_store_short %0, %1, %2\n\ts_mov_b64 exec, -1\n\ts_nop 0"
                 :
                 : "v"(vs), "v"(ov), "s"(out + (size_t)wrow * D + fc0), "s"(0x00ff00ff00ff00ffull)
                 : "memory");
    TS(14);
    DRAIN();
    asm volatile("" : "+v"(c[0]), "+v"(c[1]));
    TS_END(5, 7);
  } else {
    DRAIN();
    TIE_C();
    TS_END(3, 4);
  }
#undef F
  PROF_END2(3);
}

extern "C" void kernel_launch(void* const* d_in, const int* in_sizes, int n_in, void* d_out, int out_size, void* d_ws,
                              size_t ws_size, hipStream_t stream) {
  const u16* x = (const u16*)d_in[0];
  const u16* wqkv = (const u16*)d_in[1];
  const u16* wo = (const u16*)d_in[2];
  const u16* wr = (const u16*)d_in[4];
  const u16* wg = (const u16*)d_in[5];
  const u16* wu = (const u16*)d_in[6];
  const u16* wd = (const u16*)d_in[7];
  const u16* c = (const u16*)d_in[8];
  const u16* wmod = (const u16*)d_in[9];
  char* ws = (char*)d_ws;
  float* modp = (float*)(ws + WS_MODP);
  u16* nbuf = (u16*)(ws + WS_N);
  u16* kvf = (u16*)(ws + WS_K);
  u16* of = (u16*)(ws + WS_O);
  u16* out = (u16*)d_out;
  (void)hipFuncSetAttribute((const void*)k2_main, hipFuncAttributeMaxDynamicSharedMemorySize, K3_LDS);
  k1_mod_ln<<<256, 512, 0, stream>>>(c, wmod, x, modp, nbuf, (u32x4*)(ws + WS_SYNC), wqkv);
  k2_main<<<256, 512, K3_LDS, stream>>>(nbuf, wqkv, modp, kvf, of, wo, x, wr, wg, wu, wd, out, (unsigned long long*)(ws + WS_SYNC));
}
```

```cpp
#include <hip/hip_runtime.h>
#include <stdint.h>

typedef unsigned short u16;
using bf16x8 = __attribute__((ext_vector_type(8))) short;
using s16x4 = __attribute__((ext_vector_type(4))) short;
using f32x16 = __attribute__((ext_vector_type(16))) float;
using f32x4 = __attribute__((ext_vector_type(4))) float;
using f32x2 = __attribute__((ext_vector_type(2))) float;
using u32x4 = __attribute__((ext_vector_type(4))) unsigned;
using u32x2 = __attribute__((ext_vector_type(2))) unsigned;
typedef __bf16 bf16x2_t __attribute__((ext_vector_type(2)));
#define LAS __attribute__((address_space(3)))
#define DI __device__ __forceinline__

constexpr int D = 1024, FF = 3584;
constexpr float QSCALE = 0.17677669529663687f * 1.4426950408889634f;

constexpr size_t WS_MODP = 0;
constexpr size_t WS_SYNC = 131072;
constexpr int SY_OC = 0, SY_KV = SY_OC + 64 * 16;
constexpr int SY_KEY = SY_KV + 128 * 16;
constexpr int SY_MV = SY_KEY + 256;
constexpr int SY_GB = SY_MV + 32768;
constexpr int SY_HC = SY_GB + 640, HCP = 272;
constexpr int SY_XC = SY_HC + 16 * HCP;
constexpr int SY_GP = SY_XC + 16 * HCP, GPP = 3648;
constexpr int SY_GY = SY_GP + 4 * GPP, GYP = 528;
constexpr int SY_ST = SY_GY + 16 * GYP, STP = 272;
constexpr size_t WS_K = 2u << 20;
constexpr size_t WS_V = WS_K + (4u << 20);
constexpr size_t WS_O = WS_V + (4u << 20);
constexpr size_t WS_N = WS_O + (4u << 20);

DI float bf2f(u16 v) { return __uint_as_float((unsigned)v << 16); }
DI float bflo(unsigned w) { return __uint_as_float(w << 16); }
DI float bfhi(unsigned w) { return __uint_as_float(w & 0xffff0000u); }
DI unsigned pack2(float lo, float hi) {
  f32x2 v = {lo, hi};
  bf16x2_t b = __builtin_convertvector(v, bf16x2_t);
  return __builtin_bit_cast(unsigned, b);
}
DI float rbf(float x) { return bflo(pack2(x, 0.f)); }
#define MFMA32(a, b, c) __builtin_amdgcn_mfma_f32_32x32x16_bf16((a), (b), (c), 0, 0, 0)
#define MFMA16(a, b, c) __builtin_amdgcn_mfma_f32_16x16x32_bf16((a), (b), (c), 0, 0, 0)
typedef __bf16 b16x8_t __attribute__((ext_vector_type(8)));
typedef __bf16 b16x16_t __attribute__((ext_vector_type(16)));
#define HALF(s, h) __builtin_bit_cast(b16x16_t, __builtin_shufflevector((s), (s), 8 * (h), 8 * (h) + 1, 8 * (h) + 2, 8 * (h) + 3, 8 * (h) + 4, 8 * (h) + 5, 8 * (h) + 6, 8 * (h) + 7))
#define SMF32(a, s, h, c, ix) __builtin_amdgcn_smfmac_f32_32x32x32_bf16(__builtin_bit_cast(b16x8_t, a), HALF(s, h), (c), (ix), 0, 0)
#define SMF16(a, s, h, c, ix) __builtin_amdgcn_smfmac_f32_16x16x64_bf16(__builtin_bit_cast(b16x8_t, a), HALF(s, h), (c), (ix), 0, 0)
#define SBAR() __builtin_amdgcn_sched_barrier(0)
#define LDS_BARRIER() asm volatile("s_waitcnt lgkmcnt(0)\n\ts_barrier" ::: "memory")

template <int CTRL>
DI float dpp_add(float v) {
  return v + __uint_as_float(__builtin_amdgcn_update_dpp(0u, __float_as_uint(v), CTRL, 0xf, 0xf, true));
}
DI float swap32_add(float a, float b) {
  auto r = __builtin_amdgcn_permlane32_swap(__float_as_uint(a), __float_as_uint(b), false, false);
  return __uint_as_float(r[0]) + __uint_as_float(r[1]);
}
DI float swap16_add(float a, float b) {
  auto r = __builtin_amdgcn_permlane16_swap(__float_as_uint(a), __float_as_uint(b), false, false);
  return __uint_as_float(r[0]) + __uint_as_float(r[1]);
}
DI unsigned pack2h(float lo, float hi) {
  unsigned r;
  asm("v_cvt_pk_f16_f32 %0, %1, %2" : "=v"(r) : "v"(lo), "v"(hi));
  return r;
}
DI void st16(void* p, u32x4 v) {
  asm volatile("s_nop 0\n\tglobal_store_dwordx4 %0, %1, off sc1\n\ts_nop 1" ::"v"(p), "v"(v) : "memory");
}
template <class T> DI T ldsr(unsigned a) { return *(const LAS T*)(uintptr_t)a; }
template <class T> DI void ldsw(unsigned a, T v) { *(LAS T*)(uintptr_t)a = v; }
DI bf16x8 lds_b128(unsigned addr) { return *(const LAS bf16x8*)(uintptr_t)addr; }
typedef short v4i16_t __attribute__((ext_vector_type(4)));
DI s16x4 lds_tr(unsigned addr) {
  return __builtin_bit_cast(s16x4, __builtin_amdgcn_ds_read_tr16_b64_v4i16((LAS v4i16_t*)(uintptr_t)addr));
}
DI bf16x8 cat8(s16x4 lo, s16x4 hi) { return __builtin_shufflevector(lo, hi, 0, 1, 2, 3, 4, 5, 6, 7); }

#define GLD(dst, ptr, pol) asm volatile("global_load_dwordx4 %0, %1, off" pol : "=v"(dst) : "v"(ptr) : "memory")
DI float red8(float v) {
  v = dpp_add<0x128>(v);
  v = swap16_add(v, v);
  return swap32_add(v, v);
}
DI float one(float v) { asm("" : "+v"(v)); return v; }
DI u32x4 splat(unsigned v) {
  unsigned a = v, b = v, c = v, d = v;
  asm volatile("" : "+v"(a), "+v"(b), "+v"(c), "+v"(d));
  return (u32x4){a, b, c, d};
}
DI float silu_bf(float v) { return rbf(v / (1.f + __expf(-v))); }
__global__ __launch_bounds__(512) void k1_mod_ln(const u16* __restrict__ c, const u16* __restrict__ wmod,
                                                 const u16* __restrict__ x, float* __restrict__ modp,
                                                 u16* __restrict__ nbuf, u32x4* __restrict__ sync,
                                                 const u16* __restrict__ wqkv) {
  __shared__ __attribute__((aligned(16))) float rs[8][8][2];
  __shared__ __attribute__((aligned(16))) float red[8][64];
  __shared__ __attribute__((aligned(16))) char dump[1024];
  const int t = threadIdx.x, lane = t & 63, bid = blockIdx.x, jj = bid >> 3;
  const int wid = __builtin_amdgcn_readfirstlane(t >> 6);
  const bool gv = bid < 128;
  const unsigned pvo = (unsigned)(((lane >> 5) * 3072 + ((bid & 7) >> 1) * 256 + (lane & 31) * 8) * 2);
  const u16* const pb = wqkv + (size_t)((jj & 15) * 8 + wid) * 6144;
#define PFR(I0)                                                                                                      \
  _Pragma("unroll") for (int i = (I0); i < (I0) + 4; ++i)                                                            \
    asm volatile("s_nop 0\n\tglobal_load_lds_dwordx4 %0, %1"                                                         \
                 :                                                                                                   \
                 : "v"(pvo), "s"(pb + (i / 3) * 128 * 6144 + (i % 3) * 1024), "{m0}"((unsigned)(uintptr_t)dump)      \
                 : "memory")
  const int r = lane & 7, ch = wid * 16 + (lane >> 3);
  u32x4 xa, xb, w[4];
  u32x2 cw;
  const u16* xp = x + (size_t)(bid * 8 + r) * D + ch * 8;
  GLD(xa, xp, " nt"); GLD(xb, xp + 64, " nt");
  const int k0 = (bid >> 5) * 256 + (t >> 3) * 4;
  if (gv) {
    asm volatile("global_load_dwordx2 %0, %1, off" : "=v"(cw) : "v"(c + k0) : "memory");
    asm volatile("s_barrier" ::: "memory");
    const u16* p0 = wmod + (size_t)k0 * 6144 + (bid & 31) * 64 + (t & 7) * 8;
#pragma unroll
    for (int i = 0; i < 4; ++i) GLD(w[i], p0 + i * 6144, " nt");
    asm volatile("s_waitcnt vmcnt(5)" ::: "memory");
  } else {
    asm volatile("s_barrier" ::: "memory");
    PFR(0);
    asm volatile("s_waitcnt vmcnt(4)" ::: "memory");
  }
  asm volatile("" : "+v"(xa), "+v"(xb)::"memory");
  float v[16];
#pragma unroll
  for (int j = 0; j < 4; ++j) {
    v[2 * j] = bflo(xa[j]); v[2 * j + 1] = bfhi(xa[j]);
    v[8 + 2 * j] = bflo(xb[j]); v[8 + 2 * j + 1] = bfhi(xb[j]);
  }
  {
    float a = 0.f;
#pragma unroll
    for (int j = 0; j < 16; ++j) a += v[j];
    const float mw = red8(a) * (1.f / 128);
    float q = 0.f;
#pragma unroll
    for (int j = 0; j < 16; ++j) q = fmaf(v[j] - mw, v[j] - mw, q);
    q = red8(q);
    if (lane < 8) *(f32x2*)rs[r][wid] = (f32x2){mw, q};
  }
  LDS_BARRIER();
  {
    f32x4 e[4];
#pragma unroll
    for (int i = 0; i < 4; ++i) e[i] = *(const f32x4*)rs[r][2 * i];
    const float mean = (((e[0][0] + e[0][2]) + (e[1][0] + e[1][2])) + ((e[2][0] + e[2][2]) + (e[3][0] + e[3][2]))) * 0.125f;
    float q = 0.f;
#pragma unroll
    for (int i = 0; i < 4; ++i) {
      const float d0 = e[i][0] - mean, d1 = e[i][2] - mean;
      q += fmaf(128.f * d0, d0, e[i][1]) + fmaf(128.f * d1, d1, e[i][3]);
    }
    const float rstd = __builtin_amdgcn_rsqf(q * (1.f / D) + 1e-5f);
    u32x4 o0, o1;
#pragma unroll
    for (int j = 0; j < 4; ++j) {
      o0[j] = pack2h((v[2 * j] - mean) * rstd, (v[2 * j + 1] - mean) * rstd);
      o1[j] = pack2h((v[8 + 2 * j] - mean) * rstd, (v[8 + 2 * j + 1] - mean) * rstd);
    }
    u16* np = nbuf + (size_t)(bid >> 2) * 32768 + (ch >> 2) * 1024 + (bid & 2) * 256 + (ch & 3) * 128 + ((bid & 1) * 8 + r) * 8;
    st16(np, o0);
    st16(np + 2048, o1);
  }
  if (gv) {
    PFR(8);
    asm volatile("s_waitcnt vmcnt(10)" : "+v"(cw)::"memory");
    const float sc[4] = {silu_bf(bflo(cw[0])), silu_bf(bfhi(cw[0])), silu_bf(bflo(cw[1])), silu_bf(bfhi(cw[1]))};
    asm volatile("s_waitcnt vmcnt(6)" : "+v"(w[0]), "+v"(w[1]), "+v"(w[2]), "+v"(w[3])::"memory");
    float acc[8];
#pragma unroll
    for (int i = 0; i < 4; ++i)
#pragma unroll
      for (int j = 0; j < 4; ++j) {
        acc[2 * j] = i ? fmaf(sc[i], bflo(w[i][j]), acc[2 * j]) : sc[0] * bflo(w[0][j]);
        acc[2 * j + 1] = i ? fmaf(sc[i], bfhi(w[i][j]), acc[2 * j + 1]) : sc[0] * bfhi(w[0][j]);
      }
    float v4[4], v2[2];
#pragma unroll
    for (int j = 0; j < 4; ++j) v4[j] = swap32_add(acc[j], acc[4 + j]);
#pragma unroll
    for (int j = 0; j < 2; ++j) v2[j] = dpp_add<0x128>(swap16_add(v4[j], v4[2 + j]));
    if ((lane & 8) == 0) *(f32x2*)&red[wid][(lane & 7) * 8 + (lane >> 5) * 4 + ((lane >> 4) & 1) * 2] = (f32x2){v2[0], v2[1]};
    LDS_BARRIER();
    if (t < 16) {
      f32x4 a = *(const f32x4*)&red[0][t * 4];
#pragma unroll
      for (int i = 1; i < 8; ++i) a += *(const f32x4*)&red[i][t * 4];
      st16(modp + (bid >> 5) * 2048 + (bid & 31) * 64 + t * 4, __builtin_bit_cast(u32x4, a));
    }
    if ((bid & 31) == 0 && (t & 7) == 0)
      asm volatile("global_store_dwordx2 %0, %1, off sc1\n\ts_nop 0"
                   :
                   : "v"((u16*)(modp + 8192) + k0), "v"((u32x2){pack2(sc[0], sc[1]), pack2(sc[2], sc[3])})
                   : "memory");
  } else {
    const int z = wid * 128 + bid - 128;
    const unsigned v = z < SY_HC / 128 ? 0u : 0xffffffffu;
    if (z < SY_GP / 128) st16(sync + z * 64 + lane, (u32x4){v, v, v, v});
    PFR(4);
  }
  asm volatile("s_waitcnt vmcnt(0)" ::: "memory");
#undef PFR
}

constexpr int K2_NB = 6, K2_BSLOT = 24576, K2_DW = 3, K2_DN = 6;
constexpr int K2_LDS_GS = K2_NB * K2_BSLOT;

#define GLOAD16(dst, voff, sbase) \
  asm volatile("global_load_dwordx4 %0, %1, %2" : "=v"(dst) : "v"(voff), "s"(sbase) : "memory")

#define DMA(V, P, L, POL) \
  asm volatile("s_nop 0\n\tglobal_load_lds_dwordx4 %0, %1" POL : : "v"(V), "s"(P), "{m0}"(L) : "memory")

constexpr unsigned POISON = 0xffffffffu;
constexpr int k2_touch(int p) { return p == 8 || p == 9 ? p - 8 : -1; }
constexpr int PA_T = 12;
constexpr int k2_npf(int t) { return t == PA_T ? 2 : 0; }
struct K2Sched { int wm, wn[16], ww[17]; };
constexpr int k2_younger(const int* seq, int n, int id) {
  int c = 0;
  for (int i = n - 1; i >= 0 && seq[i] != id; --i) ++c;
  return c;
}
constexpr K2Sched k2_make() {
  K2Sched s{};
  int seq[160] = {};
  int n = 0;
  seq[n++] = 200;
  for (int i = 0; i < 5; ++i) {
    if (2 * i < K2_DN) seq[n++] = 2 * i;
    if (2 * i + 1 < K2_DN) seq[n++] = 2 * i + 1;
    if (i < K2_DW) { seq[n++] = 100 + 3 * i; seq[n++] = 101 + 3 * i; seq[n++] = 102 + 3 * i; }
  }
  s.wm = k2_younger(seq, n, 200);
  s.ww[16] = k2_younger(seq, n, 102);
  for (int t = 0; t < 16; ++t) {
    if (2 * t + K2_DN <= 31) { seq[n++] = 2 * t + K2_DN; seq[n++] = 2 * t + 1 + K2_DN; }
    if (t + K2_DW <= 15) { seq[n++] = 100 + 3 * (t + K2_DW); seq[n++] = 101 + 3 * (t + K2_DW); seq[n++] = 102 + 3 * (t + K2_DW); }
    if (k2_touch(t) >= 0) seq[n++] = 300 + t;
    for (int i = 0; i < k2_npf(t); ++i) seq[n++] = 400;
    if (t < 15) {
      s.wn[t] = k2_younger(seq, n, 2 * t + 3);
      s.ww[t] = k2_younger(seq, n, 100 + 3 * (t + 1) + 2);
    }
  }
  return s;
}
constexpr K2Sched K2S = k2_make();

template <int KT, int PC>
DI void k2_issue_b(const u16* wq, unsigned wvoff, unsigned lds_dst) {
  if constexpr (KT <= 15) {
    const u16* sb = wq + (size_t)KT * 64 * 3072 + (PC == 2 ? 2048 - 512 : 0);
    asm volatile("s_nop 0\n\tglobal_load_lds_dwordx4 %0, %1 offset:%2"
                 :
                 : "v"(wvoff), "s"(sb), "i"(PC == 0 ? 0 : PC == 1 ? 2048 : 1024), "{m0}"(lds_dst + (KT % K2_NB) * K2_BSLOT)
                 : "memory");
  }
}
template <int F>
DI void k2_issue_n(const u16* nb, unsigned nvoff, u32x4& r) {
  if constexpr (F <= 31)
    asm volatile("global_load_dwordx4 %0, %1, %2 offset:%3" : "=v"(r) : "v"(nvoff), "s"(nb + (F >> 1) * 2048), "i"((F & 1) * 1024) : "memory");
}
struct K2State {
  f32x4 acc[2][6][2];
  bf16x8 fb[2][6];
  u32x4 nr[K2_DN];
  u32x4 hf[2][2];
  f32x4 g[2], s[2];
  const u16* ob;
  const u16* pa;
};
template <int T>
DI void k2_step(K2State& st, const unsigned (&bb)[4][2], unsigned gsa, const u16* wq, unsigned wvoff, unsigned wdst,
                const u16* nb, unsigned nvoff, unsigned lds0, int qg, unsigned ibo) {
  constexpr bool NEXT = T < 15;
  constexpr int slot = T % K2_NB;
  unsigned xl[2], xh[2], ho[8];
#define LB(J, I) do { const unsigned a_ = bb[(J) * 2 + ((I) & 1)][slot / 3] + (slot % 3) * K2_BSLOT + ((I) < 2 ? 0 : (I) < 4 ? 2048 : 1024); \
                      st.fb[J][I] = cat8(lds_tr(a_), lds_tr(a_ + 512)); SBAR(); } while (0)
#define LG(TT) do { st.g[0] = ldsr<f32x4>(gsa + (TT) * 256); st.g[1] = ldsr<f32x4>(gsa + (TT) * 256 + 16);                 \
                    st.s[0] = ldsr<f32x4>(gsa + (TT) * 256 + 4096); st.s[1] = ldsr<f32x4>(gsa + (TT) * 256 + 4096 + 16); SBAR(); } while (0)
#define VN(I) do { k2_issue_n<2 * T + (I) + K2_DN>(nb, nvoff, st.nr[(2 * T + (I)) % K2_DN]); SBAR(); } while (0)
#define VB(I) do { k2_issue_b<T + K2_DW, I>(wq, wvoff, wdst); SBAR(); } while (0)
#define XF(F, W)                                                                                             \
  asm volatile("v_fma_mix_f32 %0, %2, %3, %5 op_sel_hi:[1,0,0]\n\tv_fma_mix_f32 %1, %2, %4, %6 op_sel:[1,0,0] op_sel_hi:[1,0,0]" \
               : "=&v"(xl[(W) & 1]), "=&v"(xh[(W) & 1])                                                      \
               : "v"(st.nr[((F) + ((W) >> 2)) % K2_DN][(W) & 3]), "v"(st.g[((W) >> 1) & 1][((W) & 1) * 2]),  \
                 "v"(st.g[((W) >> 1) & 1][((W) & 1) * 2 + 1]), "v"(st.s[((W) >> 1) & 1][((W) & 1) * 2]),     \
                 "v"(st.s[((W) >> 1) & 1][((W) & 1) * 2 + 1]))
#define XC(W)                                                                                                \
  asm volatile("v_cvt_pk_bf16_f32 %0, %2, %3\n\tv_cvt_pk_bf16_f32 %1, %4, %5"                                \
               : "=&v"(ho[W]), "=&v"(ho[(W) + 1]) : "v"(xl[0]), "v"(xh[0]), "v"(xl[1]), "v"(xh[1]))
#define XSET(TT) do { st.hf[(TT) & 1][0] = (u32x4){ho[0], ho[1], ho[2], ho[3]}; st.hf[(TT) & 1][1] = (u32x4){ho[4], ho[5], ho[6], ho[7]}; } while (0)
  if constexpr (T == 0) {
    LG(0); LB(1, 0); LB(1, 1); LB(1, 2);
    asm volatile("" : "+v"(st.nr[0]), "+v"(st.nr[1]));
#pragma unroll
    for (int w = 0; w < 8; w += 2) { XF(0, w); XF(0, w + 1); XC(w); }
    XSET(0);
    SBAR();
  }
  if constexpr (NEXT) LG(T + 1);
  if constexpr (T != 0) { LB(1, 0); LB(1, 1); }
  VN(0);
  if constexpr (T != 0) LB(1, 2);
  LB(1, 3);
  VN(1); LB(1, 4); LB(1, 5);
  VB(0); LB(0, 0); LB(0, 1);
  VB(1); LB(0, 2); LB(0, 3);
  VB(2); LB(0, 4); LB(0, 5);
  if constexpr (k2_touch(T) >= 0)
    asm volatile("s_nop 0\n\tglobal_store_dwordx4 %0, %1, %2 offset:%3 sc1\n\ts_nop 1"
                 :
                 : "v"(nvoff), "v"(splat(POISON)), "s"(st.ob), "i"(k2_touch(T) * 1024)
                 : "memory");
  if constexpr (T == PA_T) {
    unsigned v = nvoff;
    asm volatile("" : "+v"(v));
    v = (v >> 7) * 2048 + (v & 127);
    DMA(v, st.pa, lds0 + 163840 - 1024, ""); DMA(v, st.pa + 8 * D, lds0 + 163840 - 1024, "");
  }
  if constexpr (NEXT)
    asm volatile("s_waitcnt vmcnt(%2)" : "+v"(st.nr[(2 * T + 2) % K2_DN]), "+v"(st.nr[(2 * T + 3) % K2_DN]) : "i"(K2S.wn[T]) : "memory");
  __builtin_amdgcn_s_waitcnt(0xc87f);
  if constexpr (T == 0) asm volatile("s_cmp_eq_u32 %0, 0\n\ts_cbranch_scc1 1f\n\ts_barrier\n\t1:" ::"s"(qg) : "memory", "scc");
  else asm volatile("s_barrier" ::: "memory");
  const bf16x8 h0 = __builtin_bit_cast(bf16x8, st.hf[T & 1][0]), h1 = __builtin_bit_cast(bf16x8, st.hf[T & 1][1]);
#define M1(J, I, TT) do { SBAR();                                                                            \
    if ((I) >= 4) st.acc[J][I][TT] = MFMA16(TT ? h1 : h0, st.fb[J][I], st.acc[J][I][TT]);                    \
    else st.acc[J][I][TT] = MFMA16(st.fb[J][I], TT ? h1 : h0, st.acc[J][I][TT]);                             \
    SBAR(); } while (0)
#define X(W) do { if constexpr (NEXT) XF(2 * T + 2, W); } while (0)
#define C(W) do { if constexpr (NEXT) XC(W); } while (0)
#define SW(I) do { if constexpr (!NEXT) ldsw(ibo + (I) * 1024, st.acc[1][(I) >> 1][(I) & 1]); } while (0)
  M1(1, 0, 0); M1(1, 0, 1); M1(1, 1, 0); M1(1, 1, 1); X(0); M1(1, 2, 0); X(1); M1(1, 2, 1); C(0);
  M1(1, 3, 0); X(2); M1(1, 3, 1); X(3); M1(1, 4, 0); C(2); M1(1, 4, 1); M1(1, 5, 0); M1(1, 5, 1);
  M1(0, 0, 0); SW(0); X(4); M1(0, 0, 1); SW(1); X(5); M1(0, 1, 0); SW(2); C(4); M1(0, 1, 1); SW(3);
  M1(0, 2, 0); SW(4); X(6); M1(0, 2, 1); SW(5); X(7); M1(0, 3, 0); SW(6); C(6); M1(0, 3, 1); SW(7);
  M1(0, 4, 0); SW(8); M1(0, 4, 1); SW(9); M1(0, 5, 0); SW(10); SW(11); M1(0, 5, 1);
  if constexpr (NEXT) {
    XSET(T + 1);
    asm volatile("s_waitcnt vmcnt(%0)\n\ts_barrier" ::"i"(K2S.ww[T]) : "memory");
    k2_step<T + 1>(st, bb, gsa, wq, wvoff, wdst, nb, nvoff, lds0, qg, ibo);
  } else {
    asm volatile("s_cmp_lg_u32 %0, 0\n\ts_cbranch_scc1 1f\n\ts_barrier\n\t1:" ::"s"(qg) : "memory", "scc");
  }
#undef LB
#undef LG
#undef VN
#undef VB
#undef XF
#undef XC
#undef XSET
#undef M1
#undef X
#undef C
#undef SW
}

#define GAS __attribute__((address_space(1)))
typedef GAS unsigned long long gu64;
typedef GAS unsigned gu32;
#define RLX_AGENT __ATOMIC_RELAXED, __HIP_MEMORY_SCOPE_AGENT
constexpr unsigned SPIN_CAP = 1u << 18;
DI unsigned long long mk64(unsigned lo, unsigned hi) { return ((unsigned long long)hi << 32) | lo; }
#define AND4(v_) ((v_[0] & v_[1]) & (v_[2] & v_[3]))
#define MAX4(v_) max(max(v_[0], v_[1]), max(v_[2], v_[3]))
template <int CTRL>
DI unsigned dpp_maxu(unsigned v) {
  const unsigned o = __builtin_amdgcn_update_dpp(0u, v, CTRL, 0xf, 0xf, true);
  return v > o ? v : o;
}
DI unsigned rowmaxu(unsigned v) {
  v = dpp_maxu<0xB1>(v); v = dpp_maxu<0x4E>(v); v = dpp_maxu<0x141>(v); return dpp_maxu<0x140>(v);
}
DI float sum8(float v) { return dpp_add<0x141>(dpp_add<0x4E>(dpp_add<0xB1>(v))); }
DI float sumrows(float v) { v = swap16_add(v, v); return swap32_add(v, v); }
DI f32x2 up2(unsigned w) { return (f32x2){bflo(w), bfhi(w)}; }
DI unsigned okey(float v) {
  const unsigned raw = __float_as_uint(v);
  return v == v ? (raw ^ ((unsigned)((int)raw >> 31) | 0x80000000u)) : 0u;
}
#define GLOADC(dst, voff, sbase, off) \
  asm volatile("global_load_dwordx4 %0, %1, %2 offset:%3 sc1" : "=v"(dst) : "v"(voff), "s"(sbase), "i"(off) : "memory")
DI float ex2(float x) { return __builtin_amdgcn_exp2f(x); }
DI float xhalf_max(float v) {
  auto rr = __builtin_amdgcn_permlane32_swap(__float_as_uint(v), __float_as_uint(v), false, false);
  return fmaxf(__uint_as_float(rr[0]), __uint_as_float(rr[1]));
}
DI void lds_arrive(unsigned a) {
  asm volatile("s_waitcnt lgkmcnt(0)\n\ts_mov_b64 exec, 1\n\tds_add_u32 %0, %1\n\ts_mov_b64 exec, -1" ::"v"(a), "v"(1u) : "memory");
}
DI unsigned lds_peek(unsigned a) { return __hip_atomic_load((LAS unsigned*)(uintptr_t)a, __ATOMIC_RELAXED, __HIP_MEMORY_SCOPE_WORKGROUP); }
DI void lds_await(unsigned a, unsigned n) {
  if (__builtin_expect(lds_peek(a) < n, 0)) {
    do __builtin_amdgcn_s_sleep(2); while (lds_peek(a) < n);
  }
  asm volatile("" ::: "memory");
}

constexpr int HD0 = 20, HD0R = 8, HD0B = 10, HD1 = 16, HD2 = 32, HD3 = 16, HD4 = 16, PG = 4, HDK = 10, HDKR = 8;
constexpr int SH_D = 4, SH_L1 = 4, MV_AT = 16;
constexpr int K4_SM = 153600;
constexpr int K4_VEC = K4_SM + 2048;
constexpr int K4_FIN = K4_VEC + 1536;
constexpr int K4_RED = K4_FIN + 512;
constexpr int K3_CNT = K4_RED + 1024;
constexpr int K4_IMG = 0;
constexpr int K3_QX = 49152;
constexpr int K3_LDS = 163840, K3_PART = 4096 + 256;

__global__ __launch_bounds__(512) void k2_main(const u16* __restrict__ nf, const u16* __restrict__ wqkv,
                                               const float* __restrict__ modp, u16* kvf, u16* of,
                                               const u16* __restrict__ wo, const u16* __restrict__ x,
                                               const u16* __restrict__ wr, const u16* __restrict__ wg,
                                               const u16* __restrict__ wu, const u16* __restrict__ wd,
                                               u16* __restrict__ out, unsigned long long* sync,
                                               const u16* __restrict__ wmod) {
  extern __shared__ __attribute__((aligned(16))) char smem[];
  const unsigned lds0 = (unsigned)(uintptr_t)smem;
  const int tid = threadIdx.x, lane = tid & 63;
  const int wid = __builtin_amdgcn_readfirstlane(tid >> 6);
  const int b = blockIdx.x, xcd = b & 7, jj = b >> 3;
  const int mt = (xcd & 1) * 8 + (jj & 7), nt = (xcd >> 1) * 4 + (jj >> 3);
  const int qg = wid >> 2, kq = wid & 3;
  const int ot = (kq + 3) & 3;
  const int kr = lane >> 3, pos = lane & 7;
  const bool has_gu = b < 224;
  const int cg = b % 56, ks = has_gu ? b / 56 : 0;
  const int dcg = b & 15, dks = b >> 4;
  asm volatile("" : "+s"(kvf), "+s"(of), "+s"(wo), "+s"(x), "+s"(wr), "+s"(wg), "+s"(wu), "+s"(wd), "+s"(out), "+s"(sync), "+s"(wmod));
  u32x4 fq[2], fk[2], fv[2];
  {
    const int wm = kq, kh = qg;
    K2State st;
    if (tid < 16) ldsw(lds0 + K3_CNT + tid * 4, 0u);
    st.ob = of + ((size_t)(((mt * 4 + ot) * 2 + qg) * 16 + nt) * 2) * 512;
    st.pa = wo + (size_t)((mt & 7) * 128 + wid * 16) * D + nt * 64;
    f32x4 mp0, mp1, mp2, mp3;
    {
      const unsigned vo = (tid & 255) * 16;
      const float* mb = modp + (kh == 0 ? 1024 : 0);
      GLOAD16(mp0, vo, mb); GLOAD16(mp1, vo, mb + 2048); GLOAD16(mp2, vo, mb + 4096); GLOAD16(mp3, vo, mb + 6144);
    }
    const unsigned wvoff = (unsigned)(((wid * 8 + kr) * 3072 + (pos ^ ((kr & 2) | ((wid & 1) << 2))) * 8) * 2);
    const u16* wq = wqkv + nt * 64;
    const unsigned wdst = lds0 + wid * 3072;
    const u16* nb = nf + (size_t)(mt * 4 + wm) * 32768 + kh * 1024;
    const unsigned nvoff = lane * 16;
#define WT(KT) do { if constexpr (KT < K2_DW) { k2_issue_b<KT, 0>(wq, wvoff, wdst); k2_issue_b<KT, 1>(wq, wvoff, wdst); \
                                                  k2_issue_b<KT, 2>(wq, wvoff, wdst); } } while (0)
#define NT(F) do { if constexpr (F < K2_DN) k2_issue_n<F>(nb, nvoff, st.nr[(F) % K2_DN]); } while (0)
    NT(0); NT(1); WT(0); NT(2); NT(3); WT(1); NT(4); NT(5); WT(2); NT(6); NT(7); WT(3); NT(8); NT(9); WT(4);
#undef WT
#undef NT
    asm volatile("s_waitcnt vmcnt(%4)" : "+v"(mp0), "+v"(mp1), "+v"(mp2), "+v"(mp3) : "i"(K2S.wm) : "memory");
    {
      f32x4 v;
      const float one = kh == 0 ? 1.f : 0.f;
#pragma unroll
      for (int j = 0; j < 4; ++j) {
        v[j] = rbf(one + rbf((mp0[j] + mp1[j]) + (mp2[j] + mp3[j])));
      }
      ldsw(lds0 + K2_LDS_GS + tid * 16, v);
    }
    unsigned bb[4][2];
#pragma unroll
    for (int c = 0; c < 4; ++c) {
      bb[c][0] = lds0 + (kh * 4 + (lane >> 4)) * 3072 + ((lane >> 2) & 3) * 128 + (((c ^ (kh * 2)) ^ ((lane >> 3) & 3)) * 32) + (lane & 3) * 8;
      bb[c][1] = bb[c][0] + 3 * K2_BSLOT;
      asm volatile("" : "+v"(bb[c][0]), "+v"(bb[c][1]));
    }
    unsigned gsa = lds0 + K2_LDS_GS + (32 * kh + 8 * (lane >> 4)) * 4;
    asm volatile("" : "+v"(gsa));
#pragma unroll
    for (int i = 0; i < 24; ++i) st.acc[i / 12][(i >> 1) % 6][i & 1] = (f32x4){};
    asm volatile("s_waitcnt vmcnt(%0) lgkmcnt(0)\n\ts_barrier" ::"i"(K2S.ww[16]) : "memory");
    const unsigned ibo = lds0 + (qg == 1 ? 0 : 98304) + kq * 12288 + lane * 16;
    const unsigned ibm = lds0 + (qg == 0 ? 0 : 98304) + kq * 12288 + lane * 16;
    k2_step<0>(st, bb, gsa, wq, wvoff, wdst, nb, nvoff, lds0, qg, ibo);
    LDS_BARRIER();
    f32x4 in[12];
#pragma unroll
    for (int i = 0; i < 12; ++i) in[i] = ldsr<f32x4>(ibm + (i < 8 ? 4096 + i * 1024 : (i - 8) * 1024));
#pragma unroll
    for (int i = 0; i < 3; ++i) {
      const int sg = i < 2 ? i + 1 : 0;
      const float sc = sg == 0 ? QSCALE : 1.f;
      unsigned w[2][2][2];
#pragma unroll
      for (int e = 0; e < 4; ++e) {
        const f32x4 a = st.acc[0][sg * 2 + (e >> 1)][e & 1], c = in[i * 4 + e];
        w[e >> 1][e & 1][0] = pack2((a[0] + c[0]) * sc, (a[1] + c[1]) * sc);
        w[e >> 1][e & 1][1] = pack2((a[2] + c[2]) * sc, (a[3] + c[3]) * sc);
      }
#pragma unroll
      for (int s = 0; s < 2; ++s) {
        u32x4 o;
#pragma unroll
        for (int j = 0; j < 2; ++j) {
          auto r = sg == 2 ? __builtin_amdgcn_permlane16_swap(w[0][s][j], w[1][s][j], false, false)
                           : __builtin_amdgcn_permlane16_swap(w[s][0][j], w[s][1][j], false, false);
          o[j] = r[0]; o[2 + j] = r[1];
        }
        if (sg == 0) { fq[s] = o; ldsw(lds0 + K3_QX + wid * 2048 + s * 1024 + lane * 16, o); }
        else {
          if (sg == 1) fk[s] = o; else fv[s] = o;
          st16(kvf + (size_t)(sg - 1) * (2u << 20) + ((size_t)((nt * 2 + qg) * 64 + mt * 4 + kq) * 2 + s) * 512 + lane * 8, o);
        }
      }
    }
  }
  u16* const op = of + ((size_t)(((mt * 4 + ot) * 2 + qg) * 16 + nt) * 2) * 512;
  const int erow = kq * 32 + qg * 16 + kr;
  bf16x8 wgu[16], wdn[7];
  u32x4 xr[4];
  u32x4 ofr[16];
  const int flip = nt & 1;
  const u16* const ob = of + (size_t)(mt * 4 + kq) * 32768 + (qg ^ flip) * 8192;
#define OLOAD(BASE, F0, F1)                                                                          \
  _Pragma("unroll") for (int f = F0; f < F1; ++f)                                                    \
    asm volatile("global_load_dwordx4 %0, %1, %2 offset:%3 sc1"                                      \
                 : "=v"(ofr[f]) : "v"(lane * 16), "s"((BASE) + (f >> 2) * 2048), "i"((f & 3) * 1024) : "memory")
  const unsigned wvoff = (unsigned)(((kq * 8 + kr) * D + (pos ^ (kr & 2)) * 8) * 2);
  const u16* const wb[2] = {wo + nt * 64 + (size_t)flip * 512 * D, wo + nt * 64 + (size_t)(1 - flip) * 512 * D};
#define WDMA(P, HF)                                                                                  \
  asm volatile("s_nop 0\n\tglobal_load_lds_dwordx4 %0, %1"                                           \
               :                                                                                     \
               : "v"(wvoff), "s"(wb[(P) >> 3] + (size_t)(((P) & 7) * 64 + (HF) * 32) * D),           \
                 "{m0}"(lds0 + K4_IMG + ((HF) * 16 + (P)) % 24 * 4096 + kq * 1024)                   \
               : "memory")
  const int sc = pos ^ ((kr & 2) | ((kq & 1) << 2));
  const unsigned vgu = (unsigned)(((kq * 8 + kr) * FF + sc * 8) * 2), vdn = (unsigned)(((kq * 8 + kr) * D + sc * 8) * 2);
  const size_t gbase = (size_t)(ks * 256) * FF + cg * 64;
#define SDMA(U)                                                                                      \
  asm volatile("s_nop 0\n\tglobal_load_lds_dwordx4 %0, %1 nt"                                        \
               :                                                                                     \
               : "v"(sv), "s"(sp + ((U) == 3 ? s3 : (U) * ss)), "{m0}"(sl + (U) * 4096)              \
               : "memory")
  {
    const int head = nt * 2 + qg;
    const __amdgpu_buffer_rsrc_t rs = __builtin_amdgcn_make_buffer_rsrc((void*)kvf, 0, 8 << 20, 0x00020000);
    const int vk = lane * 16, vv = lane * 16 + (4 << 20);
    const int hb = head * 131072 + kq * 2048;
#define LDK(so, c) __builtin_bit_cast(bf16x8, __builtin_amdgcn_raw_buffer_load_b128(rs, vk + (c), (so), 16))
#define LDV(so, c) __builtin_bit_cast(bf16x8, __builtin_amdgcn_raw_buffer_load_b128(rs, vv + (c), (so), 16))
#define TOFF(i) (hb + ((mt + (i)) & 15) * 8192)
    const u32x4 rv = {(unsigned)(uintptr_t)kvf, (unsigned)((uintptr_t)kvf >> 32) & 0xffffu, 8u << 20, 0x00020000u};
#define KVL(R, VO, so, c) \
  asm volatile("buffer_load_dwordx4 %0, %1, %2, %3 offen offset:%4 sc1" : "+v"(R) : "v"(VO), "s"(rv), "s"(so), "i"(c) : "memory")
#define KVW(R, N) do { if constexpr ((N) >= 0) { __builtin_amdgcn_s_waitcnt(0x0f70 | (N)); SBAR(); } } while (0)
    LDS_BARRIER();
    bf16x8 q[4][2];
    q[0][0] = __builtin_bit_cast(bf16x8, fq[0]); q[0][1] = __builtin_bit_cast(bf16x8, fq[1]);
#pragma unroll
    for (int i = 1; i < 4; ++i) {
      const unsigned qa = lds0 + K3_QX + (qg * 4 + ((i + kq) & 3)) * 2048 + lane * 16;
      q[i][0] = lds_b128(qa); q[i][1] = lds_b128(qa + 1024);
    }
    bf16x8 ka0 = __builtin_bit_cast(bf16x8, fk[0]), ka1 = __builtin_bit_cast(bf16x8, fk[1]);
    bf16x8 va0 = __builtin_bit_cast(bf16x8, fv[0]), va1 = __builtin_bit_cast(bf16x8, fv[1]);
    bf16x8 kb0, kb1, vb0, vb1;
    asm volatile("" : "=v"(kb0), "=v"(kb1), "=v"(vb0), "=v"(vb1));
    bf16x8 ones;
    {
#pragma unroll
      for (int j = 0; j < 8; ++j) ones[j] = ((lane >> 2) & 1) == (j >> 2) ? (short)0x3F80 : (short)0;
    }
    int ix = (int)0xddddddddu;
    asm volatile("" : "+v"(ix), "+v"(ones));
    const f32x16 zero = (f32x16){};
    f32x16 o[4], s[4];
    f32x4 l[4];
    s[0] = MFMA32(ka0, q[0][0], zero);
    s[0] = MFMA32(ka1, q[0][1], s[0]);
    asm volatile("s_nop 1\n\tv_mfma_f32_32x32x16_bf16 %0, %9, %9, 0\n\tv_mfma_f32_16x16x32_bf16 %4, %9, %9, 0\n\t"
                 "v_mfma_f32_32x32x16_bf16 %1, %9, %9, 0\n\tv_mfma_f32_16x16x32_bf16 %5, %9, %9, 0\n\t"
                 "v_mfma_f32_32x32x16_bf16 %2, %9, %9, 0\n\tv_mfma_f32_16x16x32_bf16 %6, %9, %9, 0\n\t"
                 "v_mfma_f32_32x32x16_bf16 %3, %9, %9, 0\n\tv_mfma_f32_16x16x32_bf16 %7, %9, %9, 0\n\ts_nop 3"
                 : "=&v"(o[0]), "=&v"(o[1]), "=&v"(o[2]), "=&v"(o[3]), "=&v"(l[0]), "=&v"(l[1]), "=&v"(l[2]), "=&v"(l[3]), "+v"(s[0])
                 : "v"(splat(0u)));
    asm volatile("s_waitcnt lgkmcnt(0)\n\ts_barrier"
                 : "+v"(q[1][0]), "+v"(q[1][1]), "+v"(q[2][0]), "+v"(q[2][1]), "+v"(q[3][0]), "+v"(q[3][1])::"memory");
#define PIN(x_) asm volatile("" : "+v"(x_))
#define EX(C, i) do { float t_ = ex2(s[C][i]); PIN(t_); s[C][i] = t_; } while (0)
#define UNITW(KN0, KN1, C, V0C, V1P, LD_A, LD_B, LD_C, WK0, WK1, WV1, WV0)                 \
  do {                                                                                     \
    constexpr int N = ((C) + 1) & 3, P = ((C) + 3) & 3;                                    \
    KVW(KN0, WK0); if ((WK0) != -2) { s[N] = MFMA32(KN0, q[N][0], zero); PIN(s[N]); } SBAR(); \
    EX(C, 0); EX(C, 1); EX(C, 2); SBAR();                                                  \
    KVW(KN1, WK1); if ((WK0) != -2) { s[N] = MFMA32(KN1, q[N][1], s[N]); PIN(s[N]); } SBAR(); \
    EX(C, 3); EX(C, 4); EX(C, 5); SBAR();                                                  \
    KVW(V1P, WV1); if ((WV1) != -2) { o[P] = SMF32(V1P, s[P], 1, o[P], ix); PIN(o[P]); } SBAR(); \
    LD_A;                                                                                  \
    EX(C, 6); EX(C, 7); EX(C, 8); SBAR();                                                  \
    if ((WV1) != -2) { l[P] = SMF16(ones, s[P], 1, l[P], ix); PIN(l[P]); } SBAR();         \
    LD_B;                                                                                  \
    EX(C, 9); EX(C, 10); SBAR();                                                           \
    KVW(V0C, WV0); o[C] = SMF32(V0C, s[C], 0, o[C], ix); PIN(o[C]); SBAR();                \
    EX(C, 11); EX(C, 12); EX(C, 13); SBAR();                                               \
    l[C] = SMF16(ones, s[C], 0, l[C], ix); PIN(l[C]); SBAR();                              \
    EX(C, 14); EX(C, 15); SBAR();                                                          \
    LD_C;                                                                                  \
  } while (0)
#define NO (void)0
#define UNIT(KN0, KN1, C, V0C, V1P) UNITW(KN0, KN1, C, V0C, V1P, NO, NO, NO, -1, -1, -1, -1)
    const unsigned fl = (unsigned)((SY_KV + (head * 4 + kq) * 16) * 8);
    const unsigned fo = (unsigned)((SY_OC + (mt * 4 + ot) * 16) * 8);
#define KVFLAG()                                                                                       \
  do {                                                                                                 \
    asm volatile("s_waitcnt vmcnt(0)" ::: "memory");                                                   \
    if (lane < 2) __hip_atomic_store((gu32*)((char*)sync + (lane ? fo + head * 4 : fl + mt * 4)), 1u, RLX_AGENT); \
    SBAR();                                                                                            \
  } while (0)
    UNITW(ka0, ka1, 0, va0, va1, NO, NO, NO, -1, -1, -2, -1);
    KVFLAG();
    UNIT(ka0, ka1, 1, va0, va1);
    UNIT(ka0, ka1, 2, va0, va1);
    {
      const unsigned vo = fl + (lane & 15) * 4;
      __builtin_amdgcn_s_sleep(HDK);
      _Pragma("nounroll") for (unsigned spin = 0; spin < SPIN_CAP; ++spin) {
        unsigned c;
        asm volatile("global_load_dword %0, %1, %2 sc1\n\ts_waitcnt vmcnt(0)" : "=v"(c) : "v"(vo), "s"(sync) : "memory");
        if (__all(c != 0u)) break;
        __builtin_amdgcn_s_sleep(HDKR);
      }
    }
    const unsigned rb = lds0 + 98304;
    KVL(kb0, vk, TOFF(1), 0); KVL(kb1, vk, TOFF(1), 1024);
    KVL(vb0, vv, TOFF(1), 0); KVL(vb1, vv, TOFF(1), 1024);
    KVL(ka0, vk, TOFF(2), 0); KVL(ka1, vk, TOFF(2), 1024);
    const unsigned vy = (unsigned)((kr * 6144 + (pos ^ (kr & 2)) * 8) * 2);
    const u16* const yp = wmod + (size_t)(mt * 64) * 6144 + 2048 + kq * 1024 + nt * 64;
    if (qg == 0) {
      const unsigned zo = 8 * (lane < 32 ? SY_ST + (mt * 16 + nt) * STP + kq * 64 + lane * 2
                               : lane < 40 ? SY_GP + ks * GPP + cg * 64 + kq * 16 + (lane - 32) * 2
                                           : SY_GY + dks * GYP + dcg * 32 + kq * 8 + (lane & 3) * 2);
      asm volatile("s_mov_b64 exec, %3\n\tglobal_store_dwordx4 %0, %1, %2 sc1\n\ts_mov_b64 exec, -1\n\ts_nop 0"
                   :
                   : "v"(zo), "v"((u32x4){0u, 0u, 0u, 0u}), "s"(sync), "s"(0xfffffffffffull)
                   : "memory");
    } else {
      DMA(vk, (const u16*)(modp + 8192) + mt * 64, rb + kq * 12288 + 8192, "");
#pragma unroll
      for (int i = 0; i < 8; ++i) DMA(vy, yp + i * 8 * 6144, rb + kq * 12288 + i * 1024, " nt");
      __builtin_amdgcn_s_setprio(1);
    }
    UNITW(kb0, kb1, 3, va0, va1, NO, NO, NO, 6, 5, -1, -1);
    UNITW(kb0, kb1, 0, vb0, va1, KVL(va0, vv, TOFF(2), 0), KVL(va1, vv, TOFF(2), 1024), NO, -1, -1, -1, 6);
    UNITW(kb0, kb1, 1, vb0, vb1, NO, NO, NO, -1, -1, 5, -1);
    UNIT(kb0, kb1, 2, vb0, vb1);
    UNITW(ka0, ka1, 3, vb0, vb1, KVL(kb0, vk, TOFF(3), 0), KVL(kb1, vk, TOFF(3), 1024), NO, 4, 3, -1, -1);
    asm volatile("s_waitcnt vmcnt(2)" ::: "memory");
#define SEND(I)                                                                                                          \
  do {                                                                                                                   \
    const unsigned wa = rb + ((((I) + kq + 1) & 3) * 3 + (I)) * K3_PART;                                                 \
    _Pragma("unroll") for (int g = 0; g < 4; ++g)                                                                        \
      ldsw(wa + g * 1024 + lane * 16, (f32x4){o[I][4 * g], o[I][4 * g + 1], o[I][4 * g + 2], o[I][4 * g + 3]});          \
    ldsw(wa + 4096 + lane * 4, l[I][0]);                                                                                 \
  } while (0)
#define PASS(X, NX)                                                                                   \
  do {                                                                                               \
    const int t1 = TOFF(j + 1), t2 = TOFF(j + 2), t3 = TOFF(j + 3);                                  \
    UNITW(ka0, ka1, 0, va0, vb1, KVL(vb0, vv, t1, 0), KVL(vb1, vv, t1, 1024), NO, -1, -1, -1, 5 + NX); \
    UNITW(ka0, ka1, 1, va0, va1, X(0), X(1), NO, -1, -1, 4 + NX, -1);                                \
    UNIT(ka0, ka1, 2, va0, va1);                                                                     \
    UNITW(kb0, kb1, 3, va0, va1, KVL(ka0, vk, t2, 0), KVL(ka1, vk, t2, 1024), NO, 3 + NX, 2 + NX, -1, -1); \
    UNITW(kb0, kb1, 0, vb0, va1, KVL(va0, vv, t2, 0), KVL(va1, vv, t2, 1024), NO, -1, -1, -1, 5 + NX); \
    UNITW(kb0, kb1, 1, vb0, vb1, X(2), X(3), NO, -1, -1, 4 + NX, -1);                                \
    UNIT(kb0, kb1, 2, vb0, vb1);                                                                     \
    UNITW(ka0, ka1, 3, vb0, vb1, KVL(kb0, vk, t3, 0), KVL(kb1, vk, t3, 1024), NO, 3 + NX, 2 + NX, -1, -1); \
  } while (0)
#define NOP1(U) (void)0
    int j = 2, e0 = qg == 0 ? 14 : 2;
    asm volatile("" : "+s"(e0));
#pragma unroll 1
    for (; j < e0; j += 2) {
      const unsigned sv = j < 10 ? vgu : vdn, sl = lds0 + (j - 2) * 8192 + kq * 1024;
      const int ss = j < 10 ? 32 * FF : 32 * D, s3 = j == 12 ? 2 * ss : 3 * ss;
      const u16* sp = (j < 6 ? wg + gbase : j < 10 ? wu + gbase : wd + (size_t)(dks * 224) * D + dcg * 64) + ((j - 2) & 2) * 2 * ss;
      PASS(SDMA, 2);
    }
    if (e0 == 2) {
      const unsigned ya = rb + kq * 12288 + (lane >> 4) * 1024 + ((lane >> 2) & 3) * 128 + ((lane >> 3) & 1) * 32 + (lane & 3) * 8;
      const bf16x8 c0 = lds_b128(rb + kq * 12288 + 8192 + (lane >> 4) * 16), c1 = lds_b128(rb + kq * 12288 + 8192 + 64 + (lane >> 4) * 16);
      float mv = 0.f;
#pragma unroll
      for (int i = 0; i < 4; ++i) {
        f32x4 a = MFMA16(c0, cat8(lds_tr((ya ^ (i & 1) * 32) + (i >> 1) * 64), lds_tr((ya ^ (i & 1) * 32) + (i >> 1) * 64 + 512)), (f32x4){});
        a = MFMA16(c1, cat8(lds_tr((ya ^ (i & 1) * 32) + (i >> 1) * 64 + 4096), lds_tr((ya ^ (i & 1) * 32) + (i >> 1) * 64 + 4608)), a);
        mv = (lane >> 4) == i ? a[0] : mv;
      }
      lds_arrive(lds0 + K3_CNT + 60);
      asm volatile("global_store_dword %0, %1, %2 sc1\n\ts_nop 0"
                   :
                   : "v"(lane * 4), "v"(__float_as_uint(mv) | 1u), "s"(sync + SY_MV + ((nt * 4 + kq) * 16 + mt) * 32)
                   : "memory");
      SBAR();
    }
#pragma unroll 1
    for (; j < 14; j += 2) {
      asm volatile("s_cmp_lg_u32 %0, 4\n\ts_cbranch_scc1 1f\n\ts_setprio 0\n\t1:" ::"s"(j) : "scc");
      PASS(NOP1, 0);
    }
    lds_await(lds0 + K3_CNT + (qg ? 4 : 60), 4u);
#define GATE()                                                                                                           \
  asm volatile("s_mov_b64 exec, %2\n\tds_add_u32 %0, %1\n\ts_mov_b64 exec, -1"                                           \
               :                                                                                                         \
               : "v"(lds0 + K3_CNT + 32 + kq * 4), "v"(1u), "s"((unsigned long long)qg)                                  \
               : "memory")
    const unsigned fr = (unsigned)((SY_OC + (mt * 4 + kq) * 16) * 8 + (lane & 31) * 4);
    unsigned fc;
    UNITW(ka0, ka1, 0, va0, vb1, KVL(vb0, vv, TOFF(15), 0), KVL(vb1, vv, TOFF(15), 1024), GATE(), -1, -1, -1, 5);
    UNITW(ka0, ka1, 1, va0, va1, NO, NO, NO, -1, -1, 4, -1);
    UNIT(ka0, ka1, 2, va0, va1);
    UNITW(kb0, kb1, 3, va0, va1, NO, NO, NO, 3, 2, -1, -1);
    UNITW(kb0, kb1, 0, vb0, va1, NO, NO, NO, -1, -1, -1, 1);
    UNITW(kb0, kb1, 1, vb0, vb1, NO, NO, SEND(0), -1, -1, 0, -1);
    UNITW(kb0, kb1, 2, vb0, vb1, NO, NO, SEND(1), -1, -1, -1, -1);
    EX(3, 0); EX(3, 1); SBAR();
    o[2] = SMF32(vb1, s[2], 1, o[2], ix); PIN(o[2]); SBAR();
    EX(3, 2); EX(3, 3); EX(3, 4); SBAR();
    l[2] = SMF16(ones, s[2], 1, l[2], ix); PIN(l[2]); SBAR();
    EX(3, 5); EX(3, 6); EX(3, 7); EX(3, 8); EX(3, 9); SBAR();
    o[3] = SMF32(vb0, s[3], 0, o[3], ix); PIN(o[3]); SBAR();
    EX(3, 10); EX(3, 11); SBAR();
    SEND(2);
    asm volatile("s_mov_b64 exec, 1\n\tds_add_u32 %0, %1\n\ts_mov_b64 exec, %3\n\tds_add_u32 %2, %1\n\ts_mov_b64 exec, -1"
                 :
                 : "v"(lds0 + K3_CNT), "v"(1u), "v"(lds0 + K3_CNT + 32 + kq * 4), "s"((unsigned long long)qg)
                 : "memory");
    SBAR();
    EX(3, 12); EX(3, 13); SBAR();
    l[3] = SMF16(ones, s[3], 0, l[3], ix); PIN(l[3]); SBAR();
    EX(3, 14); EX(3, 15); SBAR();
    asm volatile("s_mov_b64 exec, %7\n\tglobal_load_dword %0, %5, %6 sc1\n\t"
                 "global_load_dwordx4 %1, %8, %9 nt\n\tglobal_load_dwordx4 %2, %8, %9 offset:2048 nt\n\t"
                 "global_load_dwordx4 %3, %8, %10 nt\n\tglobal_load_dwordx4 %4, %8, %10 offset:2048 nt\n\ts_mov_b64 exec, -1"
                 : "=&v"(fc), "=&v"(xr[0]), "=&v"(xr[1]), "=&v"(xr[2]), "=&v"(xr[3])
                 : "v"(fr), "s"(sync), "s"((long long)(qg - 1)), "v"((lane & 28) * (D * 2) + (lane >> 5) * 64 + (lane & 3) * 16),
                   "s"(x + (size_t)(mt * 128 + kq * 32) * D + nt * 64), "s"(x + (size_t)(mt * 128 + kq * 32 + 2) * D + nt * 64)
                 : "memory");
    o[3] = SMF32(vb1, s[3], 1, o[3], ix);
    l[3] = SMF16(ones, s[3], 1, l[3], ix);
    PIN(o[3]); PIN(l[3]);
#undef UNIT
#undef UNITW
    unsigned ro_ = rb + kq * 3 * K3_PART + lane * 16, rl_ = rb + kq * 3 * K3_PART + 4096 + lane * 4;
    PIN(ro_); PIN(rl_);
    lds_await(lds0 + K3_CNT, qg == 0 ? 4u : 8u);
    float lsum = l[3][0], big = 0.f;
    u32x4 w[2];
    {
      f32x4 pv[4][3];
      float pl[3];
#pragma unroll
      for (int i = 0; i < 3; ++i) pl[i] = ldsr<float>(rl_ + i * K3_PART);
#pragma unroll
      for (int g = 0; g < 4; ++g)
#pragma unroll
        for (int i = 0; i < 3; ++i) pv[g][i] = ldsr<f32x4>(ro_ + i * K3_PART + g * 1024);
      SBAR();
      lsum = ((lsum + pl[0]) + pl[1]) + pl[2];
      const float rl = __builtin_amdgcn_rcpf(lsum);
      SBAR();
#pragma unroll
      for (int g = 0; g < 4; ++g) {
        float a0, a1, a2, a3;
        unsigned w0, w1;
        asm volatile("v_add_f32 %0, %20, %7\n\tv_add_f32 %1, %21, %8\n\tv_add_f32 %2, %22, %9\n\tv_add_f32 %3, %23, %10\n\t"
            "v_add_f32 %0, %0, %11\n\tv_add_f32 %1, %1, %12\n\tv_add_f32 %2, %2, %13\n\tv_add_f32 %3, %3, %14\n\t"
            "v_add_f32 %0, %0, %15\n\tv_add_f32 %1, %1, %16\n\tv_add_f32 %2, %2, %17\n\tv_add_f32 %3, %3, %18\n\t"
            "v_maximum3_f32 %4, |%0|, |%1|, %4\n\tv_mul_f32 %0, %0, %19\n\tv_mul_f32 %1, %1, %19\n\t"
            "v_maximum3_f32 %4, |%2|, |%3|, %4\n\tv_mul_f32 %2, %2, %19\n\tv_mul_f32 %3, %3, %19\n\t"
            "v_cvt_pk_bf16_f32 %5, %0, %1\n\tv_cvt_pk_bf16_f32 %6, %2, %3"
            : "=&v"(a0), "=&v"(a1), "=&v"(a2), "=&v"(a3), "+v"(big), "=v"(w0), "=v"(w1)
            : "v"(pv[g][0][0]), "v"(pv[g][0][1]), "v"(pv[g][0][2]), "v"(pv[g][0][3]), "v"(pv[g][1][0]), "v"(pv[g][1][1]),
              "v"(pv[g][1][2]), "v"(pv[g][1][3]), "v"(pv[g][2][0]), "v"(pv[g][2][1]), "v"(pv[g][2][2]), "v"(pv[g][2][3]), "v"(rl),
              "v"(o[3][4 * g]), "v"(o[3][4 * g + 1]), "v"(o[3][4 * g + 2]), "v"(o[3][4 * g + 3]));
        w[g >> 1][(g & 1) * 2] = w0; w[g >> 1][(g & 1) * 2 + 1] = w1;
      }
    }
#define OST()                                                                                                             \
  asm volatile("s_nop 0\n\tglobal_store_dwordx4 %0, %1, %3 sc1\n\tglobal_store_dwordx4 %0, %2, %3 offset:1024 sc1\n\ts_nop 1" \
               :                                                                                                          \
               : "v"(vk), "v"(w[0]), "v"(w[1]), "s"(op)                                                                   \
               : "memory")
    if (__builtin_expect(__any(!(lsum < 1.0e30f && lsum > 1.0e-30f && big < 3.0e38f)), 0)) {
      asm volatile("s_waitcnt vmcnt(0)" ::: "memory");
      const bf16x8 q0 = q[3][0], q1 = q[3][1];
      float m = -INFINITY, ls = 0.f;
      f32x16 oo = zero;
      for (int t = 0; t < 64; ++t) {
        const int so = head * 131072 + t * 2048;
        const bf16x8 a0 = LDK(so, 0), a1 = LDK(so, 1024), b0 = LDV(so, 0), b1 = LDV(so, 1024);
        f32x16 sf = MFMA32(a0, q0, zero);
        sf = MFMA32(a1, q1, sf);
        float tm = sf[0];
#pragma unroll
        for (int r = 1; r < 16; ++r) tm = fmaxf(tm, sf[r]);
        tm = xhalf_max(tm);
        const float mn = fmaxf(m, tm);
        const float alpha = ex2(m - mn);
        m = mn;
        float ps = 0.f;
#pragma unroll
        for (int r = 0; r < 16; ++r) {
          sf[r] = ex2(sf[r] - mn);
          ps += bfhi(__float_as_uint(sf[r]));
        }
        ps += __shfl_xor(ps, 32);
        ls = ls * alpha + ps;
#pragma unroll
        for (int r = 0; r < 16; ++r) oo[r] *= alpha;
        oo = SMF32(b0, sf, 0, oo, ix);
        oo = SMF32(b1, sf, 1, oo, ix);
      }
      const float rl = 1.f / ls;
#pragma unroll
      for (int j = 0; j < 8; ++j) w[j >> 2][j & 3] = min(pack2(oo[2 * j] * rl, oo[2 * j + 1] * rl), 0xfffeffffu);
      OST();
    } else {
      OST();
    }
    if (qg == 0) {
      asm volatile("s_waitcnt vmcnt(6)" : "+v"(fc)::"memory");
      _Pragma("nounroll") for (unsigned spin = 0; spin < SPIN_CAP && !__all(fc != 0u); ++spin) {
        __builtin_amdgcn_s_sleep(8);
        asm volatile("global_load_dword %0, %1, %2 sc1\n\ts_waitcnt vmcnt(0)" : "=v"(fc) : "v"(fr), "s"(sync) : "memory");
      }
      lds_arrive(lds0 + K3_CNT + 4);
    }
#undef EX
#undef LDK
#undef LDV
#undef TOFF
  }
  if (qg == 0) {
#define PACE() asm volatile("s_waitcnt vmcnt(%0)" ::"i"(SH_D - 1) : "memory")
    {
      int ln = lane;
      PIN(ln);
      const unsigned ta = lds0 + (ln >> 4) * 1024 + ((ln >> 2) & 3) * 128 + (kq ^ ((ln >> 3) & 3)) * 32 + (ln & 3) * 8;
#pragma unroll
      for (int i = 0; i < 16; ++i) wgu[i] = cat8(lds_tr(ta + i * 4096), lds_tr(ta + i * 4096 + 512));
#pragma unroll
      for (int i = 0; i < 7; ++i) wdn[i] = cat8(lds_tr(ta + 65536 + i * 4096), lds_tr(ta + 65536 + i * 4096 + 512));
    }
    asm volatile("" : "+v"(wgu[0]), "+v"(wgu[1]), "+v"(wgu[2]), "+v"(wgu[3]), "+v"(wgu[4]), "+v"(wgu[5]), "+v"(wgu[6]), "+v"(wgu[7]),
                      "+v"(wgu[8]), "+v"(wgu[9]), "+v"(wgu[10]), "+v"(wgu[11]), "+v"(wgu[12]), "+v"(wgu[13]), "+v"(wgu[14]), "+v"(wgu[15]));
    asm volatile("" : "+v"(wdn[0]), "+v"(wdn[1]), "+v"(wdn[2]), "+v"(wdn[3]), "+v"(wdn[4]), "+v"(wdn[5]), "+v"(wdn[6])::"memory");
    lds_arrive(lds0 + K3_CNT + 12);
    lds_await(lds0 + K3_CNT + 12, 4u);
#pragma unroll
    for (int p = 0; p < SH_L1; ++p) WDMA(p & 15, p >> 4);
    u32x4 mv[4];
    u32x2 wrw;
    const unsigned long long* const mp = sync + SY_MV + (nt * 4 + kq) * 512;
#pragma unroll
    for (int p = SH_L1; p < MV_AT; ++p) { PACE(); WDMA(p & 15, p >> 4); }
#pragma unroll
    for (int i = 0; i < 4; ++i) GLOADC(mv[i], lane * 16, mp, i * 1024);
    asm volatile("global_load_dwordx2 %0, %1, %2" : "=v"(wrw) : "v"((lane & 15) * 8), "s"(wr + nt * 64) : "memory");
#pragma unroll
    for (int p = MV_AT; p < 24; ++p) { PACE(); WDMA(p & 15, p >> 4); }
    asm volatile("s_waitcnt vmcnt(0)"
                 : "+v"(mv[0]), "+v"(mv[1]), "+v"(mv[2]), "+v"(mv[3]), "+v"(wrw), "+v"(xr[0]), "+v"(xr[1]), "+v"(xr[2]), "+v"(xr[3])::"memory");
    _Pragma("nounroll") for (unsigned spin = 0; spin < SPIN_CAP; ++spin) {
      if (__builtin_expect(__all((AND4(mv[0]) & AND4(mv[1])) & (AND4(mv[2]) & AND4(mv[3])) & 1u), 1)) break;
      __builtin_amdgcn_s_sleep(8);
#pragma unroll
      for (int i = 0; i < 4; ++i) GLOADC(mv[i], lane * 16, mp, i * 1024);
      asm volatile("s_waitcnt vmcnt(0)" : "+v"(mv[0]), "+v"(mv[1]), "+v"(mv[2]), "+v"(mv[3])::"memory");
    }
    {
      f32x4 v, vw;
#pragma unroll
      for (int j = 0; j < 4; ++j) {
        v[j] = one(one(__uint_as_float(mv[0][j]) + __uint_as_float(mv[1][j])) + one(__uint_as_float(mv[2][j]) + __uint_as_float(mv[3][j])));
        v[j] = rbf(sumrows(v[j]));
        if (kq == 2) v[j] = rbf(1.f + v[j]);
        vw[j] = v[j] * ((j & 1) ? bfhi(wrw[j >> 1]) : bflo(wrw[j >> 1]));
      }
      const unsigned va = lds0 + K4_VEC + (lane & 15) * 16;
      const unsigned sw = __float_as_uint(dpp_add<0x140>(sum8((vw[0] + vw[1]) + (vw[2] + vw[3])))) | 1u;
      ldsw(va + (kq == 1 ? 512 : kq == 2 ? 256 : kq * 256), v);
      if (kq == 2) {
        const unsigned h0 = pack2(vw[0], vw[1]), h1 = pack2(vw[2], vw[3]);
        const unsigned l0 = pack2(one(vw[0] - bflo(h0)), one(vw[1] - bfhi(h0))), l1 = pack2(one(vw[2] - bflo(h1)), one(vw[3] - bfhi(h1)));
        const int rw = lane >> 4;
        ldsw(lds0 + K4_VEC + 1024 + lane * 8, (u32x2){rw == 0 ? 0x3f803f80u : rw == 1 ? h0 : rw == 2 ? l0 : sw,
                                                      rw == 0 ? 0x3f803f80u : rw == 1 ? h1 : rw == 2 ? l1 : sw});
      }
      if (mt == 0) {
        if (kq == 3 && lane < 16)
          asm volatile("s_nop 0\n\tglobal_store_dwordx4 %0, %1, %2 sc1\n\ts_nop 1" ::"v"(lane * 16), "v"(__builtin_bit_cast(u32x4, v) | 1u), "s"(sync + SY_GB + nt * 32) : "memory");
        if (kq == 1 && lane == 0) __hip_atomic_store((gu32*)(sync + SY_GB + 512) + nt, sw, RLX_AGENT);
      }
    }
    lds_arrive(lds0 + K3_CNT + 8);
#pragma unroll
    for (int d = 1; d < 3; ++d)
#pragma unroll
      for (int i = 0; i < 2; ++i)
#pragma unroll
        for (int j = 0; j < 4; ++j) {
          const int r = i * (3 - d);
          const unsigned a = xr[r][j], c = xr[r + d][j];
          const unsigned pa = d == 1 ? __builtin_amdgcn_update_dpp(0u, a, 0xB1, 0xf, 0xf, true) : __builtin_amdgcn_update_dpp(0u, a, 0x4E, 0xf, 0xf, true);
          const unsigned pc = d == 1 ? __builtin_amdgcn_update_dpp(0u, c, 0xB1, 0xf, 0xf, true) : __builtin_amdgcn_update_dpp(0u, c, 0x4E, 0xf, 0xf, true);
          xr[r][j] = (lane & d) ? pc : a;
          xr[r + d][j] = (lane & d) ? c : pa;
        }
    asm volatile("" : "+v"(xr[0]), "+v"(xr[1]), "+v"(xr[2]), "+v"(xr[3]));
  } else {
    lds_arrive(lds0 + K3_CNT + 28);
  }
  if (qg == 0) lds_await(lds0 + K3_CNT + 32 + kq * 4, 1u);
  OLOAD(ob, 0, 16);
  lds_await(lds0 + K3_CNT + 8, 4u);
  if (qg == 0) lds_await(lds0 + K3_CNT + 32 + kq * 4, 2u);
  f32x2 xv[2][4];
  u32x4 xw[2];
  {
    const int h = lane >> 5;
    unsigned bb[2];
    {
      const int q4 = (lane & 15) >> 2, p4 = lane & 3, ch = (lane >> 4) & 1;
#pragma unroll
      for (int c = 0; c < 2; ++c)
        bb[c] = lds0 + K4_IMG + qg * 32768 + (4 * h + q4) * 128 + (p4 & 1) * 64 + ((c ^ ((q4 >> 1) & 1)) * 32) + ch * 16 + (p4 >> 1) * 8;
    }
#define WFRAG(C, F) cat8(lds_tr(bb[C] + (F) * 2048), lds_tr(bb[C] + (F) * 2048 + 1024))
    f32x16 acc0 = (f32x16){}, acc1 = (f32x16){};
#define OBAD(C)                                                                                                            \
  __any(max(max(max(max(ofr[4 * (C)][0], ofr[4 * (C)][1]), max(ofr[4 * (C)][2], ofr[4 * (C)][3])),                         \
                max(max(ofr[4 * (C) + 1][0], ofr[4 * (C) + 1][1]), max(ofr[4 * (C) + 1][2], ofr[4 * (C) + 1][3]))),        \
            max(max(max(ofr[4 * (C) + 2][0], ofr[4 * (C) + 2][1]), max(ofr[4 * (C) + 2][2], ofr[4 * (C) + 2][3])),         \
                max(max(ofr[4 * (C) + 3][0], ofr[4 * (C) + 3][1]), max(ofr[4 * (C) + 3][2], ofr[4 * (C) + 3][3])))) == POISON)
#define OWAIT(C, N)                                                                                                        \
  asm volatile("s_waitcnt vmcnt(%4)"                                                                                       \
               : "+v"(ofr[4 * (C)]), "+v"(ofr[4 * (C) + 1]), "+v"(ofr[4 * (C) + 2]), "+v"(ofr[4 * (C) + 3]) : "i"(N) : "memory")
#define OCHK(BASE, C)                                                                                                      \
  do {                                                                                                                     \
    OWAIT(C, 12 - 4 * (C));                                                                                                \
    if (__builtin_expect(OBAD(C), 0)) {                                                                                    \
      _Pragma("nounroll") for (unsigned spin = 0; spin < SPIN_CAP; ++spin) {                                               \
        __builtin_amdgcn_s_sleep(HD0R);                                                                                    \
        _Pragma("unroll") for (int d = C; d < 4; ++d) OWAIT(d, 0);                                                         \
        _Pragma("unroll") for (int d = C; d < 4; ++d) {                                                                    \
          const int go = d == (C) ? 1 : __builtin_amdgcn_readfirstlane((int)OBAD(d));                                      \
          asm volatile("s_cmp_lg_u32 %6, 1\n\ts_cbranch_scc1 1f\n\t"                                                       \
                       "global_load_dwordx4 %0, %4, %5 sc1\n\tglobal_load_dwordx4 %1, %4, %5 offset:1024 sc1\n\t"          \
                       "global_load_dwordx4 %2, %4, %5 offset:2048 sc1\n\tglobal_load_dwordx4 %3, %4, %5 offset:3072 sc1\n\t1:" \
                       : "+v"(ofr[4 * d]), "+v"(ofr[4 * d + 1]), "+v"(ofr[4 * d + 2]), "+v"(ofr[4 * d + 3])                \
                       : "v"(lane * 16), "s"((BASE) + d * 2048), "s"(go) : "memory", "scc");                               \
        }                                                                                                                  \
        _Pragma("unroll") for (int d = C; d < 4; ++d) OWAIT(d, 0);                                                         \
        if (!OBAD(C)) break;                                                                                               \
      }                                                                                                                    \
    }                                                                                                                      \
  } while (0)
    bf16x8 w[4][2];
#define WLD(F) do { w[(F) & 3][0] = WFRAG(0, F); w[(F) & 3][1] = WFRAG(1, F); } while (0)
#define MM(A, F) do { SBAR(); A = MFMA32(w[(F) & 3][&A == &acc1], __builtin_bit_cast(bf16x8, ofr[F]), A); SBAR(); } while (0)
#define OCHUNK(BASE, C, HOOK)                                                                                              \
  do {                                                                                                                     \
    OCHK(BASE, C);                                                                                                         \
    _Pragma("unroll") for (int f = 4 * (C); f < 4 * (C) + 4; ++f) {                                                        \
      MM(acc0, f); MM(acc1, f);                                                                                            \
      if (f < 12) WLD(f + 4);                                                                                              \
      HOOK(f);                                                                                                             \
    }                                                                                                                      \
    PIN(acc0); PIN(acc1);                                                                                                  \
    SBAR();                                                                                                                \
  } while (0)
    WLD(0); WLD(1); WLD(2); WLD(3);
    OCHUNK(ob, 0, NOP1); OCHUNK(ob, 1, NOP1); OCHUNK(ob, 2, NOP1); OCHUNK(ob, 3, NOP1);
    constexpr int OSPL = 12;
    const u16* const ob1 = ob + 16384;
    bb[0] += qg ? -32768 : 65536; bb[1] += qg ? -32768 : 65536;
    if (qg == 0) {
      lds_arrive(lds0 + K3_CNT + 16);
      lds_await(lds0 + K3_CNT + 16, 4u);
#pragma unroll
      for (int p = 8; p < 16; ++p) WDMA(p, 1);
      __builtin_amdgcn_s_sleep(HD0);
    } else {
      __builtin_amdgcn_s_sleep(HD0B);
    }
#undef WDMA
    OLOAD(ob1, 0, OSPL);
    lds_await(lds0 + K3_CNT + 20, 4u * qg);
    WLD(0); WLD(1); WLD(2); WLD(3);
    SBAR();
    asm volatile("s_cmp_lg_u32 %0, 0\n\ts_cbranch_scc1 1f\n\ts_waitcnt vmcnt(%2)\n\ts_mov_b64 exec, 1\n\tds_inc_u32 %1, %1\n\t"
                 "s_mov_b64 exec, -1\n\t1:"
                 :
                 : "s"(qg), "v"(lds0 + K3_CNT + 20), "i"(OSPL)
                 : "memory", "scc");
    OLOAD(ob1, OSPL, 16);
    f32x4 ga[8], bs[8];
    f32x16 st;
    float sq0 = 0.f, sq1 = 0.f, gs;
    u32x4 xb[4];
    unsigned ca = lds0 + K3_CNT + 32 + kq * 4, wa = lds0 + 98304 + kq * 8192 + lane * 16, pk;
    const unsigned long long* rp = sync + SY_ST + (mt * 16 + nt) * STP + kq * 64;
    unsigned long long rm = qg ? 0xffffffffull : 0ull;
    unsigned xa = lds0 + 131072 + kq * 5120 + h * 640 + (lane & 31) * 16;
    const unsigned gaa = lds0 + K4_VEC + h * 128, pka = qg ? lds0 + K3_CNT + 32 + kq * 4 : lds0 + K3_CNT + 28;
#define UNP(I)                                                                                                   \
    do {                                                                                                         \
      bs[I] = (f32x4){bflo(xr[(I) >> 1][((I) & 1) * 2]), bfhi(xr[(I) >> 1][((I) & 1) * 2]),                      \
                      bflo(xr[(I) >> 1][((I) & 1) * 2 + 1]), bfhi(xr[(I) >> 1][((I) & 1) * 2 + 1])};             \
      PIN(bs[I]);                                                                                                \
    } while (0)
#define HK(F)                                                                                                    \
    do {                                                                                                         \
      if ((F) == 3)                                                                                              \
        asm volatile("s_mov_b64 exec, %5\n\tds_read_b128 %0, %4\n\tds_read_b128 %1, %4 offset:16\n\t"        \
                     "ds_read_b128 %2, %4 offset:32\n\tds_read_b128 %3, %4 offset:48\n\ts_mov_b64 exec, -1"    \
                     : "+v"(wdn[0]), "+v"(wdn[1]), "+v"(wdn[2]), "+v"(wdn[3])                                    \
                     : "v"(lds0 + K4_VEC + 1024 + (lane & 3) * 128 + h * 64), "s"((long long)-qg) : "memory");   \
      if ((F) >= 4 && (F) < 8) UNP(((F) - 4) & 7);                                                               \
      if ((F) == 8 || (F) == 9) { UNP((2 * (F) - 12) & 7); UNP((2 * (F) - 11) & 7); }                            \
      if ((F) == 10) {                                                                                           \
        pk = lds_peek(pka);                                                                                      \
        _Pragma("unroll") for (int i = 0; i < 4; ++i) ga[i] = ldsr<f32x4>(gaa + i * 16);                         \
        gs = ldsr<float>(lds0 + K4_VEC + 1408);                                                                  \
        asm volatile("" : "+s"(rp), "+s"(rm));                                                         \
        PIN(ca); PIN(wa); PIN(xa); PIN(sq0); PIN(sq1);                                                           \
      }                                                                                                          \
      if ((F) == 11) {                                                                                           \
        SBAR();                                                                                                  \
        asm volatile("v_cmp_gt_u32 vcc, %2, %0\n\ts_cbranch_vccz 2f\n\t1:\n\tds_read_b32 %0, %1\n\ts_waitcnt lgkmcnt(0)\n\t" \
                     "v_cmp_gt_u32 vcc, %2, %0\n\ts_cbranch_vccnz 1b\n\t2:"                                    \
                     : "+v"(pk) : "v"(pka), "s"(4u - qg) : "memory", "vcc");                                     \
        asm volatile("s_mov_b64 exec, %9\n\tds_read_b128 %0, %8\n\tds_read_b128 %1, %8 offset:1024\n\t"      \
                     "ds_read_b128 %2, %8 offset:2048\n\tds_read_b128 %3, %8 offset:3072\n\t"                  \
                     "ds_read_b128 %4, %8 offset:4096\n\tds_read_b128 %5, %8 offset:5120\n\t"                  \
                     "ds_read_b128 %6, %8 offset:6144\n\tds_read_b128 %7, %8 offset:7168\n\ts_mov_b64 exec, -1" \
                     : "+v"(bs[0]), "+v"(bs[1]), "+v"(bs[2]), "+v"(bs[3]), "+v"(bs[4]), "+v"(bs[5]), "+v"(bs[6]), "+v"(bs[7]) \
                     : "v"(wa), "s"((long long)-qg) : "memory");                                                 \
        _Pragma("unroll") for (int i = 4; i < 8; ++i) ga[i] = ldsr<f32x4>(gaa + i * 16);                         \
      }                                                                                                          \
    } while (0)
#define BW()                                                                                                     \
    asm volatile("s_waitcnt lgkmcnt(0)\n\ts_nop 4"     \
                 : "+v"(bs[0]), "+v"(bs[1]), "+v"(bs[2]), "+v"(bs[3]), "+v"(bs[4]), "+v"(bs[5]), "+v"(bs[6]), "+v"(bs[7]), \
                   "+v"(wdn[0]), "+v"(wdn[1]), "+v"(wdn[2]), "+v"(wdn[3])::"memory")
#define EH(A, J, X0, X1)                                                                                         \
    do {                                                                                                         \
      float b0, b1, b2, b3;                                                                                      \
      asm volatile("v_fma_f32 %0, %6, %10, %14\n\tv_fma_f32 %1, %7, %11, %15\n\tv_fma_f32 %2, %8, %12, %16\n\t" \
                   "v_fma_f32 %3, %9, %13, %17\n\tv_cvt_pk_bf16_f32 %4, %0, %1\n\tv_cvt_pk_bf16_f32 %5, %2, %3" \
                   : "=&v"(b0), "=&v"(b1), "=&v"(b2), "=&v"(b3), "=v"(X0), "=v"(X1)                              \
                   : "v"(ga[J][0]), "v"(ga[J][1]), "v"(ga[J][2]), "v"(ga[J][3]), "v"(A[(4 * (J)) & 15]),         \
                     "v"(A[(4 * (J) + 1) & 15]), "v"(A[(4 * (J) + 2) & 15]), "v"(A[(4 * (J) + 3) & 15]),         \
                     "v"(bs[J][0]), "v"(bs[J][1]), "v"(bs[J][2]), "v"(bs[J][3]));                                \
      bs[J] = (f32x4){b0, b1, b2, b3}; SBAR();                                                                   \
    } while (0)
#define EC(I, ST, C)                                                                                             \
    do {                                                                                                         \
      xb[I] = (u32x4){xq[4 * (I)], xq[4 * (I) + 1], xq[4 * (I) + 2], xq[4 * (I) + 3]};                           \
      asm volatile("v_dot2c_f32_bf16 %1, %3, %3\n\tv_dot2c_f32_bf16 %2, %4, %4\n\tv_dot2c_f32_bf16 %1, %5, %5\n\t" \
                   "v_dot2c_f32_bf16 %2, %6, %6\n\tv_mfma_f32_32x32x16_bf16 %0, %7, %8, " C                     \
                   : ST(st), "+v"(sq0), "+v"(sq1)                                                                \
                   : "v"(xq[4 * (I)]), "v"(xq[4 * (I) + 1]), "v"(xq[4 * (I) + 2]), "v"(xq[4 * (I) + 3]),         \
                     "v"(wdn[I]), "v"(xb[I]));                                                                   \
      SBAR();                                                                                                    \
    } while (0)
#define BST(J, END)                                                                                              \
    do {                                                                                                         \
      asm volatile("s_cmp_lg_u32 %5, 0\n\ts_cbranch_scc1 1f\n\tds_write_b128 %4, %0 offset:%7\n\t"             \
                   "ds_write_b128 %4, %1 offset:%7+1024\n\tds_write_b128 %4, %2 offset:%7+2048\n\t"            \
                   "ds_write_b128 %4, %3 offset:%7+3072\n\t" END "1:"                                           \
                   :                                                                                             \
                   : "v"(bs[J]), "v"(bs[(J) + 1]), "v"(bs[(J) + 2]), "v"(bs[(J) + 3]), "v"(wa), "s"(qg), "v"(ca), "i"((J) * 1024) \
                   : "memory", "scc");                                                                           \
      SBAR();                                                                                                    \
    } while (0)
#define E2(A, J) EH(A, J, xq[2 * (J)], xq[2 * (J) + 1])
    unsigned xq[16];
    OCHUNK(ob1, 0, HK); OCHUNK(ob1, 1, HK); OCHUNK(ob1, 2, HK);
    OCHK(ob1, 3);
    MM(acc0, 12); MM(acc0, 13); MM(acc0, 14); MM(acc0, 15); MM(acc1, 12);
    asm volatile("" : "+v"(ga[4]), "+v"(ga[5]), "+v"(ga[6]), "+v"(ga[7]), "+v"(w[3][1]));
    BW();
    E2(acc0, 0); MM(acc1, 13); E2(acc0, 1); MM(acc1, 14); E2(acc0, 2); MM(acc1, 15); E2(acc0, 3);
    BST(0, "");
    EC(0, "=&v", "0"); E2(acc1, 4); E2(acc1, 5); EC(1, "+v", "%0"); E2(acc1, 6); E2(acc1, 7);
    BST(4, "s_mov_b64 exec, 1\n\tds_inc_u32 %6, %6\n\ts_mov_b64 exec, -1\n\t");
    EC(2, "+v", "%0"); EC(3, "+v", "%0");
#undef WFRAG
    unsigned r0, r1, r2;
    asm volatile("s_nop 1\n\tv_add_f32 %3, %3, %4\n\tv_mov_b32 %4, %3\n\ts_nop 1\n\tv_permlane32_swap_b32 %3, %4\n\ts_nop 0\n\t"
                 "v_add_f32 %1, %3, %4\n\tv_or_b32 %1, 1, %1\n\ts_nop 2\n\tv_add_f32 %2, %6, %7\n\tv_or_b32 %0, 1, %5\n\tv_or_b32 %2, 1, %2"
                 : "=&v"(r0), "=&v"(r1), "=&v"(r2), "+v"(sq0), "+v"(sq1)
                 : "v"(st[0]), "v"(st[1]), "v"(st[2]));
    const u32x4 rec = {r0, r1, r2, __float_as_uint(gs)};
    asm volatile("s_mov_b64 exec, %3\n\tglobal_store_dwordx4 %0, %1, %2 sc1\n\ts_mov_b64 exec, %9\n\t"
                 "ds_write_b128 %4, %5\n\tds_write_b128 %4, %6 offset:1312\n\tds_write_b128 %4, %7 offset:2624\n\t"
                 "ds_write_b128 %4, %8 offset:3936\n\ts_mov_b64 exec, %11\n\tds_inc_u32 %10, %10\n\ts_mov_b64 exec, -1"
                 :
                 : "v"(lane * 16), "v"(rec), "s"(rp), "s"(rm), "v"(xa), "v"(xb[0]), "v"(xb[1]), "v"(xb[2]), "v"(xb[3]),
                   "s"((long long)-qg), "v"(ca), "s"((unsigned long long)qg)
                 : "memory");
    lds_await(ca, 4u);
#pragma unroll
    for (int i = 0; i < 2; ++i) {
      xw[i] = ldsr<u32x4>(lds0 + 131072 + kq * 5120 + (pos & 3) * 1312 + (pos >> 2) * 640 + (qg * 16 + 8 * i + kr) * 16);
#pragma unroll
      for (int j = 0; j < 4; ++j) { xv[i][j] = up2(xw[i][j]); PIN(xv[i][j]); }
    }
  }
#define POLL(ISSUE, TIE, BAD, DELAY, GAP)                                                     \
  do {                                                                                        \
    unsigned long long todo = ~0ull;                                                          \
    unsigned spin = SPIN_CAP;                                                                 \
    __builtin_amdgcn_s_sleep((DELAY) - (GAP));                                                \
    _Pragma("nounroll") do {                                                                  \
      __builtin_amdgcn_s_sleep(GAP);                                                          \
      ISSUE;                                                                                  \
      unsigned long long go;                                                                  \
      asm volatile("s_sub_u32 %1, %1, 1\n\ts_cselect_b64 %0, 0, -1\n\ts_waitcnt vmcnt(0)"      \
                   : "=s"(go), "+s"(spin)::"memory", "scc");                                  \
      TIE;                                                                                    \
      todo = __ballot(BAD) & go;                                                              \
    } while (__builtin_expect(todo != 0ull, 0));                                              \
  } while (0)
  u32x4 c[4] = {(u32x4){}, (u32x4){}, (u32x4){}, (u32x4){}};
  u32x4 o[2];
  unsigned long long om[2];
  unsigned vout = (unsigned)((erow * D + pos * 8) * 2);
#define OUTST()                                                                                                    \
  asm volatile("s_mov_b64 exec, %5\n\tglobal_store_dwordx4 %0, %1, %3 sc1\n\ts_mov_b64 exec, %6\n\t"               \
               "global_store_dwordx4 %0, %2, %4 sc1\n\ts_mov_b64 exec, -1\n\ts_nop 0"                              \
               :                                                                                                   \
               : "v"(vout), "v"(o[0]), "v"(o[1]), "s"(out + (size_t)(mt * 128) * D + nt * 64),                     \
                 "s"(out + (size_t)(mt * 128 + 8) * D + nt * 64), "s"(om[0]), "s"(om[1])                           \
               : "memory")
#define TIE_C() asm volatile("" : "+v"(c[0]), "+v"(c[1]), "+v"(c[2]), "+v"(c[3]))
  {
    const unsigned va = lds0 + K4_VEC + pos * 32;
    f32x4 t[6];
#pragma unroll
    for (int i = 0; i < 6; ++i) t[i] = ldsr<f32x4>(va + 256 + (i >> 1) * 256 + (i & 1) * 16);
    asm volatile("" : "+v"(t[0]), "+v"(t[1]), "+v"(t[2]), "+v"(t[3]), "+v"(t[4]), "+v"(t[5]));
    unsigned vo = (unsigned)(pos * (STP * 16) + erow * 16), vc = (unsigned)(mt * (HCP * 8) + nt * 128 + pos * 16);
    unsigned ba = lds0 + K3_CNT + 48;
    PIN(vo); PIN(vc); PIN(vout); PIN(ba);
#define ISSUE1                                                                                                     \
    asm volatile("s_mov_b64 exec, %6\n\tglobal_load_dwordx4 %0, %4, %5 sc1\n\tglobal_load_dwordx4 %1, %4, %5 offset:%7 sc1\n\t" \
                 "global_load_dwordx4 %2, %4, %5 offset:128 sc1\n\tglobal_load_dwordx4 %3, %4, %5 offset:%8 sc1\n\t" \
                 "s_mov_b64 exec, -1"                                                                              \
                 : "+v"(c[0]), "+v"(c[1]), "+v"(c[2]), "+v"(c[3])                                                  \
                 : "v"(vo), "s"(sync + SY_ST + mt * 16 * STP), "s"(todo), "i"(STP * 8), "i"(STP * 8 + 128)         \
                 : "memory")
    POLL(ISSUE1, TIE_C(), !((AND4(c[0]) & AND4(c[1])) & (AND4(c[2]) & AND4(c[3])) & 1u), HD1, PG);
#define F(x_) __uint_as_float(x_)
    float mean[2], rstd[2];
    unsigned key[2];
    float e[7];
#pragma unroll
    for (int i = 0; i < 7; ++i) e[i] = F(c[(i >> 2) * 2][i & 3]) + F(c[(i >> 2) * 2 + 1][i & 3]);
#define DS7(P)                                                                                                     \
    asm volatile("s_nop 0\n\tv_add_f32_dpp %0, %0, %0 " P "\n\tv_add_f32_dpp %1, %1, %1 " P "\n\tv_add_f32_dpp %2, %2, %2 " P "\n\t" \
                 "v_add_f32_dpp %3, %3, %3 " P "\n\tv_add_f32_dpp %4, %4, %4 " P "\n\tv_add_f32_dpp %5, %5, %5 " P "\n\t" \
                 "v_add_f32_dpp %6, %6, %6 " P                                                                     \
                 : "+v"(e[0]), "+v"(e[4]), "+v"(e[1]), "+v"(e[5]), "+v"(e[3]), "+v"(e[2]), "+v"(e[6]))
    asm volatile("s_nop 0" : "+v"(e[0]), "+v"(e[1]), "+v"(e[2]), "+v"(e[3]), "+v"(e[4]), "+v"(e[5]), "+v"(e[6]));
    DS7("quad_perm:[1,0,3,2] row_mask:0xf bank_mask:0xf");
    DS7("quad_perm:[2,3,0,1] row_mask:0xf bank_mask:0xf");
    DS7("row_half_mirror row_mask:0xf bank_mask:0xf");
#pragma unroll
    for (int i = 0; i < 2; ++i) {
      mean[i] = e[4 * i] * (1.f / D);
      rstd[i] = __builtin_amdgcn_rsqf(fmaxf(fmaf(-mean[i], mean[i], e[4 * i + 1] * (1.f / D)), 0.f) + 1e-5f);
      key[i] = okey(rstd[i] * fmaf(-mean[i], e[3], e[4 * i + 2]));
    }
    const bool hi = key[1] > key[0];
    const u32x2 mine = {~(unsigned)(erow + (hi ? 8 : 0)), hi ? key[1] : key[0]};
    asm volatile("s_mov_b64 exec, %3\n\tds_max_u64 %0, %1\n\ts_mov_b64 exec, 1\n\tds_add_u32 %0, %2 offset:8\n\ts_mov_b64 exec, -1"
                 :
                 : "v"(ba), "v"(mine), "v"(1u), "s"(0x0101010101010101ull)
                 : "memory");
    u32x4 hw[2], hc, xc;
#pragma unroll
    for (int i = 0; i < 2; ++i)
#pragma unroll
      for (int j = 0; j < 4; ++j) {
        const f32x2 g = (j & 1) ? t[j >> 1].hi : t[j >> 1].lo, sh = (j & 1) ? t[2 + (j >> 1)].hi : t[2 + (j >> 1)].lo;
        const f32x2 v = ((xv[i][j] - mean[i]) * rstd[i]) * g + sh;
        hw[i][j] = pack2(v[0], v[1]);
      }
#pragma unroll
    for (int j = 0; j < 4; ++j) {
      hc[j] = min(hi ? hw[1][j] : hw[0][j], 0xfffeffffu);
      xc[j] = min(hi ? xw[1][j] : xw[0][j], 0xfffeffffu);
    }
    PIN(hc); PIN(xc);
    u32x4 best = ldsr<u32x4>(ba);
    while (__builtin_expect(__builtin_amdgcn_readfirstlane(best[2]) < 8u, 0)) {
      __builtin_amdgcn_s_sleep(1);
      asm volatile("" ::: "memory");
      best = ldsr<u32x4>(ba);
    }
    if (best[0] == mine[0]) {
      asm volatile("s_nop 0\n\tglobal_store_dwordx4 %0, %1, %3 sc1\n\tglobal_store_dwordx4 %0, %2, %4 sc1\n\ts_nop 1"
                   :
                   : "v"(vc), "v"(hc), "v"(xc), "s"(sync + SY_HC), "s"(sync + SY_XC)
                   : "memory");
      if (pos == 0)
        __hip_atomic_store((gu64*)sync + SY_KEY + nt * 16 + mt, mk64(mine[1] | 1u, (unsigned)(mt * 128) + ~mine[0]), RLX_AGENT);
    }
#pragma unroll
    for (int i = 0; i < 2; ++i) {
#pragma unroll
      for (int j = 0; j < 4; ++j) {
        const f32x2 v = ((j & 1) ? t[4 + (j >> 1)].hi : t[4 + (j >> 1)].lo) * up2(hw[i][j]) + xv[i][j];
        o[i][j] = pack2(v[0], v[1]);
      }
      om[i] = __ballot(best[0] != ~(unsigned)(erow + 8 * i));
    }
    OUTST();
  }
  if (qg == 0) {
    const int n = lane & 15, kg = lane >> 4;
    u32x4 hb[2] = {(u32x4){POISON, POISON, POISON, POISON}, (u32x4){POISON, POISON, POISON, POISON}};
    unsigned key = 0u;
#define TIE_H() asm volatile("" : "+v"(hb[0]), "+v"(hb[1]), "+v"(key))
    {
      unsigned vh = (unsigned)(kr * (HCP * 8) + pos * 16), vk = (unsigned)(n * 8), vg = kg * 16;
      unsigned wa = lds0 + K4_RED + 512 + kq * 128 + pos * 16, ra = lds0 + K4_RED + 512 + kg * 16;
      PIN(vh); PIN(vk); PIN(vg); PIN(wa); PIN(ra);
      const unsigned long long* hp = sync + SY_HC + (ks * 4 + kq) * 16;
#define ISSUE2                                                                                                     \
      asm volatile("global_load_dword %2, %4, %7 sc1\n\ts_mov_b64 exec, %8\n\t"                                    \
                   "global_load_dwordx4 %0, %3, %5 sc1\n\tglobal_load_dwordx4 %1, %3, %6 sc1\n\ts_mov_b64 exec, -1" \
                   : "+v"(hb[0]), "+v"(hb[1]), "+v"(key)                                                           \
                   : "v"(vh), "v"(vk), "s"(hp), "s"(hp + 8 * HCP), "s"(sync + SY_KEY + (ks * 4 + kq) * 16), "s"(todo) \
                   : "memory")
      POLL(ISSUE2, TIE_H(), max(MAX4(hb[0]), MAX4(hb[1])) == POISON || !(key & 1u), HD2, PG);
      TIE_C();
      const int win = __builtin_ctzll(__ballot(key == rowmaxu(key)));
      asm volatile("s_mov_b64 exec, %3\n\tds_write_b128 %0, %1\n\ts_mov_b64 exec, 1\n\tds_add_u32 %2, %4\n\ts_mov_b64 exec, -1"
                   :
                   : "v"(wa), "v"(win < 8 ? hb[0] : hb[1]), "v"(lds0 + K3_CNT + 24), "s"(0xffull << ((win & 7) * 8)), "v"(1u)
                   : "memory");
      while (lds_peek(lds0 + K3_CNT + 24) < 4u) {}
      asm volatile("" ::: "memory");
      f32x4 ag = (f32x4){}, au = (f32x4){};
#pragma unroll
      for (int i = 0; i < 8; ++i) {
        const bf16x8 h = lds_b128(ra + i * 64);
        ag = MFMA16(wgu[i], h, ag);
        au = MFMA16(wgu[8 + i], h, au);
      }
      const u32x4 g = __builtin_bit_cast(u32x4, ag) | 1u, u = __builtin_bit_cast(u32x4, au) | 1u;
      asm volatile("s_mov_b64 exec, %4\n\tglobal_store_dwordx4 %0, %1, %3 sc1\n\tglobal_store_dwordx4 %0, %2, %3 offset:64 sc1\n\t"
                   "s_mov_b64 exec, -1\n\ts_nop 0"
                   :
                   : "v"(vg), "v"(g), "v"(u), "s"(sync + SY_GP + ks * GPP + cg * 64 + kq * 16),
                     "s"(has_gu ? 0x0001000100010001ull : 0ull)
                   : "memory");
    }
    {
      const int q = kq * 14 + min(lane >> 2, 13);
      unsigned vo = (unsigned)((lane & 3) * (GPP * 8) + (q >> 2) * 128 + (q & 3) * 16);
      unsigned wa = lds0 + K4_RED + (q * 4 + (lane & 3)) * 2, ra = lds0 + K4_RED + kg * 16, vy = kg * 16;
      PIN(vo); PIN(wa); PIN(ra); PIN(vy);
      c[0] = (u32x4){}; c[1] = (u32x4){};
#define ISSUE3                                                                                                     \
      asm volatile("s_mov_b64 exec, %4\n\tglobal_load_dwordx4 %0, %2, %3 sc1\n\tglobal_load_dwordx4 %1, %2, %3 offset:64 sc1\n\t" \
                   "s_mov_b64 exec, -1"                                                                            \
                   : "+v"(c[0]), "+v"(c[1])                                                                        \
                   : "v"(vo), "s"(sync + SY_GP + dks * 224), "s"(todo)                                             \
                   : "memory")
      POLL(ISSUE3, asm volatile("" : "+v"(c[0]), "+v"(c[1])), !(AND4(c[0]) & AND4(c[1]) & 1u), HD3, PG);
      TIE_H();
      float sg[4], su[4];
#pragma unroll
      for (int j = 0; j < 4; ++j) {
        sg[j] = dpp_add<0x4E>(dpp_add<0xB1>(F(c[0][j])));
        su[j] = dpp_add<0x4E>(dpp_add<0xB1>(F(c[1][j])));
      }
      const float g = (lane & 2) ? ((lane & 1) ? sg[3] : sg[2]) : ((lane & 1) ? sg[1] : sg[0]);
      const float u = (lane & 2) ? ((lane & 1) ? su[3] : su[2]) : ((lane & 1) ? su[1] : su[0]);
      ldsw(wa, (u16)pack2(silu_bf(rbf(g)) * rbf(u), 0.f));
      asm volatile("s_mov_b64 exec, 1\n\tds_add_u32 %0, %1\n\ts_mov_b64 exec, -1" ::"v"(lds0 + K3_CNT + 24), "v"(1u) : "memory");
      while (lds_peek(lds0 + K3_CNT + 24) < 8u) {}
      asm volatile("" ::: "memory");
      f32x4 y = (f32x4){};
#pragma unroll
      for (int i = 0; i < 7; ++i) y = MFMA16(wdn[i], lds_b128(ra + i * 64), y);
      asm volatile("s_mov_b64 exec, %3\n\tglobal_store_dwordx4 %0, %1, %2 sc1\n\ts_mov_b64 exec, -1\n\ts_nop 0"
                   :
                   : "v"(vy), "v"(__builtin_bit_cast(u32x4, y) | 1u), "s"(sync + SY_GY + dks * GYP + dcg * 32 + kq * 8),
                     "s"(0x0001000100010001ull)
                   : "memory");
      asm volatile("" : "+v"(c[0]), "+v"(c[1]));
    }
  } else if (!has_gu && wid == 7) {
    const int fc0 = (b - 224) * 32;
    const unsigned vh = (unsigned)((lane >> 2) * (HCP * 8) + fc0 * 2 + (lane & 3) * 16), vk = (unsigned)((SY_KEY + (fc0 >> 6) * 16 + (lane >> 2)) * 8);
    const int col = pos * 4 + (lane >> 5) + ((lane >> 4) & 1) * 2;
    u32x4 hc, xc, gw0, gw1;
    u32x2 kr2;
    unsigned gw2, csw;
    _Pragma("nounroll") for (unsigned spin = 0; spin < SPIN_CAP; ++spin) {
      GLOADC(gw0, (lane & 3) * 32, sync + SY_GB + fc0 / 2, 0); GLOADC(gw1, (lane & 3) * 32, sync + SY_GB + fc0 / 2, 16);
      asm volatile("global_load_dword %0, %4, %6 sc1\n\tglobal_load_dword %1, %5, %7 sc1\n\ts_waitcnt vmcnt(0)"
                   : "=&v"(gw2), "=&v"(csw), "+v"(gw0), "+v"(gw1)
                   : "v"(col * 4), "v"((lane & 15) * 4), "s"(sync + SY_GB + fc0 / 2), "s"(sync + SY_GB + 512)
                   : "memory");
      if (__all(AND4(gw0) & AND4(gw1) & gw2 & csw & 1u)) break;
      __builtin_amdgcn_s_sleep(8);
    }
    const f32x4 g0 = __builtin_bit_cast(f32x4, gw0 & 0xffff0000u), g1 = __builtin_bit_cast(f32x4, gw1 & 0xffff0000u);
    const float cs = dpp_add<0x140>(sum8(F(csw)));
    __builtin_amdgcn_s_sleep(HD2 / 2);
    _Pragma("nounroll") for (unsigned spin = 0; spin < SPIN_CAP; ++spin) {
      GLOADC(hc, vh, sync + SY_HC, 0); GLOADC(xc, vh, sync + SY_XC, 0);
      asm volatile("global_load_dwordx2 %0, %1, %2 sc1" : "=v"(kr2) : "v"(vk), "s"(sync) : "memory");
      asm volatile("s_waitcnt vmcnt(0)" : "+v"(hc), "+v"(xc), "+v"(kr2)::"memory");
      if (__all(max(MAX4(hc), MAX4(xc)) != POISON && (kr2[0] & 1u))) break;
      __builtin_amdgcn_s_sleep(8);
    }
    TIE_C();
    unsigned km = rowmaxu(kr2[0]);
    km = max(max(__builtin_amdgcn_readlane(km, 0), __builtin_amdgcn_readlane(km, 16)),
             max(__builtin_amdgcn_readlane(km, 32), __builtin_amdgcn_readlane(km, 48)));
    const int wl = __builtin_ctzll(__ballot(kr2[0] == km));
    const unsigned wrow = __builtin_amdgcn_readlane(kr2[1], wl);
    if ((lane >> 2) == (wl >> 2)) {
      ldsw(lds0 + K4_FIN + 256 + (lane & 3) * 16, xc);
      ldsw(lds0 + K4_FIN + 320 + (lane & 3) * 16, hc);
    } else {
      u32x4 o;
#pragma unroll
      for (int j = 0; j < 4; ++j) {
        const f32x2 v = (j == 0 ? g0.lo : j == 1 ? g0.hi : j == 2 ? g1.lo : g1.hi) * up2(hc[j]) + up2(xc[j]);
        o[j] = pack2(v[0], v[1]);
      }
      st16(out + (size_t)kr2[1] * D + fc0 + (lane & 3) * 8, o);
    }
    float fx = bf2f(ldsr<u16>(lds0 + K4_FIN + 256 + col * 2)), fh = bf2f(ldsr<u16>(lds0 + K4_FIN + 320 + col * 2));
    float fg = bfhi(gw2);
    const float rr = __uint_as_float(km ^ ((km >> 31) ? 0x80000000u : 0xffffffffu)) + cs;
    float fsig = rbf(1.f / (1.f + __expf(-rbf(rr))));
    unsigned vo = (unsigned)(kr * (GYP * 8) + fc0 * 4 + pos * 16), vs = (unsigned)(col * 2);
    PIN(fsig); PIN(fx); PIN(fh); PIN(fg); PIN(vo); PIN(vs);
    c[0] = (u32x4){}; c[1] = (u32x4){};
#define ISSUE4                                                                                                     \
    asm volatile("s_mov_b64 exec, %5\n\tglobal_load_dwordx4 %0, %2, %3 sc1\n\tglobal_load_dwordx4 %1, %2, %4 sc1\n\t" \
                 "s_mov_b64 exec, -1"                                                                              \
                 : "+v"(c[0]), "+v"(c[1])                                                                          \
                 : "v"(vo), "s"(sync + SY_GY), "s"(sync + SY_GY + 8 * GYP), "s"(todo)                              \
                 : "memory")
    POLL(ISSUE4, asm volatile("" : "+v"(c[0]), "+v"(c[1])), !(AND4(c[0]) & AND4(c[1]) & 1u), HD4, PG);
    const f32x4 v = __builtin_bit_cast(f32x4, c[0]) + __builtin_bit_cast(f32x4, c[1]);
    const float y = dpp_add<0x128>(swap16_add(swap32_add(v[0], v[1]), swap32_add(v[2], v[3])));
    const unsigned ov = pack2(fmaf(fg, rbf(fh + rbf(rbf(y) * fsig)), fx), 0.f);
    asm volatile("s_mov_b64 exec, %3\n\tglobal_store_short %0, %1, %2\n\ts_mov_b64 exec, -1\n\ts_nop 0"
                 :
                 : "v"(vs), "v"(ov), "s"(out + (size_t)wrow * D + fc0), "s"(0x00ff00ff00ff00ffull)
                 : "memory");
    asm volatile("" : "+v"(c[0]), "+v"(c[1]));
  } else {
    TIE_C();
  }
#undef F
}

extern "C" void kernel_launch(void* const* d_in, const int* in_sizes, int n_in, void* d_out, int out_size, void* d_ws,
                              size_t ws_size, hipStream_t stream) {
  const u16* x = (const u16*)d_in[0];
  const u16* wqkv = (const u16*)d_in[1];
  const u16* wo = (const u16*)d_in[2];
  const u16* wr = (const u16*)d_in[4];
  const u16* wg = (const u16*)d_in[5];
  const u16* wu = (const u16*)d_in[6];
  const u16* wd = (const u16*)d_in[7];
  const u16* c = (const u16*)d_in[8];
  const u16* wmod = (const u16*)d_in[9];
  char* ws = (char*)d_ws;
  float* modp = (float*)(ws + WS_MODP);
  u16* nbuf = (u16*)(ws + WS_N);
  u16* kvf = (u16*)(ws + WS_K);
  u16* of = (u16*)(ws + WS_O);
  u16* out = (u16*)d_out;
  (void)hipFuncSetAttribute((const void*)k2_main, hipFuncAttributeMaxDynamicSharedMemorySize, K3_LDS);
  k1_mod_ln<<<256, 512, 0, stream>>>(c, wmod, x, modp, nbuf, (u32x4*)(ws + WS_SYNC), wqkv);
  k2_main<<<256, 512, K3_LDS, stream>>>(nbuf, wqkv, modp, kvf, of, wo, x, wr, wg, wu, wd, out, (unsigned long long*)(ws + WS_SYNC), wmod);
}
```
